```python
import torch

def _softmax(x, axis=-1):
    return torch.softmax(x, dim=axis)
def _gelu(x):
    return torch.nn.functional.gelu(x, approximate="tanh")
def _silu(x):
    return torch.nn.functional.silu(x)
def _sigmoid(x):
    return torch.sigmoid(x)
def _layernorm(x, axis=-1, eps=1e-5):
    m = x.mean(dim=axis, keepdim=True)
    v = x.var(dim=axis, keepdim=True, unbiased=False)
    return (x - m) / torch.sqrt(v + eps)
def _scatter_add(tgt, idx, src, axis=0):
    return tgt.clone().index_add_(axis, idx, src)
def _segment_sum(src, seg, n):
    out = torch.zeros((n,) + src.shape[1:], dtype=src.dtype)
    return out.index_add_(0, seg, src)
def _pad(x, pw):
    flat = tuple(v for pair in reversed(pw) for v in pair)
    return torch.nn.functional.pad(x, flat)

kernel_name = 'gated_mix__linear_kernel_hedgehog__dense_activation_silu__prompt'

def setup_inputs():

    _w = (lambda a: (lambda t: t.to(torch.bfloat16) if t.is_floating_point() else t)(torch.as_tensor(a)))
    def _A_setup():
        B, S, D, H = 1, 2048, 640, 32
        Dh = D // H
        return {
            "x": _w((lambda *s: torch.randn(*s, dtype=torch.bfloat16) * 0.1)(B, S, D)),
            "wqkv": _w((lambda *s: torch.randn(*s, dtype=torch.bfloat16) * 0.8)(D, 3 * D)),
            "w_phi": _w((lambda *s: torch.randn(*s, dtype=torch.bfloat16) * 0.8)(H, Dh, Dh) * (Dh ** -0.5)),
            "wo": _w((lambda *s: torch.randn(*s, dtype=torch.bfloat16) * 0.2)(D, D)),
        }
    _A_in = _A_setup()

    def _B_setup():
        B, S, D, F = 1, 2048, 640, 1728
        return {
            "x": _w((lambda *s: torch.randn(*s, dtype=torch.bfloat16) * 0.1)(B, S, D)),
            "w1": _w((lambda *s: torch.randn(*s, dtype=torch.bfloat16) * 3.2)(D, F) * (D ** -0.5)),
            "w2": _w((lambda *s: torch.randn(*s, dtype=torch.bfloat16) * 3.2)(F, D) * (F ** -0.5)),
        }
    _B_in = _B_setup()

    return {
        "A_x": _A_in["x"],
        "A_wqkv": _A_in["wqkv"],
        "A_w_phi": _A_in["w_phi"],
        "A_wo": _A_in["wo"],
        "B_x": _B_in["x"],
        "B_w1": _B_in["w1"],
        "B_w2": _B_in["w2"],
        "gate_w": _w((lambda *s: torch.randn(*s, dtype=torch.bfloat16) * 0.1)(640) * 16.0),
    }

def reference(A_x, A_wqkv, A_w_phi, A_wo, B_x, B_w1, B_w2, gate_w):
    def _A_ref(x, wqkv, w_phi, wo):
        B, S, D = x.shape
        H = 32; Dh = D // H
        qkv = (lambda x, *d: x.permute(*d))((x @ wqkv).reshape(B, S, 3, H, Dh), 2, 0, 3, 1, 4)
        q, k, v = qkv[0], qkv[1], qkv[2]
        pq = torch.einsum("bhsd,hdf->bhsf", q, w_phi)
        pk = torch.einsum("bhsd,hdf->bhsf", k, w_phi)
        phi_q = torch.concatenate([_softmax(pq, axis=-1), _softmax(-pq, axis=-1)], axis=-1)
        phi_k = torch.concatenate([_softmax(pk, axis=-1), _softmax(-pk, axis=-1)], axis=-1)
        kv = torch.einsum("bhsf,bhsd->bhfd", phi_k, v)
        z  = torch.einsum("bhsf->bhf", phi_k)
        num = torch.einsum("bhqf,bhfd->bhqd", phi_q, kv)
        den = torch.einsum("bhqf,bhf->bhq", phi_q, z)[..., None] + 1e-6
        o = num / den
        return (lambda x, *d: x.permute(*d))(o, 0, 2, 1, 3).reshape(B, S, D) @ wo

    def _B_ref(x, w1, w2):
        h = torch.einsum("bsd,df->bsf", x, w1)
        h = _silu(h)
        out = torch.einsum("bsf,fd->bsd", h, w2)
        return out

    x = A_x
    g = _sigmoid((x * gate_w).sum(-1, keepdims=True))
    a = _A_ref(x, A_wqkv, A_w_phi, A_wo)
    b = _B_ref(x, B_w1, B_w2)
    return g * a + (1 - g) * b
```

```cpp
#include <hip/hip_runtime.h>
#include <hip/hip_bf16.h>
typedef __hip_bfloat16 bf16;
#define S 2048
#define D 640
#define H 32
#define DH 20
#define F 1728
__device__ __forceinline__ float b2f(bf16 v){ return __bfloat162float(v);}
__device__ __forceinline__ float rb(float v){ return __bfloat162float(__float2bfloat16(v)); }

__global__ void gemm_naive(const bf16* A, const bf16* B, float* C, int M, int N, int K, int act){
  int n = blockIdx.x*blockDim.x+threadIdx.x; int m = blockIdx.y;
  if(n>=N) return;
  float acc=0; for(int k=0;k<K;k++) acc += b2f(A[m*K+k])*b2f(B[k*N+n]);
  acc = rb(acc);
  if(act){ acc = rb(acc/(1.f+__expf(-acc))); }
  C[m*N+n]=acc;
}
__global__ void gemm_naive_f(const float* A, const bf16* B, float* C, int M, int N, int K){
  int n = blockIdx.x*blockDim.x+threadIdx.x; int m = blockIdx.y;
  if(n>=N) return;
  float acc=0; for(int k=0;k<K;k++) acc += A[m*K+k]*b2f(B[k*N+n]);
  C[m*N+n]=rb(acc);
}
__global__ void phi_k(const float* qkv, const bf16* wphi, float* phiq, float* phik){
  int idx = blockIdx.x*blockDim.x+threadIdx.x;
  if(idx>=S*H*2) return;
  int which = idx&1; int h=(idx>>1)%H; int s=(idx>>1)/H;
  const float* q = qkv + s*3*D + which*D + h*DH;
  float* o = (which?phik:phiq) + (s*H+h)*40;
  float mx=-1e30f, mn=1e30f;
  for(int f=0;f<DH;f++){ float a=0; for(int d=0;d<DH;d++) a+=q[d]*b2f(wphi[(h*DH+d)*DH+f]); a=rb(a); o[f]=a; mx=fmaxf(mx,a); mn=fminf(mn,a);}
  float sp=0,sn=0; for(int f=0;f<DH;f++){ sp+=__expf(o[f]-mx); sn+=__expf(mn-o[f]); }
  for(int f=0;f<DH;f++){ float a=o[f]; o[20+f]=rb(__expf(mn-a)/sn); o[f]=rb(__expf(a-mx)/sp);}
}
__global__ void kv_k(const float* qkv, const float* phik, float* kv){
  int idx = blockIdx.x*blockDim.x+threadIdx.x; if(idx>=H*40*21) return;
  int d=idx%21, f=(idx/21)%40, h=idx/(21*40);
  float a=0; for(int s=0;s<S;s++){ float v = d<20? qkv[s*3*D+2*D+h*DH+d]:1.f; a+=phik[(s*H+h)*40+f]*v; }
  kv[idx]=rb(a);
}
__global__ void o_k(const float* phiq, const float* kv, float* o){
  int idx = blockIdx.x*blockDim.x+threadIdx.x; if(idx>=S*D) return;
  int s=idx/D, c=idx%D, h=c/DH, d=c%DH;
  float num=0,den=0; for(int f=0;f<40;f++){ float p=phiq[(s*H+h)*40+f]; num+=p*kv[(h*40+f)*21+d]; den+=p*kv[(h*40+f)*21+20]; }
  num=rb(num); den=rb(rb(den)+1e-6f);
  o[idx]=rb(num/den);
}
__global__ void fin_k(const bf16* x, const bf16* gw, const float* a, const float* b, bf16* out){
  int s=blockIdx.x; __shared__ float g;
  if(threadIdx.x==0){ float t=0; for(int k=0;k<D;k++) t+=rb(b2f(x[s*D+k])*b2f(gw[k])); t=rb(t); g=rb(1.f/(1.f+__expf(-t))); }
  __syncthreads();
  for(int c=threadIdx.x;c<D;c+=blockDim.x){ float r=rb(g*a[s*D+c])+rb(rb(1.f-g)*b[s*D+c]); out[s*D+c]=__float2bfloat16(r);}
}
extern "C" void kernel_launch(void* const* d_in, const int* in_sizes, int n_in, void* d_out, int out_size, void* d_ws, size_t ws_size, hipStream_t stream){
  const bf16* x=(const bf16*)d_in[0]; const bf16* wqkv=(const bf16*)d_in[1]; const bf16* wphi=(const bf16*)d_in[2]; const bf16* wo=(const bf16*)d_in[3];
  const bf16* w1=(const bf16*)d_in[5]; const bf16* w2=(const bf16*)d_in[6]; const bf16* gw=(const bf16*)d_in[7];
  float* ws=(float*)d_ws; float* qkv=ws; float* h=qkv+S*3*D; float* phiq=h+S*F; float* phik=phiq+S*H*40; float* kv=phik+S*H*40; float* o=kv+H*40*21+64; float* a=o+S*D; float* b=a+S*D;
  gemm_naive<<<dim3((3*D+255)/256,S),256,0,stream>>>(x,wqkv,qkv,S,3*D,D,0);
  gemm_naive<<<dim3((F+255)/256,S),256,0,stream>>>(x,w1,h,S,F,D,1);
  phi_k<<<(S*H*2+255)/256,256,0,stream>>>(qkv,wphi,phiq,phik);
  kv_k<<<(H*40*21+63)/64,64,0,stream>>>(qkv,phik,kv);
  o_k<<<(S*D+255)/256,256,0,stream>>>(phiq,kv,o);
  gemm_naive_f<<<dim3((D+255)/256,S),256,0,stream>>>(o,wo,a,S,D,D);
  gemm_naive_f<<<dim3((D+255)/256,S),256,0,stream>>>(h,w2,b,S,D,F);
  fin_k<<<S,256,0,stream>>>(x,gw,a,b,(bf16*)d_out);
}
```

```cpp
#include <hip/hip_runtime.h>
#include <stdint.h>

typedef __attribute__((ext_vector_type(8))) __bf16 bf16x8;
typedef __attribute__((ext_vector_type(2))) __bf16 bf16x2;
typedef __attribute__((ext_vector_type(4))) short s16x4;
typedef __attribute__((ext_vector_type(4))) float f32x4;
typedef __attribute__((ext_vector_type(2))) float f32x2;
typedef __attribute__((ext_vector_type(4))) unsigned u32x4;
typedef __attribute__((ext_vector_type(2))) unsigned u32x2;
typedef unsigned long long u64;
#define LAS __attribute__((address_space(3)))
#define DI __device__ __forceinline__

constexpr unsigned WS_EPOCH = 0;
constexpr unsigned WS_RT = 1024;
constexpr unsigned WS_HF = 4096;
constexpr unsigned WS_KF = 8192;
constexpr unsigned WS_OF = 12288;
constexpr unsigned WS_G = 16384;
constexpr unsigned WS_PART = 32768;
constexpr unsigned PART_STRIDE = 6784;
constexpr unsigned WS_B = 1u << 20;
constexpr unsigned WS_A = 8u << 20;
constexpr unsigned WS_PROF = 24u << 20;

constexpr unsigned SLOT = 47104;
constexpr unsigned L_S1 = 0, L_S2 = SLOT, L_S0 = 2 * SLOT, L_MISC = 3 * SLOT;
constexpr unsigned L_PHIK = L_S2 + 40960;
constexpr unsigned L_V = L_PHIK + 128 * 352;
constexpr unsigned L_KV = L_MISC;
constexpr unsigned L_WPHI = 153600;
constexpr unsigned L_GATE = L_WPHI + 4096;
constexpr unsigned L_ZERO = L_GATE + 2048;
constexpr unsigned L_ONE = L_ZERO + 2048;
constexpr unsigned LDS_BYTES = 163840;
static_assert(L_ONE + 16 <= LDS_BYTES, "lds");

constexpr unsigned SPIN_CAP = 1u << 20;

DI unsigned pk2(float a, float b) {
  bf16x2 v = __builtin_convertvector((f32x2){a, b}, bf16x2);
  return __builtin_bit_cast(unsigned, v);
}
DI f32x4 mfma(u32x4 a, u32x4 b, f32x4 c) {
  return __builtin_amdgcn_mfma_f32_16x16x32_bf16(__builtin_bit_cast(bf16x8, a), __builtin_bit_cast(bf16x8, b), c, 0, 0, 0);
}
DI u32x4 lds128(unsigned a) { return *(LAS u32x4*)a; }
DI u32x2 ldstr(unsigned a) {
  return __builtin_bit_cast(u32x2, __builtin_amdgcn_ds_read_tr16_b64_v4i16((LAS s16x4*)a));
}
DI u32x4 cat(u32x2 a, u32x2 b) { return (u32x4){a[0], a[1], b[0], b[1]}; }
DI void glds(unsigned voff, const void* sbase, unsigned lds) {
  asm volatile("s_mov_b32 m0, %2\n\ts_nop 0\n\tglobal_load_lds_dwordx4 %0, %1" ::"v"(voff), "s"(sbase), "s"(lds) : "memory");
}
DI void glds5_sc1(unsigned voff, const void* sbase, unsigned lds) {
  asm volatile(
      "s_mov_b32 m0, %2\n\ts_nop 0\n\t"
      "global_load_lds_dwordx4 %0, %1 sc1\n\t"
      "global_load_lds_dwordx4 %0, %1 offset:1024 sc1\n\t"
      "global_load_lds_dwordx4 %0, %1 offset:2048 sc1\n\t"
      "global_load_lds_dwordx4 %0, %1 offset:3072 sc1\n\t"
      "s_add_u32 m0, m0, 1024\n\ts_nop 0\n\t"
      "global_load_lds_dwordx4 %3, %1 offset:3072 sc1" ::"v"(voff), "s"(sbase), "s"(lds), "v"(voff + 1024)
      : "memory", "scc");
}
DI u32x4 gld128_sc1(unsigned voff, const void* sbase) {
  u32x4 r;
  asm volatile("global_load_dwordx4 %0, %1, %2 sc1" : "=v"(r) : "v"(voff), "s"(sbase) : "memory");
  return r;
}
DI u32x2 gld64_sc1(unsigned voff, const void* sbase) {
  u32x2 r;
  asm volatile("global_load_dwordx2 %0, %1, %2 sc1" : "=v"(r) : "v"(voff), "s"(sbase) : "memory");
  return r;
}
DI unsigned gld32_sc1(unsigned voff, const void* sbase) {
  unsigned r;
  asm volatile("global_load_dword %0, %1, %2 sc1" : "=v"(r) : "v"(voff), "s"(sbase) : "memory");
  return r;
}
DI void gst128_sc1(unsigned voff, void* sbase, u32x4 v) {
  asm volatile("global_store_dwordx4 %0, %1, %2 sc1\n\ts_nop 1" ::"v"(voff), "v"(v), "s"(sbase) : "memory");
}
DI void gst64_sc1(unsigned voff, void* sbase, u32x2 v) {
  asm volatile("global_store_dwordx2 %0, %1, %2 sc1" ::"v"(voff), "v"(v), "s"(sbase) : "memory");
}
DI void gst32_sc1(unsigned voff, void* sbase, unsigned v) {
  asm volatile("global_store_dword %0, %1, %2 sc1" ::"v"(voff), "v"(v), "s"(sbase) : "memory");
}
DI void gst64(unsigned voff, void* sbase, u32x2 v) {
  asm volatile("global_store_dwordx2 %0, %1, %2" ::"v"(voff), "v"(v), "s"(sbase) : "memory");
}
#define WAITV(n) asm volatile("s_waitcnt vmcnt(" #n ")" ::: "memory")
#define WAITL0() asm volatile("s_waitcnt lgkmcnt(0)" ::: "memory")
#define BAR() asm volatile("s_waitcnt lgkmcnt(0)\n\ts_barrier" ::: "memory")
#define PIN(x) asm volatile("" : "+v"(x))

DI void poll_words(const char* ws, unsigned off, int nw, int lane, unsigned tlo, unsigned thi) {
  unsigned voff = off + (unsigned)(lane < nw ? lane : 0) * 8u;
  for (unsigned spins = 0; spins < SPIN_CAP; ++spins) {
    u32x2 v = gld64_sc1(voff, ws);
    asm volatile("s_waitcnt vmcnt(0)" : "+v"(v)::"memory");
    if (__all(v[0] == tlo && v[1] == thi)) break;
    __builtin_amdgcn_s_sleep(1);
  }
}

DI void dual_softmax(const f32x4 (&p)[5], unsigned (&o)[20]) {
  float mx = p[0][0], mn = p[0][0];
#pragma unroll
  for (int i = 1; i < 20; ++i) { mx = fmaxf(mx, p[i >> 2][i & 3]); mn = fminf(mn, p[i >> 2][i & 3]); }
  const float L = 1.4426950408889634f;
  const float mxl = -mx * L, mnl = mn * L;
  float ep[20], en[20], sp = 0.f, sn = 0.f;
#pragma unroll
  for (int i = 0; i < 20; ++i) {
    float v = p[i >> 2][i & 3];
    ep[i] = __builtin_amdgcn_exp2f(__builtin_fmaf(v, L, mxl));
    en[i] = __builtin_amdgcn_exp2f(__builtin_fmaf(v, -L, mnl));
    sp += ep[i]; sn += en[i];
  }
  const float rp = __builtin_amdgcn_rcpf(sp), rn = __builtin_amdgcn_rcpf(sn);
#pragma unroll
  for (int i = 0; i < 10; ++i) {
    o[i] = pk2(ep[2 * i] * rp, ep[2 * i + 1] * rp);
    o[10 + i] = pk2(en[2 * i] * rn, en[2 * i + 1] * rn);
  }
}
DI float silu(float v) { return v * __builtin_amdgcn_rcpf(1.f + __builtin_amdgcn_exp2f(v * -1.4426950408889634f)); }

#define STAMP(i)

__global__ __launch_bounds__(512, 2) void fused_kernel(const char* __restrict__ x, const char* __restrict__ wqkv,
                                                       const char* __restrict__ wphi, const char* __restrict__ wo,
                                                       const char* __restrict__ w1, const char* __restrict__ w2,
                                                       const char* __restrict__ gate, char* out, char* ws) {
  extern __shared__ __attribute__((aligned(16))) char smem[];
  STAMP(0);
  const int tid = threadIdx.x, lane = tid & 63;
  const int w = __builtin_amdgcn_readfirstlane(tid >> 6);
  const int b = blockIdx.x, xcd = b & 7, idx = b >> 3;
  const int rt = (xcd & 1) * 8 + (idx & 7);
  const int cb = idx >> 3, cq = xcd >> 1;
  const bool isq = cb < 2;
  const int jg = 2 * cq + (cb & 1);
  const int rt64 = (xcd >> 1) * 8 + (idx >> 2), ct = (xcd & 1) * 4 + (idx & 3);
  const int g = lane >> 4, c16 = lane & 15, q4 = c16 >> 2, p4 = lane & 3;
  const int mw = w & 3, nw = w >> 2;

  const int unit = b * 3 + w;
  const bool retile = (w < 3) && (unit < 740);
  const int rk = unit / 10, rcu = unit - rk * 10;
  if (retile) {
    const char* src = rk < 54 ? w2 : wo;
    const int kp = rk - 54, P = kp / 5, s5 = kp - P * 5;
#pragma unroll
    for (int n = 0; n < 4; ++n) {
      const int rho = 8 * n + (lane >> 3), c = lane & 7;
      const int t = rho >> 4, gg = (rho >> 2) & 3, e = rho & 3;
      int row;
      if (rk < 54) row = 32 * rk + rho;
      else if (s5 < 4) row = 80 * (2 * P + (s5 >> 1)) + 20 * gg + 8 * (s5 & 1) + 4 * t + e;
      else row = 80 * (2 * P + t) + 20 * gg + 16 + e;
      const int sc = (((c >> 1) ^ ((rho >> 1) & 3)) << 1) | (c & 1);
      glds(row * 1280 + rcu * 128 + sc * 16, src, L_MISC + w * 4096 + n * 1024);
    }
  } else if (isq && w >= 3 && w < 7) {
    int ch = (w - 3) * 64 + lane; ch = ch < 200 ? ch : 199;
    glds(jg * 3200 + ch * 16, wphi, L_WPHI + (w - 3) * 1024);
  } else if (isq && w == 7) {
    glds(lane * 16, gate, L_GATE);
    glds((lane < 16 ? 64 + lane : 79) * 16, gate, L_GATE + 1024);
  }
  unsigned xoff0, xoff1, woff[4];
  {
    const int row = 8 * w + (lane >> 3), c = lane & 7;
    xoff0 = (rt * 128 + row) * 1280 + ((c ^ ((row >> 1) & 7)) << 4);
    xoff1 = xoff0 + 64 * 1280;
  }
  const int ldw = isq ? 3840 : 3456;
  const int hstart = jg * 448 - (jg == 7 ? 64 : 0);
  const int npos = jg < 6 ? 28 : 24;
#pragma unroll
  for (int m = 0; m < 4; ++m) {
    const int ch = 64 * (w + 8 * m) + lane;
    const int rho = ch / 30, pos = ch - rho * 30;
    const int k = (rho & 32) + 8 * ((rho >> 2) & 3) + 4 * ((rho >> 4) & 1) + (rho & 3);
    const int sec = pos / 10;
    const int col = isq ? sec * 1280 + jg * 160 + (pos - sec * 10) * 16 : hstart + (pos < npos ? pos : npos - 1) * 16;
    woff[m] = k * ldw + col;
  }
  const char* xs = x;
  const char* wsrc = isq ? wqkv : w1;
  auto issue_stage = [&](unsigned sb) {
    glds(xoff0, xs, sb + w * 1024);
    glds(xoff1, xs, sb + 8192 + w * 1024);
    glds(woff[0], wsrc, sb + 16384 + w * 1024);
    glds(woff[1], wsrc, sb + 16384 + 8192 + w * 1024);
    glds(woff[2], wsrc, sb + 16384 + 16384 + w * 1024);
    if (w < 6) glds(woff[3], wsrc, sb + 16384 + 24576 + w * 1024);
    xs += 128;
    wsrc += 64 * ldw;
  };
  issue_stage(L_S0);
  issue_stage(L_S1);

  if (tid < 128) *(LAS u32x4*)(L_ZERO + tid * 16) = (u32x4){0u, 0u, 0u, 0u};
  if (tid == 128) *(LAS u32x4*)(L_ONE) = (u32x4){0x3f803f80u, 0x3f803f80u, 0x3f803f80u, 0x3f803f80u};
  u64 epoch;
  asm volatile("s_load_dwordx2 %0, %1, 0x0\n\ts_waitcnt lgkmcnt(0)" : "=s"(epoch) : "s"(ws) : "memory");
  const u64 tag = epoch * 6364136223846793005ull + 1442695040888963407ull;
  const unsigned tlo = (unsigned)tag, thi = (unsigned)(tag >> 32);
  const u32x2 tagv = {tlo, thi};

  const unsigned xrd0 = (32 * mw + c16) * 128 + ((g ^ ((c16 >> 1) & 7)) << 4);
  const unsigned xrd1 = xrd0 ^ 64;
  const unsigned wrd = 16384 + (4 * g + q4) * 480 + (isq ? 40 : 8) * p4;
  int toff[8];
#pragma unroll
  for (int t = 0; t < 8; ++t) {
    int tq = nw == 0 ? (t < 5 ? 160 + 8 * t : 320 + 8 * (t - 5)) : (t < 5 ? 8 * t : 320 + 8 * (t - 2));
    if (t == 7 && nw == 1) tq = 0;
    const int hsplit = jg < 6 ? 256 : 192;
    int th = (nw == 0 ? 0 : hsplit) + 32 * t;
    if (th > 448) th = 448;
    toff[t] = isq ? tq : th;
  }
  const int nt = isq ? 8 : ((nw == 0 && jg < 6) ? 8 : 6);
  const bool gatew = isq && nw == 1;
  const unsigned boff = lane * 16;
  const char* bsrc = ws + WS_B + ct * (74 * 5120);

  f32x4 acc[8][2];
#pragma unroll
  for (int t = 0; t < 8; ++t) { acc[t][0] = (f32x4){0.f, 0.f, 0.f, 0.f}; acc[t][1] = (f32x4){0.f, 0.f, 0.f, 0.f}; }
  u32x4 rtf0 = {0u, 0u, 0u, 0u}, rtf1 = {0u, 0u, 0u, 0u};
  STAMP(1);

  unsigned sb = L_S0, sbn = L_S2;
#pragma unroll 1
  for (int t = 0; t < 10; ++t) {
    if (t == 9) { WAITV(5); }
    else if (retile && (t == 1 || t == 2)) { WAITV(10); }
    else if (w < 6) { WAITV(6); }
    else { WAITV(5); }
    if (t == 8 && w == 7) {
      asm volatile("" : "+v"(rtf0), "+v"(rtf1));
      bool ok = rtf0[0] == tlo && rtf0[1] == thi && rtf0[2] == tlo && rtf0[3] == thi && rtf1[0] == tlo && rtf1[1] == thi &&
                rtf1[2] == tlo && rtf1[3] == thi;
      if (!__all(ok)) {
        poll_words(ws, WS_RT, 64, lane, tlo, thi);
        poll_words(ws, WS_RT + 512, 64, lane, tlo, thi);
        poll_words(ws, WS_RT + 1024, 64, lane, tlo, thi);
        poll_words(ws, WS_RT + 1536, 64, lane, tlo, thi);
      }
    }
    BAR();
    if (t < 8) issue_stage(sbn);
    else glds5_sc1(boff, bsrc + ((t - 8) * 8 + w) * 5120, sbn + w * 5120);
    if (t == 0 && retile) {
      const unsigned base = L_MISC + w * 4096 + (4 * g + q4) * 128 + 8 * p4;
      const int sw = (2 * g + (q4 >> 1)) & 3;
#pragma unroll
      for (int n = 0; n < 4; ++n) {
        const unsigned a = base + ((n ^ sw) << 5);
        u32x4 f = cat(ldstr(a), ldstr(a + 2048));
        const int N = 4 * rcu + n, c5 = N / 5;
        gst128_sc1(boff, ws + WS_B + ((c5 * 74 + rk) * 5 + (N - c5 * 5)) * 1024, f);
      }
    }
    if (t == 3 && w == 0 && lane == 0) gst64_sc1(b * 8, ws + WS_RT, tagv);
    if (t == 6 && w == 7) {
      rtf0 = gld128_sc1(lane * 32, ws + WS_RT);
      rtf1 = gld128_sc1(lane * 32 + 16, ws + WS_RT);
    }
    u32x4 xf[2][2], wf[2][8];
    xf[0][0] = lds128(sb + xrd0); xf[0][1] = lds128(sb + xrd0 + 2048);
#pragma unroll
    for (int i = 0; i < 8; ++i) { const unsigned a = wrd + sb + toff[i]; wf[0][i] = cat(ldstr(a), ldstr(a + 7680)); }
    xf[1][0] = lds128(sb + xrd1); xf[1][1] = lds128(sb + xrd1 + 2048);
#pragma unroll
    for (int i = 0; i < 8; ++i) { const unsigned a = wrd + sb + toff[i]; wf[1][i] = cat(ldstr(a + 15360), ldstr(a + 23040)); }
#pragma unroll
    for (int ks = 0; ks < 2; ++ks)
#pragma unroll
      for (int i = 0; i < 6; ++i) {
        acc[i][0] = mfma(wf[ks][i], xf[ks][0], acc[i][0]);
        acc[i][1] = mfma(wf[ks][i], xf[ks][1], acc[i][1]);
      }
    if (nt > 6) {
      if (gatew) {
        wf[0][7] = lds128(L_GATE + t * 128 + g * 16);
        wf[1][7] = lds128(L_GATE + t * 128 + 64 + g * 16);
      }
#pragma unroll
      for (int ks = 0; ks < 2; ++ks)
#pragma unroll
        for (int i = 6; i < 8; ++i) {
          acc[i][0] = mfma(wf[ks][i], xf[ks][0], acc[i][0]);
          acc[i][1] = mfma(wf[ks][i], xf[ks][1], acc[i][1]);
        }
    }
    sbn = sb;
    sb = sb == L_S0 ? L_S1 : sb + SLOT;
  }
  STAMP(2);
  BAR();
  if (b == 0 && tid == 0) gst64_sc1(0, ws + WS_EPOCH, tagv);

  if (!isq) {
    glds5_sc1(boff, bsrc + (16 + w) * 5120, L_S0 + w * 5120);
    const int k0 = (hstart >> 6) + (nw == 0 ? 0 : (jg < 6 ? 4 : 3));
    char* abase = ws + WS_A + ((rt * 8 + 2 * mw) * 74 + k0) * 1024;
#pragma unroll
    for (int pr = 0; pr < 4; ++pr) {
      if (pr < 3 || nt > 6) {
#pragma unroll
        for (int mt = 0; mt < 2; ++mt) {
          const f32x4 a = acc[2 * pr][mt], c = acc[2 * pr + 1][mt];
          u32x4 f = {pk2(silu(a[0]), silu(a[1])), pk2(silu(a[2]), silu(a[3])), pk2(silu(c[0]), silu(c[1])), pk2(silu(c[2]), silu(c[3]))};
          gst128_sc1(boff, abase + (mt * 74 + pr) * 1024, f);
        }
      }
    }
    STAMP(3);
    WAITV(0);
    BAR();
    if (tid == 0) gst64_sc1(rt * 128 + jg * 8, ws + WS_HF, tagv);
    STAMP(4);
  } else {
#pragma unroll
    for (int i = 5; i < 8; ++i) {
      if (i < 7 || nw == 0) {
        const int tt = nw == 0 ? i - 5 : i - 2;
#pragma unroll
        for (int mt = 0; mt < 2; ++mt) {
          const f32x4 a = acc[i][mt];
          *(LAS u32x2*)(L_V + (32 * mw + 16 * mt + c16) * 160 + 40 * g + 8 * tt) = (u32x2){pk2(a[0], a[1]), pk2(a[2], a[3])};
        }
      }
    }
    u32x4 kf[2][3];
#pragma unroll
    for (int mt = 0; mt < 2; ++mt) {
      kf[mt][0] = (u32x4){pk2(acc[0][mt][0], acc[0][mt][1]), pk2(acc[0][mt][2], acc[0][mt][3]), pk2(acc[1][mt][0], acc[1][mt][1]), pk2(acc[1][mt][2], acc[1][mt][3])};
      kf[mt][1] = (u32x4){pk2(acc[2][mt][0], acc[2][mt][1]), pk2(acc[2][mt][2], acc[2][mt][3]), pk2(acc[3][mt][0], acc[3][mt][1]), pk2(acc[3][mt][2], acc[3][mt][3])};
      kf[mt][2] = (u32x4){pk2(acc[4][mt][0], acc[4][mt][1]), pk2(acc[4][mt][2], acc[4][mt][3]), 0u, 0u};
    }
    float gv[2];
#pragma unroll
    for (int mt = 0; mt < 2; ++mt) gv[mt] = __builtin_amdgcn_rcpf(1.f + __builtin_amdgcn_exp2f(acc[7][mt][0] * -1.4426950408889634f));
    const unsigned pbase = (p4 == g) ? L_WPHI + g * 800 + q4 * 40 : L_ZERO;
    f32x4 pp[2][5];
#pragma unroll
    for (int t = 0; t < 5; ++t) {
      const u32x4 a0 = cat(ldstr(pbase + 8 * t), ldstr(pbase + 8 * t + 160));
      const u32x4 a1 = cat(ldstr(pbase + 8 * t + 320), ldstr(pbase + 8 * t + 480));
      const u32x4 a2 = cat(ldstr(pbase + 8 * t + 640), (u32x2){0u, 0u});
#pragma unroll
      for (int mt = 0; mt < 2; ++mt) {
        f32x4 c = {0.f, 0.f, 0.f, 0.f};
        c = mfma(a0, kf[mt][0], c);
        c = mfma(a1, kf[mt][1], c);
        c = mfma(a2, kf[mt][2], c);
        pp[mt][t] = c;
      }
    }
    unsigned phi[2][20];
    dual_softmax(pp[0], phi[0]);
    dual_softmax(pp[1], phi[1]);
    if (nw == 0) {
#pragma unroll
      for (int mt = 0; mt < 2; ++mt)
#pragma unroll
        for (int i = 0; i < 5; ++i)
          *(LAS u32x4*)(L_PHIK + (32 * mw + 16 * mt + c16) * 352 + 80 * g + 16 * i) =
              (u32x4){phi[mt][4 * i], phi[mt][4 * i + 1], phi[mt][4 * i + 2], phi[mt][4 * i + 3]};
    } else if (jg == 0 && g == 0) {
      gst32_sc1((rt * 128 + 32 * mw + c16) * 4, ws + WS_G, __builtin_bit_cast(unsigned, gv[0]));
      gst32_sc1((rt * 128 + 32 * mw + 16 + c16) * 4, ws + WS_G, __builtin_bit_cast(unsigned, gv[1]));
    }
    BAR();
    STAMP(3);
    {
      const unsigned ab = L_PHIK + (4 * g + q4) * 352 + 80 * mw + 8 * p4;
      const unsigned ab2 = L_PHIK + (4 * g + q4) * 352 + 80 * mw + 64 + 8 * (p4 & 1);
      const bool vreal = nw == 0 || p4 == 0;
      const unsigned vb = vreal ? L_V + (4 * g + q4) * 160 + 40 * mw + 32 * nw + 8 * p4 : L_ONE;
      const unsigned vs = vreal ? 16 * 160 : 0;
      f32x4 kv[3];
#pragma unroll
      for (int i = 0; i < 3; ++i) kv[i] = (f32x4){0.f, 0.f, 0.f, 0.f};
#pragma unroll
      for (int ks = 0; ks < 4; ++ks) {
        const u32x4 vf = cat(ldstr(vb + (2 * ks) * vs), ldstr(vb + (2 * ks + 1) * vs));
        const unsigned o = ks * 32 * 352;
        kv[0] = mfma(cat(ldstr(ab + o), ldstr(ab + o + 16 * 352)), vf, kv[0]);
        kv[1] = mfma(cat(ldstr(ab + o + 32), ldstr(ab + o + 32 + 16 * 352)), vf, kv[1]);
        kv[2] = mfma(cat(ldstr(ab2 + o), ldstr(ab2 + o + 16 * 352)), vf, kv[2]);
      }
      char* pb = ws + WS_PART + (jg * 16 + rt) * PART_STRIDE;
      const unsigned po = mw * 1680 + (16 * nw + c16) * 80 + 8 * g;
      if (nw == 0 || c16 < 5) {
        gst64_sc1(po, pb, (u32x2){pk2(kv[0][0], kv[0][1]), pk2(kv[0][2], kv[0][3])});
        gst64_sc1(po + 32, pb, (u32x2){pk2(kv[1][0], kv[1][1]), pk2(kv[1][2], kv[1][3])});
        if (g < 2) gst64_sc1(po + 64, pb, (u32x2){pk2(kv[2][0], kv[2][1]), pk2(kv[2][2], kv[2][3])});
      }
    }
    WAITV(0);
    BAR();
    if (tid == 0) gst64_sc1(jg * 128 + rt * 8, ws + WS_KF, tagv);
    STAMP(4);
    if (w == 0) poll_words(ws, WS_KF + jg * 128, 16, lane, tlo, thi);
    glds5_sc1(boff, bsrc + (16 + w) * 5120, L_S0 + w * 5120);
    BAR();
    STAMP(5);
    if (w < 7) {
      const unsigned go = (tid < 420 ? tid : 419) * 16;
      const char* pb = ws + WS_PART + jg * 16 * PART_STRIDE;
      u32x4 pv[16];
#pragma unroll
      for (int i = 0; i < 16; ++i) pv[i] = gld128_sc1(go, pb + i * PART_STRIDE);
      asm volatile("s_waitcnt vmcnt(0)"
                   : "+v"(pv[0]), "+v"(pv[1]), "+v"(pv[2]), "+v"(pv[3]), "+v"(pv[4]), "+v"(pv[5]), "+v"(pv[6]), "+v"(pv[7]),
                     "+v"(pv[8]), "+v"(pv[9]), "+v"(pv[10]), "+v"(pv[11]), "+v"(pv[12]), "+v"(pv[13]), "+v"(pv[14]), "+v"(pv[15])::"memory");
      float s[8];
#pragma unroll
      for (int e = 0; e < 8; ++e) s[e] = 0.f;
#pragma unroll
      for (int i = 0; i < 16; ++i)
#pragma unroll
        for (int e = 0; e < 4; ++e) {
          s[2 * e] += __builtin_bit_cast(float, pv[i][e] << 16);
          s[2 * e + 1] += __builtin_bit_cast(float, pv[i][e] & 0xffff0000u);
        }
      if (tid < 420) *(LAS u32x4*)(L_KV + go) = (u32x4){pk2(s[0], s[1]), pk2(s[2], s[3]), pk2(s[4], s[5]), pk2(s[6], s[7])};
    }
    BAR();
    STAMP(6);
    if (nw == 1) {
      const unsigned kb = (q4 == g) ? L_KV + g * 1680 + p4 * 80 : L_ZERO;
      const unsigned zb = (q4 == g) ? L_KV + g * 1680 + 1600 : L_ZERO;
      f32x4 o[2][6];
#pragma unroll
      for (int t = 0; t < 6; ++t) { o[0][t] = (f32x4){0.f, 0.f, 0.f, 0.f}; o[1][t] = (f32x4){0.f, 0.f, 0.f, 0.f}; }
#pragma unroll
      for (int s = 0; s < 5; ++s) {
        const u32x4 b0 = {phi[0][4 * s], phi[0][4 * s + 1], phi[0][4 * s + 2], phi[0][4 * s + 3]};
        const u32x4 b1 = {phi[1][4 * s], phi[1][4 * s + 1], phi[1][4 * s + 2], phi[1][4 * s + 3]};
#pragma unroll
        for (int t = 0; t < 6; ++t) {
          const u32x4 a = t < 5 ? lds128(kb + t * 320 + s * 16) : lds128(zb + s * 16);
          o[0][t] = mfma(a, b0, o[0][t]);
          o[1][t] = mfma(a, b1, o[1][t]);
        }
      }
      char* abase = ws + WS_A + ((rt * 8 + 2 * mw) * 74 + 54 + (jg >> 1) * 5) * 1024;
#pragma unroll
      for (int mt = 0; mt < 2; ++mt) {
        const float sc = gv[mt] * __builtin_amdgcn_rcpf(o[mt][5][0] + 1e-6f);
        unsigned r[10];
#pragma unroll
        for (int t = 0; t < 5; ++t) { r[2 * t] = pk2(o[mt][t][0] * sc, o[mt][t][1] * sc); r[2 * t + 1] = pk2(o[mt][t][2] * sc, o[mt][t][3] * sc); }
        gst128_sc1(boff, abase + (mt * 74 + 2 * (jg & 1)) * 1024, (u32x4){r[0], r[1], r[2], r[3]});
        gst128_sc1(boff, abase + (mt * 74 + 2 * (jg & 1) + 1) * 1024, (u32x4){r[4], r[5], r[6], r[7]});
        gst64_sc1(boff + 8 * (jg & 1), abase + (mt * 74 + 4) * 1024, (u32x2){r[8], r[9]});
      }
    }
    WAITV(0);
    BAR();
    if (tid == 0) gst64_sc1(rt * 128 + jg * 8, ws + WS_OF, tagv);
    STAMP(7);
  }

  if (w == 0) poll_words(ws, WS_HF + (rt64 >> 1) * 128, 8, lane, tlo, thi);
  BAR();
  STAMP(8);
  {
    const char* asrc = ws + WS_A + ((rt64 * 4 + mw) * 74 + nw) * 1024;
    u32x4 af[37];
#pragma unroll
    for (int a = 0; a < 27; ++a) af[a] = gld128_sc1(boff, asrc + a * 2048);
    f32x4 o2[5];
#pragma unroll
    for (int n = 0; n < 5; ++n) o2[n] = (f32x4){0.f, 0.f, 0.f, 0.f};
    const unsigned brd = nw * 5120 + lane * 16;
    u32x2 ofl = {0u, 0u};
    unsigned gbits = 0;
#define KSTEP(SL, J, A)                                                          \
  {                                                                              \
    PIN(af[A]);                                                                  \
    u32x4 bf[5];                                                                 \
    _Pragma("unroll") for (int n = 0; n < 5; ++n) bf[n] = lds128(SL + brd + (J) * 10240 + n * 1024); \
    _Pragma("unroll") for (int n = 0; n < 5; ++n) o2[n] = mfma(bf[n], af[A], o2[n]);                 \
  }
#define STAGE4(SL, A) KSTEP(SL, 0, A) KSTEP(SL, 1, A + 1) KSTEP(SL, 2, A + 2) KSTEP(SL, 3, A + 3)
#define ISSUEB(K0, CNT, SL) if (w < (CNT)) glds5_sc1(boff, bsrc + ((K0) + w) * 5120, SL + w * 5120)
    WAITV(23); STAGE4(L_S1, 0)
    BAR(); ISSUEB(24, 8, L_S1); WAITV(24); STAGE4(L_S2, 4)
    BAR(); ISSUEB(32, 8, L_S2); WAITV(25); STAGE4(L_S0, 8)
    WAITV(5); BAR(); ISSUEB(40, 8, L_S0); STAGE4(L_S1, 12)
    WAITV(5); BAR();
    if (w == 0) ofl = gld64_sc1(WS_OF + (rt64 >> 1) * 128 + (lane & 7) * 8, ws);
    ISSUEB(48, 6, L_S1); STAGE4(L_S2, 16)
    if (w < 6) { WAITV(5); } else { WAITV(0); }
    if (w == 0) {
      PIN(ofl);
      if (!__all(ofl[0] == tlo && ofl[1] == thi)) poll_words(ws, WS_OF + (rt64 >> 1) * 128, 8, lane, tlo, thi);
    }
    BAR();
    STAMP(9);
    ISSUEB(54, 8, L_S2);
    gbits = gld32_sc1((rt64 * 64 + mw * 16 + c16) * 4, ws + WS_G);
#pragma unroll
    for (int a = 27; a < 37; ++a) af[a] = gld128_sc1(boff, asrc + a * 2048);
    STAGE4(L_S0, 20)
    WAITV(16); BAR(); ISSUEB(62, 8, L_S0);
    KSTEP(L_S1, 0, 24) KSTEP(L_S1, 1, 25) KSTEP(L_S1, 2, 26)
    WAITV(11); BAR(); ISSUEB(70, 4, L_S1);
    {
      PIN(gbits);
      const float omg = 1.f - __builtin_bit_cast(float, gbits);
#pragma unroll
      for (int n = 0; n < 5; ++n) o2[n] *= omg;
    }
    STAGE4(L_S2, 27)
    if (w < 4) { WAITV(5); } else { WAITV(0); }
    BAR(); STAGE4(L_S0, 31)
    WAITV(0); BAR();
    KSTEP(L_S1, 0, 35) KSTEP(L_S1, 1, 36)
    STAMP(10);
    const unsigned rb = L_S2 + mw * 5120 + lane * 16;
    if (nw == 1) {
#pragma unroll
      for (int n = 0; n < 3; ++n) *(LAS f32x4*)(rb + n * 1024) = o2[n];
    } else {
#pragma unroll
      for (int n = 3; n < 5; ++n) *(LAS f32x4*)(rb + n * 1024) = o2[n];
    }
    BAR();
    const unsigned oo = (rt64 * 64 + mw * 16 + c16) * 1280 + ct * 160 + g * 8;
    if (nw == 0) {
#pragma unroll
      for (int n = 0; n < 3; ++n) {
        const f32x4 r = o2[n] + *(LAS f32x4*)(rb + n * 1024);
        gst64(oo + n * 32, out, (u32x2){pk2(r[0], r[1]), pk2(r[2], r[3])});
      }
    } else {
#pragma unroll
      for (int n = 3; n < 5; ++n) {
        const f32x4 r = o2[n] + *(LAS f32x4*)(rb + n * 1024);
        gst64(oo + n * 32, out, (u32x2){pk2(r[0], r[1]), pk2(r[2], r[3])});
      }
    }
  }
}

extern "C" void kernel_launch(void* const* d_in, const int* in_sizes, int n_in, void* d_out, int out_size, void* d_ws,
                              size_t ws_size, hipStream_t stream) {
  (void)in_sizes; (void)n_in; (void)out_size; (void)ws_size;
  hipFuncSetAttribute((const void*)fused_kernel, hipFuncAttributeMaxDynamicSharedMemorySize, LDS_BYTES);
  fused_kernel<<<256, 512, LDS_BYTES, stream>>>((const char*)d_in[0], (const char*)d_in[1], (const char*)d_in[2],
                                                (const char*)d_in[3], (const char*)d_in[5], (const char*)d_in[6],
                                                (const char*)d_in[7], (char*)d_out, (char*)d_ws);
}
```

```cpp
#include <hip/hip_runtime.h>
#include <stdint.h>

typedef __attribute__((ext_vector_type(8))) __bf16 bf16x8;
typedef __attribute__((ext_vector_type(2))) __bf16 bf16x2;
typedef __attribute__((ext_vector_type(4))) short s16x4;
typedef __attribute__((ext_vector_type(4))) float f32x4;
typedef __attribute__((ext_vector_type(2))) float f32x2;
typedef __attribute__((ext_vector_type(4))) unsigned u32x4;
typedef __attribute__((ext_vector_type(2))) unsigned u32x2;
typedef unsigned long long u64;
#define LAS __attribute__((address_space(3)))
#define DI __device__ __forceinline__

constexpr unsigned WS_EPOCH = 0;
constexpr unsigned WS_RT = 1024;
constexpr unsigned WS_HF = 4096;
constexpr unsigned WS_OF = 12288;
constexpr unsigned WS_G = 20480;
constexpr unsigned WS_RED = 32768;
constexpr unsigned GR_BYTES = 840 * 16;
constexpr unsigned WS_PART = 32768 + 8 * GR_BYTES;
constexpr unsigned WS_B = 2u << 20;
constexpr unsigned WS_A = 5u << 20;
constexpr unsigned WS_PROF = 24u << 20;
static_assert(WS_PART + 128 * GR_BYTES <= WS_B, "ws");

constexpr unsigned SLOT = 47104;
constexpr unsigned L_S1 = 0, L_S2 = SLOT, L_S0 = 2 * SLOT, L_MISC = 3 * SLOT;
constexpr unsigned L_PHIK = L_S2 + 40960;
constexpr unsigned L_V = L_PHIK + 128 * 352;
constexpr unsigned L_KV = L_MISC;
constexpr unsigned L_WPHI = 153600;
constexpr unsigned L_GATE = L_WPHI;
constexpr unsigned L_ZERO = L_WPHI + 4096;
constexpr unsigned L_ONE = L_ZERO + 2048;
constexpr unsigned LDS_BYTES = 163840;
static_assert(L_ONE + 16 <= LDS_BYTES, "lds");

constexpr unsigned SPIN_CAP = 1u << 16;

DI unsigned pk2(float a, float b) {
  bf16x2 v = __builtin_convertvector((f32x2){a, b}, bf16x2);
  return __builtin_bit_cast(unsigned, v);
}
DI f32x4 mfma(u32x4 a, u32x4 b, f32x4 c) {
  return __builtin_amdgcn_mfma_f32_16x16x32_bf16(__builtin_bit_cast(bf16x8, a), __builtin_bit_cast(bf16x8, b), c, 0, 0, 0);
}
DI u32x4 lds128(unsigned a) { return *(LAS u32x4*)(size_t)a; }
DI u32x2 lds64(unsigned a) { return *(LAS u32x2*)(size_t)a; }
DI void sts128(unsigned a, u32x4 v) { *(LAS u32x4*)(size_t)a = v; }
DI void sts64(unsigned a, u32x2 v) { *(LAS u32x2*)(size_t)a = v; }
DI u32x2 ldstr(unsigned a) {
  return __builtin_bit_cast(u32x2, __builtin_amdgcn_ds_read_tr16_b64_v4i16((LAS s16x4*)(size_t)a));
}
DI u32x4 cat(u32x2 a, u32x2 b) { return (u32x4){a[0], a[1], b[0], b[1]}; }
DI void glds(unsigned voff, const void* sbase, unsigned lds) {
  asm volatile("s_mov_b32 m0, %2\n\ts_nop 0\n\tglobal_load_lds_dwordx4 %0, %1" ::"v"(voff), "s"(sbase), "s"(lds) : "memory");
}
DI void glds_m(unsigned voff, const void* sbase, unsigned lds, u64 mask) {
  asm volatile("s_mov_b32 m0, %2\n\ts_mov_b64 exec, %3\n\tglobal_load_lds_dwordx4 %0, %1\n\ts_mov_b64 exec, -1" ::"v"(voff), "s"(sbase), "s"(lds), "s"(mask) : "memory");
}
DI void glds5_sc1(unsigned voff, unsigned voff1k, const void* sbase, unsigned lds) {
  asm volatile(
      "s_mov_b32 m0, %2\n\ts_nop 0\n\t"
      "global_load_lds_dwordx4 %0, %1 sc1\n\t"
      "global_load_lds_dwordx4 %0, %1 offset:1024 sc1\n\t"
      "global_load_lds_dwordx4 %0, %1 offset:2048 sc1\n\t"
      "global_load_lds_dwordx4 %0, %1 offset:3072 sc1\n\t"
      "s_add_u32 m0, m0, 1024\n\ts_nop 0\n\t"
      "global_load_lds_dwordx4 %3, %1 offset:3072 sc1" ::"v"(voff), "s"(sbase), "s"(lds), "v"(voff1k)
      : "memory", "scc");
}
DI u32x4 gld128_sc1(unsigned voff, const void* sbase) {
  u32x4 r;
  asm volatile("global_load_dwordx4 %0, %1, %2 sc1" : "=v"(r) : "v"(voff), "s"(sbase) : "memory");
  return r;
}
DI u32x2 gld64_sc1(unsigned voff, const void* sbase) {
  u32x2 r;
  asm volatile("global_load_dwordx2 %0, %1, %2 sc1" : "=v"(r) : "v"(voff), "s"(sbase) : "memory");
  return r;
}
DI unsigned gld32_sc1(unsigned voff, const void* sbase) {
  unsigned r;
  asm volatile("global_load_dword %0, %1, %2 sc1" : "=v"(r) : "v"(voff), "s"(sbase) : "memory");
  return r;
}
DI unsigned gld32(unsigned voff, const void* sbase) {
  unsigned r;
  asm volatile("global_load_dword %0, %1, %2" : "=v"(r) : "v"(voff), "s"(sbase) : "memory");
  return r;
}
DI void gst128_sc1(unsigned voff, void* sbase, u32x4 v) {
  asm volatile("global_store_dwordx4 %0, %1, %2 sc1\n\ts_nop 1" ::"v"(voff), "v"(v), "s"(sbase) : "memory");
}
DI void gst64_sc1(unsigned voff, void* sbase, u32x2 v) {
  asm volatile("global_store_dwordx2 %0, %1, %2 sc1" ::"v"(voff), "v"(v), "s"(sbase) : "memory");
}
DI void gst32_sc1(unsigned voff, void* sbase, unsigned v) {
  asm volatile("global_store_dword %0, %1, %2 sc1" ::"v"(voff), "v"(v), "s"(sbase) : "memory");
}
DI void gst64(unsigned voff, void* sbase, u32x2 v) {
  asm volatile("global_store_dwordx2 %0, %1, %2" ::"v"(voff), "v"(v), "s"(sbase) : "memory");
}
#define WAITV(n) asm volatile("s_waitcnt vmcnt(" #n ")" ::: "memory")
#define BAR() asm volatile("s_waitcnt lgkmcnt(0)\n\ts_barrier" ::: "memory")
#define PIN(x) asm volatile("" : "+v"(x))
#define SB() __builtin_amdgcn_sched_barrier(0)

DI void poll_words(const char* base, unsigned voff, unsigned tlo, unsigned thi) {
#pragma unroll 1
  for (unsigned spins = 0; spins < SPIN_CAP; ++spins) {
    u32x2 v = gld64_sc1(voff, base);
    asm volatile("s_waitcnt vmcnt(0)" : "+v"(v)::"memory");
    if (__all(v[0] == tlo && v[1] == thi)) break;
    __builtin_amdgcn_s_sleep(1);
  }
}

DI void dual_softmax(const f32x4 (&p)[5], unsigned (&o)[20]) {
  float mx = p[0][0], mn = p[0][0];
#pragma unroll
  for (int i = 1; i < 20; ++i) { mx = fmaxf(mx, p[i >> 2][i & 3]); mn = fminf(mn, p[i >> 2][i & 3]); }
  const float L = 1.4426950408889634f;
  const float mxl = -mx * L, mnl = mn * L;
  float ep[20], en[20], sp = 0.f, sn = 0.f;
#pragma unroll
  for (int i = 0; i < 20; ++i) {
    float v = p[i >> 2][i & 3];
    ep[i] = __builtin_amdgcn_exp2f(__builtin_fmaf(v, L, mxl));
    en[i] = __builtin_amdgcn_exp2f(__builtin_fmaf(v, -L, mnl));
    sp += ep[i]; sn += en[i];
  }
  const float rp = __builtin_amdgcn_rcpf(sp), rn = __builtin_amdgcn_rcpf(sn);
#pragma unroll
  for (int i = 0; i < 10; ++i) {
    o[i] = pk2(ep[2 * i] * rp, ep[2 * i + 1] * rp);
    o[10 + i] = pk2(en[2 * i] * rn, en[2 * i + 1] * rn);
  }
}
DI float silu(float v) { return v * __builtin_amdgcn_rcpf(1.f + __builtin_amdgcn_exp2f(v * -1.4426950408889634f)); }

DI void phi_unit(const unsigned (&kq)[10], unsigned pbase, unsigned (&phi)[20]) {
  const u32x4 f0 = {kq[0], kq[1], kq[2], kq[3]}, f1 = {kq[4], kq[5], kq[6], kq[7]}, f2 = {kq[8], kq[9], 0u, 0u};
  u32x4 a0[5], a1[5], a2[5];
#pragma unroll
  for (int t = 0; t < 5; ++t) {
    a0[t] = cat(ldstr(pbase + 8 * t), ldstr(pbase + 8 * t + 160));
    a1[t] = cat(ldstr(pbase + 8 * t + 320), ldstr(pbase + 8 * t + 480));
    a2[t] = cat(ldstr(pbase + 8 * t + 640), (u32x2){0u, 0u});
  }
  SB();
  f32x4 pp[5];
#pragma unroll
  for (int t = 0; t < 5; ++t) {
    f32x4 c = {0.f, 0.f, 0.f, 0.f};
    c = mfma(a0[t], f0, c);
    c = mfma(a1[t], f1, c);
    pp[t] = mfma(a2[t], f2, c);
  }
  dual_softmax(pp, phi);
}

#define STAMP(i)

__global__ __launch_bounds__(768, 3) void fused_kernel(const char* __restrict__ x, const char* __restrict__ wqkv,
                                                       const char* __restrict__ wphi, const char* __restrict__ wo,
                                                       const char* __restrict__ w1, const char* __restrict__ w2,
                                                       const char* __restrict__ gate, char* out, char* ws) {
  extern __shared__ __attribute__((aligned(16))) char smem[];
  STAMP(0);
  const int tid = threadIdx.x, lane = tid & 63;
  const int w = __builtin_amdgcn_readfirstlane(tid >> 6);
  const int b = blockIdx.x, xcd = b & 7, idx = b >> 3;
  const int rt = (xcd & 1) * 8 + (idx & 7);
  const int cb = idx >> 3, cq = xcd >> 1;
  const bool isq = cb < 2;
  const int jg = 2 * cq + (cb & 1);
  const int rt64 = (xcd >> 1) * 8 + (idx >> 2), ct = (xcd & 1) * 4 + (idx & 3);
  const int g = lane >> 4, c16 = lane & 15, q4 = c16 >> 2, p4 = lane & 3;
  const int hstart = jg * 448 - (jg == 7 ? 64 : 0);
  const unsigned boff = lane * 16;

  if (w >= 8) {
    const int L = w - 8;
    const int ldw = isq ? 3840 : 3456;
    unsigned xo[4], wv[8];
    {
      const unsigned x0 = (rt * 128 + 8 * L + (lane >> 3)) * 1280 + (((lane & 7) ^ ((4 * L + (lane >> 4)) & 7)) << 4);
#pragma unroll
      for (int i = 0; i < 4; ++i) xo[i] = x0 + i * 32 * 1280;
      const int rs = lane >= 30 ? 1 : 0, pos = lane - 30 * rs;
      const int sec = (pos >= 10 ? 1 : 0) + (pos >= 20 ? 1 : 0);
      const int col = isq ? sec * 1120 + jg * 160 + pos * 16 : hstart + pos * 16;
      const unsigned w0 = (8 * (L >> 1) + 2 * (L & 1) + rs) * ldw + col;
#pragma unroll
      for (int i = 0; i < 8; ++i) wv[i] = w0 + (32 * (i >> 2) + 16 * (i & 1) + 4 * ((i >> 1) & 1)) * ldw;
    }
    const u64 half = isq ? 0x3fffffffull : (jg < 6 ? 0x0fffffffull : 0x00ffffffull);
    const u64 wmask = half | (half << 30);
    const char* xs = x;
    const char* wsrc = isq ? wqkv : w1;
    auto issue_stage = [&](unsigned sb) {
#pragma unroll
      for (int i = 0; i < 4; ++i) {
        glds(xo[i], xs, sb + (L + 4 * i) * 1024);
        glds_m(wv[2 * i], wsrc, sb + 16384 + (L + 8 * i) * 960, wmask);
        glds_m(wv[2 * i + 1], wsrc, sb + 16384 + (L + 8 * i + 4) * 960, wmask);
      }
      xs += 128;
      wsrc += 64 * ldw;
    };
    const int unit = b * 3 + L;
    const bool retile = (L < 3) && (unit < 740);
    const int rk = unit / 10, rcu = unit - rk * 10;
    const char* bsrc = ws + WS_B + ct * (74 * 5120);
    const unsigned boff1k = boff + 1024;
    auto issue_b = [&](int k0, int nk, unsigned sl) {
#pragma unroll 1
      for (int j = 0; j < nk; ++j) glds5_sc1(boff, boff1k, bsrc + (k0 + L * nk + j) * 5120, sl + (L * nk + j) * 5120);
    };
    unsigned tlo = 0, thi = 0;
#define LSTAMP()
    LSTAMP();
    u32x4 rtf0 = {0u, 0u, 0u, 0u}, rtf1 = {0u, 0u, 0u, 0u};
    unsigned sb = L_S0;
#pragma unroll 1
    for (int t = -3; t < 7; ++t) {
      if (t >= 0) {
        if (retile && t < 2) { WAITV(16); } else { WAITV(12); }
        LSTAMP();
        BAR();
        LSTAMP();
      }
      issue_stage(sb);
      LSTAMP();
      sb = sb == L_S0 ? L_S1 : sb + SLOT;
      if (t == -3) {
        if (retile) {
          const char* src = rk < 54 ? w2 : wo;
          const int kp = rk - 54, P = kp / 5, s5 = kp - P * 5;
#pragma unroll
          for (int n = 0; n < 4; ++n) {
            const int rho = 8 * n + (lane >> 3), c = lane & 7;
            const int tt = n >> 1, gg = (rho >> 2) & 3, e = rho & 3;
            const int r0 = rk < 54 ? 32 * rk + 16 * tt : (s5 < 4 ? 80 * (2 * P + (s5 >> 1)) + 8 * (s5 & 1) + 4 * tt : 80 * (2 * P + tt) + 16);
            const int row = r0 + gg * (rk < 54 ? 4 : 20) + e;
            const int sc = (((c >> 1) ^ ((rho >> 1) & 3)) << 1) | (c & 1);
            glds(row * 1280 + rcu * 128 + sc * 16, src, L_MISC + L * 4096 + n * 1024);
          }
        } else {
          const char* src = isq ? wphi + jg * 3200 : gate;
          const unsigned lim = isq ? 3184 : 1264;
#pragma unroll
          for (int n = 0; n < 4; ++n) {
            const unsigned o = n * 1024 + boff;
            glds(o < lim ? o : lim, src, (L == 3 ? L_WPHI : L_MISC + L * 4096) + n * 1024);
          }
        }
      } else if (t == -1) {
        u64 epoch;
        asm volatile("s_load_dwordx2 %0, %1, 0x0\n\ts_waitcnt lgkmcnt(0)" : "=s"(epoch) : "s"(ws) : "memory");
        const u64 tag = epoch * 6364136223846793005ull + 1442695040888963407ull;
        tlo = (unsigned)tag; thi = (unsigned)(tag >> 32);
        LSTAMP();
        WAITV(24);
        LSTAMP();
        BAR();
        LSTAMP();
        if (retile) {
          const unsigned base = L_MISC + L * 4096 + (4 * g + q4) * 128 + 8 * p4;
          const int sw = (2 * g + (q4 >> 1)) & 3;
          u32x4 f[4];
#pragma unroll
          for (int n = 0; n < 4; ++n) { const unsigned a = base + ((n ^ sw) << 5); f[n] = cat(ldstr(a), ldstr(a + 2048)); }
#pragma unroll
          for (int n = 0; n < 4; ++n) {
            const int N = 4 * rcu + n, c5 = N / 5;
            gst128_sc1(boff, ws + WS_B + ((c5 * 74 + rk) * 5 + (N - c5 * 5)) * 1024, f[n]);
          }
        }
      } else if (t == 2) {
        if (L == 3 && lane == 0) gst64_sc1(b * 8, ws + WS_RT, (u32x2){tlo, thi});
      } else if (t == 5) {
        if (L == 2) {
          asm volatile("global_load_dwordx4 %0, %2, %3 sc1\n\tglobal_load_dwordx4 %1, %2, %3 offset:16 sc1"
                       : "+v"(rtf0), "+v"(rtf1) : "v"(lane * 32), "s"(ws + WS_RT) : "memory");
        }
      }
    }
    const u32x2 tagv = {tlo, thi};
    WAITV(12);
    if (L == 2) {
      asm volatile("" : "+v"(rtf0), "+v"(rtf1));
      const unsigned bad = (rtf0[0] ^ tlo) | (rtf0[1] ^ thi) | (rtf0[2] ^ tlo) | (rtf0[3] ^ thi) | (rtf1[0] ^ tlo) | (rtf1[1] ^ thi) |
                           (rtf1[2] ^ tlo) | (rtf1[3] ^ thi);
      if (!__all(bad == 0)) {
#pragma unroll 1
        for (int i = 0; i < 4; ++i) poll_words(ws + WS_RT + i * 512, lane * 8, tlo, thi);
      }
    }
    BAR();
    if (L < 2) { issue_b(0, 4, L_S1); WAITV(20); } else { WAITV(0); }
    if (b == 0 && L == 2 && lane == 0) gst64_sc1(0, ws + WS_EPOCH, tagv);
    BAR();
    if (L < 2) issue_b(8, 4, L_S2);
    BAR();
    if (isq) { BAR(); BAR(); }
    else if (L < 2) issue_b(16, 4, L_S0);
    if (L == 2) poll_words(ws + WS_HF + rt64 * 256, (lane & 31) * 8, tlo, thi);
    if (L < 2) { if (isq) { WAITV(0); } else { WAITV(20); } }
    BAR();
    if (L < 2) {
      if (isq) issue_b(16, 4, L_S0);
      BAR(); issue_b(24, 4, L_S1); WAITV(20);
      BAR(); issue_b(32, 4, L_S2); WAITV(20);
      BAR(); issue_b(40, 4, L_S0); WAITV(20);
      BAR(); issue_b(48, 3, L_S1); WAITV(15);
      BAR(); issue_b(54, 4, L_S2); WAITV(20);
      BAR(); issue_b(62, 4, L_S0); WAITV(20);
      BAR(); issue_b(70, 2, L_S1); WAITV(10);
      BAR(); WAITV(0);
      BAR();
    } else if (L == 2) {
      BAR(); BAR(); BAR(); BAR(); BAR();
      poll_words(ws + WS_OF + rt64 * 256, (lane & 31) * 8, tlo, thi);
      BAR();
    } else if (isq) {
      const unsigned l60 = lane < 60 ? lane : 59;
      if (rt < 14) {
        const char* pb = ws + WS_PART + jg * 16 * GR_BYTES + rt * 960;
        u32x4 pv[16];
#pragma unroll 1
        for (unsigned spins = 0; spins < SPIN_CAP; ++spins) {
#pragma unroll
          for (int i = 0; i < 16; ++i) pv[i] = gld128_sc1(l60 * 16, pb + i * GR_BYTES);
          asm volatile("s_waitcnt vmcnt(0)"
                       : "+v"(pv[0]), "+v"(pv[1]), "+v"(pv[2]), "+v"(pv[3]), "+v"(pv[4]), "+v"(pv[5]), "+v"(pv[6]), "+v"(pv[7]),
                         "+v"(pv[8]), "+v"(pv[9]), "+v"(pv[10]), "+v"(pv[11]), "+v"(pv[12]), "+v"(pv[13]), "+v"(pv[14]), "+v"(pv[15])::"memory");
          unsigned bad = 0;
#pragma unroll
          for (int i = 0; i < 16; ++i) bad |= (pv[i][1] ^ tlo) | (pv[i][3] ^ thi);
          if (__all(bad == 0)) break;
        }
        float s[4] = {0.f, 0.f, 0.f, 0.f};
#pragma unroll
        for (int i = 0; i < 16; ++i) {
          s[0] += __builtin_bit_cast(float, pv[i][0] << 16);
          s[1] += __builtin_bit_cast(float, pv[i][0] & 0xffff0000u);
          s[2] += __builtin_bit_cast(float, pv[i][2] << 16);
          s[3] += __builtin_bit_cast(float, pv[i][2] & 0xffff0000u);
        }
        if (lane < 60) gst128_sc1(boff, ws + WS_RED + jg * GR_BYTES + rt * 960, (u32x4){pk2(s[0], s[1]), tlo, pk2(s[2], s[3]), thi});
      }
      BAR(); BAR(); BAR();
      {
        const char* rb = ws + WS_RED + jg * GR_BYTES;
        u32x4 pv[14];
#pragma unroll 1
        for (unsigned spins = 0; spins < SPIN_CAP; ++spins) {
#pragma unroll
          for (int i = 0; i < 14; ++i) pv[i] = gld128_sc1(l60 * 16, rb + i * 960);
          asm volatile("s_waitcnt vmcnt(0)"
                       : "+v"(pv[0]), "+v"(pv[1]), "+v"(pv[2]), "+v"(pv[3]), "+v"(pv[4]), "+v"(pv[5]), "+v"(pv[6]), "+v"(pv[7]),
                         "+v"(pv[8]), "+v"(pv[9]), "+v"(pv[10]), "+v"(pv[11]), "+v"(pv[12]), "+v"(pv[13])::"memory");
          unsigned bad = 0;
#pragma unroll
          for (int i = 0; i < 14; ++i) bad |= (pv[i][1] ^ tlo) | (pv[i][3] ^ thi);
          if (__all(bad == 0)) break;
        }
#pragma unroll
        for (int i = 0; i < 14; ++i) sts64(L_KV + i * 480 + l60 * 8, (u32x2){pv[i][0], pv[i][2]});
      }
      BAR();
    }
    return;
  }

  const int mw = w & 3, nw = w >> 2;
  unsigned warm = 0;
  if (w < 4) {
    unsigned ln = b * 223 + tid;
    ln = ln < 56960 ? ln : 56959;
    const char* p = ln < 20480 ? x + ln * 128 : (ln < 39680 ? wqkv + (ln - 20480) * 128 : w1 + (ln - 39680) * 128);
    asm volatile("global_load_dword %0, %1, off" : "=v"(warm) : "v"(p) : "memory");
  }
  if (tid < 128) sts128(L_ZERO + tid * 16, (u32x4){0u, 0u, 0u, 0u});
  if (tid == 128) sts128(L_ONE, (u32x4){0x3f803f80u, 0x3f803f80u, 0x3f803f80u, 0x3f803f80u});
  u64 epoch;
  asm volatile("s_load_dwordx2 %0, %1, 0x0\n\ts_waitcnt lgkmcnt(0)" : "=s"(epoch) : "s"(ws) : "memory");
  const u64 tag = epoch * 6364136223846793005ull + 1442695040888963407ull;
  const unsigned tlo = (unsigned)tag, thi = (unsigned)(tag >> 32);
  const u32x2 tagv = {tlo, thi};

  unsigned xa = L_S0 + (32 * mw + c16) * 128 + ((g ^ ((c16 >> 1) & 7)) << 4);
  unsigned wa[8];
  {
    const unsigned wrd = L_S0 + 16384 + (4 * g + q4) * 480 + (isq ? 40 : 8) * p4;
#pragma unroll
    for (int t = 0; t < 8; ++t) {
      const int tq = nw == 0 ? (t < 5 ? 160 + 8 * t : 320 + 8 * (t - 5)) : (t < 5 ? 8 * t : 320 + 8 * (t - 2));
      const int th = (nw == 0 ? 0 : (jg < 6 ? 256 : 192)) + 32 * t;
      wa[t] = wrd + (isq ? tq : th);
    }
  }
  const bool gatew = !isq && jg == 7 && nw == 1;
  const int nt = isq ? 8 - nw : ((nw == 0 && jg < 6) ? 8 : (gatew ? 7 : 6));
  unsigned ga = L_GATE + g * 16;

  f32x4 acc[8][2];
#pragma unroll
  for (int t = 0; t < 8; ++t) { acc[t][0] = (f32x4){0.f, 0.f, 0.f, 0.f}; acc[t][1] = (f32x4){0.f, 0.f, 0.f, 0.f}; }
  u32x4 W[8], X0[2], X1[2];
#define GROUP(i, X) { acc[i][0] = mfma(W[i], X[0], acc[i][0]); acc[i][1] = mfma(W[i], X[1], acc[i][1]); }
#define RD(i, off) W[i] = cat(ldstr(wa[i] + (off)), ldstr(wa[i] + (off) + 7680))
#define RD6(off) { if (gatew) { W[6] = lds128(ga); ga += 64; } else { RD(6, off); } }
  STAMP(1);
  BAR();
  X0[0] = lds128(xa); X0[1] = lds128(xa + 2048);
#pragma unroll
  for (int i = 0; i < 6; ++i) RD(i, 0);
  RD6(0);
  RD(7, 0);
  SB();
  unsigned sb = L_S0;
#pragma unroll 1
  for (int t = 0; t < 10; ++t) {
    X1[0] = lds128(xa ^ 64); X1[1] = lds128((xa ^ 64) + 2048);
    SB();
#pragma unroll
    for (int i = 0; i < 6; ++i) { GROUP(i, X0); RD(i, 15360); SB(); }
    if (nt > 6) {
      GROUP(6, X0); RD6(15360); SB();
      if (nt > 7) { GROUP(7, X0); RD(7, 15360); SB(); }
    }
    GROUP(0, X1); GROUP(1, X1);
    SB();
    BAR();
    const unsigned sn = sb == L_S0 ? L_S1 : sb + SLOT;
    const unsigned dl = sn - sb;
    sb = sn;
    GROUP(2, X1);
    xa += dl;
    X0[0] = lds128(xa); X0[1] = lds128(xa + 2048);
    wa[0] += dl; RD(0, 0);
    SB();
    GROUP(3, X1);
    wa[1] += dl; RD(1, 0);
    wa[2] += dl; RD(2, 0);
    SB();
    GROUP(4, X1);
    wa[3] += dl; RD(3, 0);
    wa[4] += dl; RD(4, 0);
    SB();
    GROUP(5, X1);
    wa[5] += dl; RD(5, 0);
    SB();
    if (nt > 6) {
      GROUP(6, X1); wa[6] += dl; RD6(0); SB();
      if (nt > 7) { GROUP(7, X1); wa[7] += dl; RD(7, 0); SB(); }
    }
  }
  STAMP(2);
  asm volatile("s_waitcnt vmcnt(0)" : "+v"(warm)::"memory");

  const unsigned flagoff = (rt * 2 + (mw >> 1)) * 256 + (jg * 4 + nw * 2 + (mw & 1)) * 8;
  unsigned kq[10];
  const unsigned pbase = (p4 == g) ? L_WPHI + g * 800 + q4 * 40 : L_ZERO;
  if (!isq) {
    const int k0 = (hstart >> 6) + (nw == 0 ? 0 : (jg < 6 ? 4 : 3));
    char* abase = ws + WS_A + ((rt * 8 + 2 * mw) * 74 + k0) * 1024;
#pragma unroll
    for (int pr = 0; pr < 4; ++pr) {
      if (pr < 3 || nt > 7) {
#pragma unroll
        for (int mt = 0; mt < 2; ++mt) {
          const f32x4 a = acc[2 * pr][mt], c = acc[2 * pr + 1][mt];
          u32x4 f = {pk2(silu(a[0]), silu(a[1])), pk2(silu(a[2]), silu(a[3])), pk2(silu(c[0]), silu(c[1])), pk2(silu(c[2]), silu(c[3]))};
          gst128_sc1(boff, abase + (mt * 74 + pr) * 1024, f);
        }
      }
    }
    if (gatew && g == 0) {
#pragma unroll
      for (int mt = 0; mt < 2; ++mt) {
        const float gv = __builtin_amdgcn_rcpf(1.f + __builtin_amdgcn_exp2f(acc[6][mt][0] * -1.4426950408889634f));
        gst32_sc1((rt * 128 + 32 * mw + 16 * mt + c16) * 4, ws + WS_G, __builtin_bit_cast(unsigned, gv));
      }
    }
    STAMP(3);
    WAITV(0);
    if (lane == 0) gst64_sc1(flagoff, ws + WS_HF, tagv);
    STAMP(4);
#pragma unroll
    for (int i = 0; i < 10; ++i) kq[i] = 0;
  } else {
    unsigned mine[10], other[10];
#pragma unroll
    for (int t = 0; t < 5; ++t) {
      const f32x4 a = nw == 0 ? acc[t][0] : acc[t][1], c = nw == 0 ? acc[t][1] : acc[t][0];
      mine[2 * t] = pk2(a[0], a[1]); mine[2 * t + 1] = pk2(a[2], a[3]);
      other[2 * t] = pk2(c[0], c[1]); other[2 * t + 1] = pk2(c[2], c[3]);
    }
    const unsigned myrows = L_PHIK + (32 * mw + 16 * nw) * 352, prows = L_PHIK + (32 * mw + 16 * (nw ^ 1)) * 352;
    sts128(prows + boff, (u32x4){other[0], other[1], other[2], other[3]});
    sts128(prows + 1024 + boff, (u32x4){other[4], other[5], other[6], other[7]});
    sts64(prows + 2048 + lane * 8, (u32x2){other[8], other[9]});
#pragma unroll
    for (int i = 5; i < 8; ++i) {
      if (i < 7 || nw == 0) {
        const int tt = nw == 0 ? i - 5 : i - 2;
#pragma unroll
        for (int mt = 0; mt < 2; ++mt) {
          const f32x4 a = acc[i][mt];
          sts64(L_V + (32 * mw + 16 * mt + c16) * 160 + 40 * g + 8 * tt, (u32x2){pk2(a[0], a[1]), pk2(a[2], a[3])});
        }
      }
    }
    BAR();
    {
      const u32x4 r0 = lds128(myrows + boff), r1 = lds128(myrows + 1024 + boff);
      const u32x2 r2 = lds64(myrows + 2048 + lane * 8);
      other[0] = r0[0]; other[1] = r0[1]; other[2] = r0[2]; other[3] = r0[3];
      other[4] = r1[0]; other[5] = r1[1]; other[6] = r1[2]; other[7] = r1[3];
      other[8] = r2[0]; other[9] = r2[1];
    }
    unsigned kk[10];
#pragma unroll
    for (int i = 0; i < 10; ++i) { kk[i] = nw == 0 ? mine[i] : other[i]; kq[i] = nw == 0 ? other[i] : mine[i]; }
    unsigned phi[20];
    phi_unit(kk, pbase, phi);
#pragma unroll
    for (int i = 0; i < 5; ++i)
      sts128(myrows + c16 * 352 + 80 * g + 16 * i, (u32x4){phi[4 * i], phi[4 * i + 1], phi[4 * i + 2], phi[4 * i + 3]});
    BAR();
    STAMP(3);
    const unsigned ab = L_PHIK + (4 * g + q4) * 352 + 80 * mw + 8 * p4;
    const unsigned ab2 = L_PHIK + (4 * g + q4) * 352 + 80 * mw + 64 + 8 * (p4 & 1);
    const bool vreal = nw == 0 || p4 == 0;
    const unsigned vb = vreal ? L_V + (4 * g + q4) * 160 + 40 * mw + 32 * nw + 8 * p4 : L_ONE;
    const unsigned vs = vreal ? 16 * 160 : 0;
    u32x4 vf[4], pf[4][3];
#pragma unroll
    for (int ks = 0; ks < 4; ++ks) {
      const unsigned o = ks * 32 * 352;
      vf[ks] = cat(ldstr(vb + (2 * ks) * vs), ldstr(vb + (2 * ks + 1) * vs));
      pf[ks][0] = cat(ldstr(ab + o), ldstr(ab + o + 16 * 352));
      pf[ks][1] = cat(ldstr(ab + o + 32), ldstr(ab + o + 32 + 16 * 352));
      pf[ks][2] = cat(ldstr(ab2 + o), ldstr(ab2 + o + 16 * 352));
    }
    SB();
    f32x4 kv[3];
#pragma unroll
    for (int i = 0; i < 3; ++i) kv[i] = (f32x4){0.f, 0.f, 0.f, 0.f};
#pragma unroll
    for (int ks = 0; ks < 4; ++ks)
#pragma unroll
      for (int i = 0; i < 3; ++i) kv[i] = mfma(pf[ks][i], vf[ks], kv[i]);
    char* pb = ws + WS_PART + (jg * 16 + rt) * GR_BYTES;
    const unsigned po = (mw * 210 + (16 * nw + c16) * 10 + g) * 16;
    if (nw == 0 || c16 < 5) {
      gst128_sc1(po, pb, (u32x4){pk2(kv[0][0], kv[0][1]), tlo, pk2(kv[0][2], kv[0][3]), thi});
      gst128_sc1(po + 64, pb, (u32x4){pk2(kv[1][0], kv[1][1]), tlo, pk2(kv[1][2], kv[1][3]), thi});
      if (g < 2) gst128_sc1(po + 128, pb, (u32x4){pk2(kv[2][0], kv[2][1]), tlo, pk2(kv[2][2], kv[2][3]), thi});
    }
    STAMP(4);
  }

  BAR();
  STAMP(5);
  {
    const char* asrc = ws + WS_A + ((rt64 * 4 + mw) * 74 + nw) * 1024;
    u32x4 af[37];
#define LDA(a) af[a] = gld128_sc1(boff, asrc + (a) * 2048)
#define LDA4(a) LDA(a); LDA(a + 1); LDA(a + 2); LDA(a + 3)
    LDA4(0); LDA4(4); LDA4(8);
    unsigned phq[20];
    if (isq) phi_unit(kq, pbase, phq);
    else {
#pragma unroll
      for (int i = 0; i < 20; ++i) phq[i] = 0;
    }
    STAMP(6);
    f32x4 ob[5], oa[5];
#pragma unroll
    for (int n = 0; n < 5; ++n) { ob[n] = (f32x4){0.f, 0.f, 0.f, 0.f}; oa[n] = (f32x4){0.f, 0.f, 0.f, 0.f}; }
    const unsigned brd = nw * 5120 + boff;
    u32x4 b0[5], b1[5];
#define RDB(B, SL, J) { _Pragma("unroll") for (int n = 0; n < 5; ++n) B[n] = lds128(SL + brd + (J) * 10240 + n * 1024); SB(); }
#define MM(O, B, A) { PIN(af[A]); _Pragma("unroll") for (int n = 0; n < 5; ++n) O[n] = mfma(B[n], af[A], O[n]); SB(); }
#define STAGE4(O, SL, A, WT) RDB(b0, SL, 0) RDB(b1, SL, 1) WT; MM(O, b0, A) RDB(b0, SL, 2) MM(O, b1, A + 1) RDB(b1, SL, 3) MM(O, b0, A + 2) MM(O, b1, A + 3)
    STAGE4(ob, L_S1, 0, WAITV(8)) LDA4(12);
    BAR(); STAGE4(ob, L_S2, 4, WAITV(8)) LDA4(16);
    BAR(); STAGE4(ob, L_S0, 8, WAITV(8)) LDA4(20);
    BAR(); STAGE4(ob, L_S1, 12, WAITV(8)) LDA(24); LDA(25); LDA(26);
    STAMP(7);
    BAR();
    WAITV(7);
    if (isq) {
      const unsigned kb = (q4 == g) ? L_KV + g * 1680 + p4 * 80 : L_ZERO;
      const unsigned zb = (q4 == g) ? L_KV + g * 1680 + 1600 : L_ZERO;
      f32x4 o[6];
#pragma unroll
      for (int t = 0; t < 6; ++t) o[t] = (f32x4){0.f, 0.f, 0.f, 0.f};
      u32x4 ka[2][6];
#pragma unroll
      for (int t = 0; t < 6; ++t) ka[0][t] = t < 5 ? lds128(kb + t * 320) : lds128(zb);
#pragma unroll
      for (int s = 0; s < 5; ++s) {
        if (s < 4) {
#pragma unroll
          for (int t = 0; t < 6; ++t) ka[(s + 1) & 1][t] = t < 5 ? lds128(kb + t * 320 + (s + 1) * 16) : lds128(zb + (s + 1) * 16);
        }
        SB();
        const u32x4 bq = {phq[4 * s], phq[4 * s + 1], phq[4 * s + 2], phq[4 * s + 3]};
#pragma unroll
        for (int t = 0; t < 6; ++t) o[t] = mfma(ka[s & 1][t], bq, o[t]);
        SB();
      }
      char* abase = ws + WS_A + ((rt * 8 + 2 * mw + nw) * 74 + 54 + (jg >> 1) * 5) * 1024;
      const float sc = __builtin_amdgcn_rcpf(o[5][0] + 1e-6f);
      unsigned r[10];
#pragma unroll
      for (int t = 0; t < 5; ++t) { r[2 * t] = pk2(o[t][0] * sc, o[t][1] * sc); r[2 * t + 1] = pk2(o[t][2] * sc, o[t][3] * sc); }
      gst128_sc1(boff, abase + (2 * (jg & 1)) * 1024, (u32x4){r[0], r[1], r[2], r[3]});
      gst128_sc1(boff, abase + (2 * (jg & 1) + 1) * 1024, (u32x4){r[4], r[5], r[6], r[7]});
      gst64_sc1(boff + 8 * (jg & 1), abase + 4 * 1024, (u32x2){r[8], r[9]});
    }
    STAGE4(ob, L_S2, 16, )
    if (isq) {
      WAITV(0);
      if (lane == 0) gst64_sc1(flagoff, ws + WS_OF, tagv);
    }
    STAMP(8);
    BAR(); STAGE4(ob, L_S0, 20, WAITV(3))
    BAR();
    STAMP(9);
    unsigned gbits = gld32_sc1((rt64 * 64 + mw * 16 + c16) * 4, ws + WS_G);
    LDA4(27); LDA4(31); LDA(35); LDA(36);
    RDB(b0, L_S1, 0) RDB(b1, L_S1, 1) WAITV(11); MM(ob, b0, 24) RDB(b0, L_S1, 2) MM(ob, b1, 25) MM(ob, b0, 26)
    BAR(); STAGE4(oa, L_S2, 27, WAITV(6))
    BAR(); STAGE4(oa, L_S0, 31, WAITV(2))
    BAR(); RDB(b0, L_S1, 0) RDB(b1, L_S1, 1) WAITV(0); MM(oa, b0, 35) MM(oa, b1, 36)
    STAMP(10);
    PIN(gbits);
    const float gv = __builtin_bit_cast(float, gbits), omg = 1.f - gv;
#pragma unroll
    for (int n = 0; n < 5; ++n) ob[n] = ob[n] * omg + oa[n] * gv;
    const unsigned rb = L_S2 + mw * 5120 + boff;
    if (nw == 1) {
#pragma unroll
      for (int n = 0; n < 3; ++n) sts128(rb + n * 1024, __builtin_bit_cast(u32x4, ob[n]));
    } else {
#pragma unroll
      for (int n = 3; n < 5; ++n) sts128(rb + n * 1024, __builtin_bit_cast(u32x4, ob[n]));
    }
    BAR();
    const unsigned oo = (rt64 * 64 + mw * 16 + c16) * 1280 + ct * 160 + g * 8;
    if (nw == 0) {
#pragma unroll
      for (int n = 0; n < 3; ++n) {
        const f32x4 r = ob[n] + __builtin_bit_cast(f32x4, lds128(rb + n * 1024));
        gst64(oo + n * 32, out, (u32x2){pk2(r[0], r[1]), pk2(r[2], r[3])});
      }
    } else {
#pragma unroll
      for (int n = 3; n < 5; ++n) {
        const f32x4 r = ob[n] + __builtin_bit_cast(f32x4, lds128(rb + n * 1024));
        gst64(oo + n * 32, out, (u32x2){pk2(r[0], r[1]), pk2(r[2], r[3])});
      }
    }
  }
}

extern "C" void kernel_launch(void* const* d_in, const int* in_sizes, int n_in, void* d_out, int out_size, void* d_ws,
                              size_t ws_size, hipStream_t stream) {
  (void)in_sizes; (void)n_in; (void)out_size; (void)ws_size;
  (void)hipFuncSetAttribute((const void*)fused_kernel, hipFuncAttributeMaxDynamicSharedMemorySize, LDS_BYTES);
  fused_kernel<<<256, 768, LDS_BYTES, stream>>>((const char*)d_in[0], (const char*)d_in[1], (const char*)d_in[2],
                                                (const char*)d_in[3], (const char*)d_in[5], (const char*)d_in[6],
                                                (const char*)d_in[7], (char*)d_out, (char*)d_ws);
}
```

```cpp
#include <hip/hip_runtime.h>
#include <stdint.h>
#include <stdio.h>

typedef __attribute__((ext_vector_type(8))) __bf16 bf16x8;
typedef __attribute__((ext_vector_type(2))) __bf16 bf16x2;
typedef __attribute__((ext_vector_type(4))) short s16x4;
typedef __attribute__((ext_vector_type(4))) float f32x4;
typedef __attribute__((ext_vector_type(2))) float f32x2;
typedef __attribute__((ext_vector_type(4))) unsigned u32x4;
typedef __attribute__((ext_vector_type(2))) unsigned u32x2;
typedef unsigned long long u64;
#define LAS __attribute__((address_space(3)))
#define DI __device__ __forceinline__

constexpr unsigned WS_EPOCH = 0;
constexpr unsigned WS_RT = 1024;
constexpr unsigned WS_HF = 4096;
constexpr unsigned WS_OF = 12288;
constexpr unsigned WS_G = 20480;
constexpr unsigned WS_RED = 32768;
constexpr unsigned GR_BYTES = 840 * 16;
constexpr unsigned WS_PART = 32768 + 8 * GR_BYTES;
constexpr unsigned WS_B = 2u << 20;
constexpr unsigned WS_A = 5u << 20;
constexpr unsigned WS_PROF = 24u << 20;
static_assert(WS_PART + 128 * GR_BYTES <= WS_B, "ws");

constexpr unsigned SLOT = 47104;
constexpr unsigned L_S1 = 0, L_S2 = SLOT, L_S0 = 2 * SLOT, L_MISC = 3 * SLOT;
constexpr unsigned L_PHIK = L_S2 + 40960;
constexpr unsigned L_V = L_PHIK + 128 * 352;
constexpr unsigned L_KV = L_MISC;
constexpr unsigned L_WPHI = 153600;
constexpr unsigned L_GATE = L_WPHI;
constexpr unsigned L_ZERO = L_WPHI + 4096;
constexpr unsigned L_ONE = L_ZERO + 2048;
constexpr unsigned LDS_BYTES = 163840;
static_assert(L_ONE + 16 <= LDS_BYTES, "lds");

constexpr unsigned SPIN_CAP = 1u << 16;

DI unsigned pk2(float a, float b) {
  bf16x2 v = __builtin_convertvector((f32x2){a, b}, bf16x2);
  return __builtin_bit_cast(unsigned, v);
}
DI f32x4 mfma(u32x4 a, u32x4 b, f32x4 c) {
  return __builtin_amdgcn_mfma_f32_16x16x32_bf16(__builtin_bit_cast(bf16x8, a), __builtin_bit_cast(bf16x8, b), c, 0, 0, 0);
}
DI u32x4 lds128(unsigned a) { return *(LAS u32x4*)(size_t)a; }
DI u32x2 lds64(unsigned a) { return *(LAS u32x2*)(size_t)a; }
DI void sts128(unsigned a, u32x4 v) { *(LAS u32x4*)(size_t)a = v; }
DI void sts64(unsigned a, u32x2 v) { *(LAS u32x2*)(size_t)a = v; }
DI u32x2 ldstr(unsigned a) {
  return __builtin_bit_cast(u32x2, __builtin_amdgcn_ds_read_tr16_b64_v4i16((LAS s16x4*)(size_t)a));
}
DI u32x4 cat(u32x2 a, u32x2 b) { return (u32x4){a[0], a[1], b[0], b[1]}; }
DI void glds(unsigned voff, const void* sbase, unsigned lds) {
  asm volatile("s_mov_b32 m0, %2\n\ts_nop 0\n\tglobal_load_lds_dwordx4 %0, %1" ::"v"(voff), "s"(sbase), "s"(lds) : "memory");
}
DI void glds_m(unsigned voff, const void* sbase, unsigned lds, u64 mask) {
  asm volatile("s_mov_b32 m0, %2\n\ts_mov_b64 exec, %3\n\tglobal_load_lds_dwordx4 %0, %1\n\ts_mov_b64 exec, -1" ::"v"(voff), "s"(sbase), "s"(lds), "s"(mask) : "memory");
}
DI void glds5_sc1(unsigned voff, unsigned voff1k, const void* sbase, unsigned lds) {
  asm volatile(
      "s_mov_b32 m0, %2\n\ts_nop 0\n\t"
      "global_load_lds_dwordx4 %0, %1 sc1\n\t"
      "global_load_lds_dwordx4 %0, %1 offset:1024 sc1\n\t"
      "global_load_lds_dwordx4 %0, %1 offset:2048 sc1\n\t"
      "global_load_lds_dwordx4 %0, %1 offset:3072 sc1\n\t"
      "s_add_u32 m0, m0, 1024\n\ts_nop 0\n\t"
      "global_load_lds_dwordx4 %3, %1 offset:3072 sc1" ::"v"(voff), "s"(sbase), "s"(lds), "v"(voff1k)
      : "memory", "scc");
}
DI u32x4 gld128_sc1(unsigned voff, const void* sbase) {
  u32x4 r;
  asm volatile("global_load_dwordx4 %0, %1, %2 sc1" : "=v"(r) : "v"(voff), "s"(sbase) : "memory");
  return r;
}
DI u32x2 gld64_sc1(unsigned voff, const void* sbase) {
  u32x2 r;
  asm volatile("global_load_dwordx2 %0, %1, %2 sc1" : "=v"(r) : "v"(voff), "s"(sbase) : "memory");
  return r;
}
DI unsigned gld32_sc1(unsigned voff, const void* sbase) {
  unsigned r;
  asm volatile("global_load_dword %0, %1, %2 sc1" : "=v"(r) : "v"(voff), "s"(sbase) : "memory");
  return r;
}
DI unsigned gld32(unsigned voff, const void* sbase) {
  unsigned r;
  asm volatile("global_load_dword %0, %1, %2" : "=v"(r) : "v"(voff), "s"(sbase) : "memory");
  return r;
}
DI void gst128_sc1(unsigned voff, void* sbase, u32x4 v) {
  asm volatile("global_store_dwordx4 %0, %1, %2 sc1\n\ts_nop 1" ::"v"(voff), "v"(v), "s"(sbase) : "memory");
}
DI void gst64_sc1(unsigned voff, void* sbase, u32x2 v) {
  asm volatile("global_store_dwordx2 %0, %1, %2 sc1" ::"v"(voff), "v"(v), "s"(sbase) : "memory");
}
DI void gst32_sc1(unsigned voff, void* sbase, unsigned v) {
  asm volatile("global_store_dword %0, %1, %2 sc1" ::"v"(voff), "v"(v), "s"(sbase) : "memory");
}
DI void gst64(unsigned voff, void* sbase, u32x2 v) {
  asm volatile("global_store_dwordx2 %0, %1, %2 sc1" ::"v"(voff), "v"(v), "s"(sbase) : "memory");
}
#define WAITV(n) asm volatile("s_waitcnt vmcnt(" #n ")" ::: "memory")
#define BAR() asm volatile("s_waitcnt lgkmcnt(0)\n\ts_barrier" ::: "memory")
#define PIN(x) asm volatile("" : "+v"(x))
#define SB() __builtin_amdgcn_sched_barrier(0)

DI void poll_words(const char* base, unsigned voff, unsigned tlo, unsigned thi) {
#pragma unroll 1
  for (unsigned spins = 0; spins < SPIN_CAP; ++spins) {
    u32x2 v = gld64_sc1(voff, base);
    asm volatile("s_waitcnt vmcnt(0)" : "+v"(v)::"memory");
    if (__all(v[0] == tlo && v[1] == thi)) break;
    __builtin_amdgcn_s_sleep(1);
  }
}

DI void dual_softmax(const f32x4 (&p)[5], unsigned (&o)[20]) {
  float mx = p[0][0], mn = p[0][0];
#pragma unroll
  for (int i = 1; i < 20; ++i) { mx = fmaxf(mx, p[i >> 2][i & 3]); mn = fminf(mn, p[i >> 2][i & 3]); }
  const float L = 1.4426950408889634f;
  const float mxl = -mx * L, mnl = mn * L;
  float ep[20], en[20], sp = 0.f, sn = 0.f;
#pragma unroll
  for (int i = 0; i < 20; ++i) {
    float v = p[i >> 2][i & 3];
    ep[i] = __builtin_amdgcn_exp2f(__builtin_fmaf(v, L, mxl));
    en[i] = __builtin_amdgcn_exp2f(__builtin_fmaf(v, -L, mnl));
    sp += ep[i]; sn += en[i];
  }
  const float rp = __builtin_amdgcn_rcpf(sp), rn = __builtin_amdgcn_rcpf(sn);
#pragma unroll
  for (int i = 0; i < 10; ++i) {
    o[i] = pk2(ep[2 * i] * rp, ep[2 * i + 1] * rp);
    o[10 + i] = pk2(en[2 * i] * rn, en[2 * i + 1] * rn);
  }
}
DI float silu(float v) { return v * __builtin_amdgcn_rcpf(1.f + __builtin_amdgcn_exp2f(v * -1.4426950408889634f)); }

DI void phi_unit(const unsigned (&kq)[10], unsigned pbase, unsigned (&phi)[20]) {
  const u32x4 f0 = {kq[0], kq[1], kq[2], kq[3]}, f1 = {kq[4], kq[5], kq[6], kq[7]}, f2 = {kq[8], kq[9], 0u, 0u};
  u32x4 a0[5], a1[5], a2[5];
#pragma unroll
  for (int t = 0; t < 5; ++t) {
    a0[t] = cat(ldstr(pbase + 8 * t), ldstr(pbase + 8 * t + 160));
    a1[t] = cat(ldstr(pbase + 8 * t + 320), ldstr(pbase + 8 * t + 480));
    a2[t] = cat(ldstr(pbase + 8 * t + 640), (u32x2){0u, 0u});
  }
  SB();
  f32x4 pp[5];
#pragma unroll
  for (int t = 0; t < 5; ++t) {
    f32x4 c = {0.f, 0.f, 0.f, 0.f};
    c = mfma(a0[t], f0, c);
    c = mfma(a1[t], f1, c);
    pp[t] = mfma(a2[t], f2, c);
  }
  dual_softmax(pp, phi);
}

#define STAMP(i)

__global__ __launch_bounds__(768, 3) void fused_kernel(const char* __restrict__ x, const char* __restrict__ wqkv,
                                                       const char* __restrict__ wphi, const char* __restrict__ wo,
                                                       const char* __restrict__ w1, const char* __restrict__ w2,
                                                       const char* __restrict__ gate, char* out, char* ws) {
  extern __shared__ __attribute__((aligned(16))) char smem[];
  STAMP(0);
  const u64 tstart = __builtin_amdgcn_s_memrealtime();
  const int tid = threadIdx.x, lane = tid & 63;
  const int w = __builtin_amdgcn_readfirstlane(tid >> 6);
  const int b = blockIdx.x, xcd = b & 7, idx = b >> 3;
  const int rt = (xcd & 1) * 8 + (idx & 7);
  const int cb = idx >> 3, cq = xcd >> 1;
  const bool isq = cb < 2;
  const int jg = 2 * cq + (cb & 1);
  const int rt64 = (xcd >> 1) * 8 + (idx >> 2), ct = (xcd & 1) * 4 + (idx & 3);
  const int g = lane >> 4, c16 = lane & 15, q4 = c16 >> 2, p4 = lane & 3;
  const int hstart = jg * 448 - (jg == 7 ? 64 : 0);
  const unsigned boff = lane * 16;

  if (w >= 8) {
    const int L = w - 8;
    const int ldw = isq ? 3840 : 3456;
    unsigned xo[4], wv[8];
    {
      const unsigned x0 = (rt * 128 + 8 * L + (lane >> 3)) * 1280 + (((lane & 7) ^ ((4 * L + (lane >> 4)) & 7)) << 4);
#pragma unroll
      for (int i = 0; i < 4; ++i) xo[i] = x0 + i * 32 * 1280;
      const int rs = lane >= 30 ? 1 : 0, pos = lane - 30 * rs;
      const int sec = (pos >= 10 ? 1 : 0) + (pos >= 20 ? 1 : 0);
      const int col = isq ? sec * 1120 + jg * 160 + pos * 16 : hstart + pos * 16;
      const unsigned w0 = (8 * (L >> 1) + 2 * (L & 1) + rs) * ldw + col;
#pragma unroll
      for (int i = 0; i < 8; ++i) wv[i] = w0 + (32 * (i >> 2) + 16 * (i & 1) + 4 * ((i >> 1) & 1)) * ldw;
    }
    const u64 half = isq ? 0x3fffffffull : (jg < 6 ? 0x0fffffffull : 0x00ffffffull);
    const u64 wmask = half | (half << 30);
    const char* xs = x;
    const char* wsrc = isq ? wqkv : w1;
    auto issue_stage = [&](unsigned sb) {
#pragma unroll
      for (int i = 0; i < 4; ++i) {
        glds(xo[i], xs, sb + (L + 4 * i) * 1024);
        glds_m(wv[2 * i], wsrc, sb + 16384 + (L + 8 * i) * 960, wmask);
        glds_m(wv[2 * i + 1], wsrc, sb + 16384 + (L + 8 * i + 4) * 960, wmask);
      }
      xs += 128;
      wsrc += 64 * ldw;
    };
    const int unit = b * 3 + L;
    const bool retile = (L < 3) && (unit < 740);
    const int rk = unit / 10, rcu = unit - rk * 10;
    const char* bsrc = ws + WS_B + ct * (74 * 5120);
    const unsigned boff1k = boff + 1024;
    auto issue_b = [&](int k0, int nk, unsigned sl) {
#pragma unroll 1
      for (int j = 0; j < nk; ++j) glds5_sc1(boff, boff1k, bsrc + (k0 + L * nk + j) * 5120, sl + (L * nk + j) * 5120);
    };
    unsigned tlo = 0, thi = 0;
#define LSTAMP()
    LSTAMP();
    u32x4 rtf0 = {0u, 0u, 0u, 0u}, rtf1 = {0u, 0u, 0u, 0u};
    unsigned sb = L_S0;
#pragma unroll 1
    for (int t = -3; t < 7; ++t) {
      if (t >= 0) {
        if (retile && t < 2) { WAITV(16); } else { WAITV(12); }
        LSTAMP();
        BAR();
        LSTAMP();
      }
      issue_stage(sb);
      LSTAMP();
      sb = sb == L_S0 ? L_S1 : sb + SLOT;
      if (t == -3) {
        if (retile) {
          const char* src = rk < 54 ? w2 : wo;
          const int kp = rk - 54, P = kp / 5, s5 = kp - P * 5;
#pragma unroll
          for (int n = 0; n < 4; ++n) {
            const int rho = 8 * n + (lane >> 3), c = lane & 7;
            const int tt = n >> 1, gg = (rho >> 2) & 3, e = rho & 3;
            const int r0 = rk < 54 ? 32 * rk + 16 * tt : (s5 < 4 ? 80 * (2 * P + (s5 >> 1)) + 8 * (s5 & 1) + 4 * tt : 80 * (2 * P + tt) + 16);
            const int row = r0 + gg * (rk < 54 ? 4 : 20) + e;
            const int sc = (((c >> 1) ^ ((rho >> 1) & 3)) << 1) | (c & 1);
            glds(row * 1280 + rcu * 128 + sc * 16, src, L_MISC + L * 4096 + n * 1024);
          }
        } else {
          const char* src = isq ? wphi + jg * 3200 : gate;
          const unsigned lim = isq ? 3184 : 1264;
#pragma unroll
          for (int n = 0; n < 4; ++n) {
            const unsigned o = n * 1024 + boff;
            glds(o < lim ? o : lim, src, (L == 3 ? L_WPHI : L_MISC + L * 4096) + n * 1024);
          }
        }
      } else if (t == -1) {
        u64 epoch;
        asm volatile("s_load_dwordx2 %0, %1, 0x0\n\ts_waitcnt lgkmcnt(0)" : "=s"(epoch) : "s"(ws) : "memory");
        const u64 tag = epoch * 6364136223846793005ull + 1442695040888963407ull;
        tlo = (unsigned)tag; thi = (unsigned)(tag >> 32);
        LSTAMP();
        WAITV(24);
        LSTAMP();
        BAR();
        LSTAMP();
        if (retile) {
          const unsigned base = L_MISC + L * 4096 + (4 * g + q4) * 128 + 8 * p4;
          const int sw = (2 * g + (q4 >> 1)) & 3;
          u32x4 f[4];
#pragma unroll
          for (int n = 0; n < 4; ++n) { const unsigned a = base + ((n ^ sw) << 5); f[n] = cat(ldstr(a), ldstr(a + 2048)); }
#pragma unroll
          for (int n = 0; n < 4; ++n) {
            const int N = 4 * rcu + n, c5 = N / 5;
            gst128_sc1(boff, ws + WS_B + ((c5 * 74 + rk) * 5 + (N - c5 * 5)) * 1024, f[n]);
          }
        }
      } else if (t == 2) {
        if (L == 3 && lane == 0) gst64_sc1(b * 8, ws + WS_RT, (u32x2){tlo, thi});
      } else if (t == 5) {
        if (L == 2) {
          asm volatile("global_load_dwordx4 %0, %2, %3 sc1\n\tglobal_load_dwordx4 %1, %2, %3 offset:16 sc1"
                       : "+v"(rtf0), "+v"(rtf1) : "v"(lane * 32), "s"(ws + WS_RT) : "memory");
        }
      }
    }
    const u32x2 tagv = {tlo, thi};
    WAITV(12);
    if (L == 2) {
      asm volatile("" : "+v"(rtf0), "+v"(rtf1));
      const unsigned bad = (rtf0[0] ^ tlo) | (rtf0[1] ^ thi) | (rtf0[2] ^ tlo) | (rtf0[3] ^ thi) | (rtf1[0] ^ tlo) | (rtf1[1] ^ thi) |
                           (rtf1[2] ^ tlo) | (rtf1[3] ^ thi);
      if (!__all(bad == 0)) {
#pragma unroll 1
        for (int i = 0; i < 4; ++i) poll_words(ws + WS_RT + i * 512, lane * 8, tlo, thi);
      }
    }
    BAR();
    if (L < 2) { issue_b(0, 4, L_S1); WAITV(20); } else { WAITV(0); }
    if (b == 0 && L == 2 && lane == 0) gst64_sc1(0, ws + WS_EPOCH, tagv);
    BAR();
    if (L < 2) issue_b(8, 4, L_S2);
    BAR();
    if (isq) { BAR(); BAR(); }
    else if (L < 2) issue_b(16, 4, L_S0);
    if (L == 2) poll_words(ws + WS_HF + rt64 * 256, (lane & 31) * 8, tlo, thi);
    if (L < 2) { if (isq) { WAITV(0); } else { WAITV(20); } }
    BAR();
    if (L < 2) {
      if (isq) issue_b(16, 4, L_S0);
      BAR(); issue_b(24, 4, L_S1); WAITV(20);
      BAR(); issue_b(32, 4, L_S2); WAITV(20);
      BAR(); issue_b(40, 4, L_S0); WAITV(20);
      BAR(); issue_b(48, 3, L_S1); WAITV(15);
      BAR(); issue_b(54, 4, L_S2); WAITV(20);
      BAR(); issue_b(62, 4, L_S0); WAITV(20);
      BAR(); issue_b(70, 2, L_S1); WAITV(10);
      BAR(); WAITV(0);
      BAR();
    } else if (L == 2) {
      BAR(); BAR(); BAR(); BAR(); BAR();
      poll_words(ws + WS_OF + rt64 * 256, (lane & 31) * 8, tlo, thi);
      BAR();
    } else if (isq) {
      const unsigned l60 = lane < 60 ? lane : 59;
      if (rt < 14) {
        const char* pb = ws + WS_PART + jg * 16 * GR_BYTES + rt * 960;
        u32x4 pv[16];
#pragma unroll 1
        for (unsigned spins = 0; spins < SPIN_CAP; ++spins) {
#pragma unroll
          for (int i = 0; i < 16; ++i) pv[i] = gld128_sc1(l60 * 16, pb + i * GR_BYTES);
          asm volatile("s_waitcnt vmcnt(0)"
                       : "+v"(pv[0]), "+v"(pv[1]), "+v"(pv[2]), "+v"(pv[3]), "+v"(pv[4]), "+v"(pv[5]), "+v"(pv[6]), "+v"(pv[7]),
                         "+v"(pv[8]), "+v"(pv[9]), "+v"(pv[10]), "+v"(pv[11]), "+v"(pv[12]), "+v"(pv[13]), "+v"(pv[14]), "+v"(pv[15])::"memory");
          unsigned bad = 0;
#pragma unroll
          for (int i = 0; i < 16; ++i) bad |= (pv[i][1] ^ tlo) | (pv[i][3] ^ thi);
          if (__all(bad == 0)) break;
        }
        float s[4] = {0.f, 0.f, 0.f, 0.f};
#pragma unroll
        for (int i = 0; i < 16; ++i) {
          s[0] += __builtin_bit_cast(float, pv[i][0] << 16);
          s[1] += __builtin_bit_cast(float, pv[i][0] & 0xffff0000u);
          s[2] += __builtin_bit_cast(float, pv[i][2] << 16);
          s[3] += __builtin_bit_cast(float, pv[i][2] & 0xffff0000u);
        }
        if (lane < 60) gst128_sc1(boff, ws + WS_RED + jg * GR_BYTES + rt * 960, (u32x4){pk2(s[0], s[1]), tlo, pk2(s[2], s[3]), thi});
      }
      BAR(); BAR(); BAR();
      {
        const char* rb = ws + WS_RED + jg * GR_BYTES;
        u32x4 pv[14];
#pragma unroll 1
        for (unsigned spins = 0; spins < SPIN_CAP; ++spins) {
#pragma unroll
          for (int i = 0; i < 14; ++i) pv[i] = gld128_sc1(l60 * 16, rb + i * 960);
          asm volatile("s_waitcnt vmcnt(0)"
                       : "+v"(pv[0]), "+v"(pv[1]), "+v"(pv[2]), "+v"(pv[3]), "+v"(pv[4]), "+v"(pv[5]), "+v"(pv[6]), "+v"(pv[7]),
                         "+v"(pv[8]), "+v"(pv[9]), "+v"(pv[10]), "+v"(pv[11]), "+v"(pv[12]), "+v"(pv[13])::"memory");
          unsigned bad = 0;
#pragma unroll
          for (int i = 0; i < 14; ++i) bad |= (pv[i][1] ^ tlo) | (pv[i][3] ^ thi);
          if (__all(bad == 0)) break;
        }
#pragma unroll
        for (int i = 0; i < 14; ++i) sts64(L_KV + i * 480 + l60 * 8, (u32x2){pv[i][0], pv[i][2]});
      }
      BAR();
    }
    return;
  }

  const int mw = w & 3, nw = w >> 2;
  unsigned warm = 0;
  if (w < 4) {
    unsigned ln = b * 223 + tid;
    ln = ln < 56960 ? ln : 56959;
    const char* p = ln < 20480 ? x + ln * 128 : (ln < 39680 ? wqkv + (ln - 20480) * 128 : w1 + (ln - 39680) * 128);
    asm volatile("global_load_dword %0, %1, off" : "=v"(warm) : "v"(p) : "memory");
  }
  if (tid < 128) sts128(L_ZERO + tid * 16, (u32x4){0u, 0u, 0u, 0u});
  if (tid == 128) sts128(L_ONE, (u32x4){0x3f803f80u, 0x3f803f80u, 0x3f803f80u, 0x3f803f80u});
  u64 epoch;
  asm volatile("s_load_dwordx2 %0, %1, 0x0\n\ts_waitcnt lgkmcnt(0)" : "=s"(epoch) : "s"(ws) : "memory");
  const u64 tag = epoch * 6364136223846793005ull + 1442695040888963407ull;
  const unsigned tlo = (unsigned)tag, thi = (unsigned)(tag >> 32);
  const u32x2 tagv = {tlo, thi};

  unsigned xa = L_S0 + (32 * mw + c16) * 128 + ((g ^ ((c16 >> 1) & 7)) << 4);
  unsigned wa[8];
  {
    const unsigned wrd = L_S0 + 16384 + (4 * g + q4) * 480 + (isq ? 40 : 8) * p4;
#pragma unroll
    for (int t = 0; t < 8; ++t) {
      const int tq = nw == 0 ? (t < 5 ? 160 + 8 * t : 320 + 8 * (t - 5)) : (t < 5 ? 8 * t : 320 + 8 * (t - 2));
      const int th = (nw == 0 ? 0 : (jg < 6 ? 256 : 192)) + 32 * t;
      wa[t] = wrd + (isq ? tq : th);
    }
  }
  const bool gatew = !isq && jg == 7 && nw == 1;
  const int nt = isq ? 8 - nw : ((nw == 0 && jg < 6) ? 8 : (gatew ? 7 : 6));
  unsigned ga = L_GATE + g * 16;

  f32x4 acc[8][2];
#pragma unroll
  for (int t = 0; t < 8; ++t) { acc[t][0] = (f32x4){0.f, 0.f, 0.f, 0.f}; acc[t][1] = (f32x4){0.f, 0.f, 0.f, 0.f}; }
  u32x4 W[8], X0[2], X1[2];
#define GROUP(i, X) { acc[i][0] = mfma(W[i], X[0], acc[i][0]); acc[i][1] = mfma(W[i], X[1], acc[i][1]); }
#define RD(i, off) W[i] = cat(ldstr(wa[i] + (off)), ldstr(wa[i] + (off) + 7680))
#define RD6(off) { if (gatew) { W[6] = lds128(ga); ga += 64; } else { RD(6, off); } }
  STAMP(1);
  BAR();
  X0[0] = lds128(xa); X0[1] = lds128(xa + 2048);
#pragma unroll
  for (int i = 0; i < 6; ++i) RD(i, 0);
  RD6(0);
  RD(7, 0);
  SB();
  unsigned sb = L_S0;
#pragma unroll 1
  for (int t = 0; t < 10; ++t) {
    X1[0] = lds128(xa ^ 64); X1[1] = lds128((xa ^ 64) + 2048);
    SB();
#pragma unroll
    for (int i = 0; i < 6; ++i) { GROUP(i, X0); RD(i, 15360); SB(); }
    if (nt > 6) {
      GROUP(6, X0); RD6(15360); SB();
      if (nt > 7) { GROUP(7, X0); RD(7, 15360); SB(); }
    }
    GROUP(0, X1); GROUP(1, X1);
    SB();
    BAR();
    const unsigned sn = sb == L_S0 ? L_S1 : sb + SLOT;
    const unsigned dl = sn - sb;
    sb = sn;
    GROUP(2, X1);
    xa += dl;
    X0[0] = lds128(xa); X0[1] = lds128(xa + 2048);
    wa[0] += dl; RD(0, 0);
    SB();
    GROUP(3, X1);
    wa[1] += dl; RD(1, 0);
    wa[2] += dl; RD(2, 0);
    SB();
    GROUP(4, X1);
    wa[3] += dl; RD(3, 0);
    wa[4] += dl; RD(4, 0);
    SB();
    GROUP(5, X1);
    wa[5] += dl; RD(5, 0);
    SB();
    if (nt > 6) {
      GROUP(6, X1); wa[6] += dl; RD6(0); SB();
      if (nt > 7) { GROUP(7, X1); wa[7] += dl; RD(7, 0); SB(); }
    }
  }
  STAMP(2);
  asm volatile("s_waitcnt vmcnt(0)" : "+v"(warm)::"memory");

  const unsigned flagoff = (rt * 2 + (mw >> 1)) * 256 + (jg * 4 + nw * 2 + (mw & 1)) * 8;
  unsigned kq[10];
  const unsigned pbase = (p4 == g) ? L_WPHI + g * 800 + q4 * 40 : L_ZERO;
  if (!isq) {
    const int k0 = (hstart >> 6) + (nw == 0 ? 0 : (jg < 6 ? 4 : 3));
    char* abase = ws + WS_A + ((rt * 8 + 2 * mw) * 74 + k0) * 1024;
#pragma unroll
    for (int pr = 0; pr < 4; ++pr) {
      if (pr < 3 || nt > 7) {
#pragma unroll
        for (int mt = 0; mt < 2; ++mt) {
          const f32x4 a = acc[2 * pr][mt], c = acc[2 * pr + 1][mt];
          u32x4 f = {pk2(silu(a[0]), silu(a[1])), pk2(silu(a[2]), silu(a[3])), pk2(silu(c[0]), silu(c[1])), pk2(silu(c[2]), silu(c[3]))};
          gst128_sc1(boff, abase + (mt * 74 + pr) * 1024, f);
        }
      }
    }
    if (gatew && g == 0) {
#pragma unroll
      for (int mt = 0; mt < 2; ++mt) {
        const float gv = __builtin_amdgcn_rcpf(1.f + __builtin_amdgcn_exp2f(acc[6][mt][0] * -1.4426950408889634f));
        gst32_sc1((rt * 128 + 32 * mw + 16 * mt + c16) * 4, ws + WS_G, __builtin_bit_cast(unsigned, gv));
      }
    }
    STAMP(3);
    WAITV(0);
    if (lane == 0) gst64_sc1(flagoff, ws + WS_HF, tagv);
    STAMP(4);
#pragma unroll
    for (int i = 0; i < 10; ++i) kq[i] = 0;
  } else {
    unsigned mine[10], other[10];
#pragma unroll
    for (int t = 0; t < 5; ++t) {
      const f32x4 a = nw == 0 ? acc[t][0] : acc[t][1], c = nw == 0 ? acc[t][1] : acc[t][0];
      mine[2 * t] = pk2(a[0], a[1]); mine[2 * t + 1] = pk2(a[2], a[3]);
      other[2 * t] = pk2(c[0], c[1]); other[2 * t + 1] = pk2(c[2], c[3]);
    }
    const unsigned myrows = L_PHIK + (32 * mw + 16 * nw) * 352, prows = L_PHIK + (32 * mw + 16 * (nw ^ 1)) * 352;
    sts128(prows + boff, (u32x4){other[0], other[1], other[2], other[3]});
    sts128(prows + 1024 + boff, (u32x4){other[4], other[5], other[6], other[7]});
    sts64(prows + 2048 + lane * 8, (u32x2){other[8], other[9]});
#pragma unroll
    for (int i = 5; i < 8; ++i) {
      if (i < 7 || nw == 0) {
        const int tt = nw == 0 ? i - 5 : i - 2;
#pragma unroll
        for (int mt = 0; mt < 2; ++mt) {
          const f32x4 a = acc[i][mt];
          sts64(L_V + (32 * mw + 16 * mt + c16) * 160 + 40 * g + 8 * tt, (u32x2){pk2(a[0], a[1]), pk2(a[2], a[3])});
        }
      }
    }
    BAR();
    {
      const u32x4 r0 = lds128(myrows + boff), r1 = lds128(myrows + 1024 + boff);
      const u32x2 r2 = lds64(myrows + 2048 + lane * 8);
      other[0] = r0[0]; other[1] = r0[1]; other[2] = r0[2]; other[3] = r0[3];
      other[4] = r1[0]; other[5] = r1[1]; other[6] = r1[2]; other[7] = r1[3];
      other[8] = r2[0]; other[9] = r2[1];
    }
    unsigned kk[10];
#pragma unroll
    for (int i = 0; i < 10; ++i) { kk[i] = nw == 0 ? mine[i] : other[i]; kq[i] = nw == 0 ? other[i] : mine[i]; }
    unsigned phi[20];
    phi_unit(kk, pbase, phi);
#pragma unroll
    for (int i = 0; i < 5; ++i)
      sts128(myrows + c16 * 352 + 80 * g + 16 * i, (u32x4){phi[4 * i], phi[4 * i + 1], phi[4 * i + 2], phi[4 * i + 3]});
    BAR();
    STAMP(3);
    const unsigned ab = L_PHIK + (4 * g + q4) * 352 + 80 * mw + 8 * p4;
    const unsigned ab2 = L_PHIK + (4 * g + q4) * 352 + 80 * mw + 64 + 8 * (p4 & 1);
    const bool vreal = nw == 0 || p4 == 0;
    const unsigned vb = vreal ? L_V + (4 * g + q4) * 160 + 40 * mw + 32 * nw + 8 * p4 : L_ONE;
    const unsigned vs = vreal ? 16 * 160 : 0;
    u32x4 vf[4], pf[4][3];
#pragma unroll
    for (int ks = 0; ks < 4; ++ks) {
      const unsigned o = ks * 32 * 352;
      vf[ks] = cat(ldstr(vb + (2 * ks) * vs), ldstr(vb + (2 * ks + 1) * vs));
      pf[ks][0] = cat(ldstr(ab + o), ldstr(ab + o + 16 * 352));
      pf[ks][1] = cat(ldstr(ab + o + 32), ldstr(ab + o + 32 + 16 * 352));
      pf[ks][2] = cat(ldstr(ab2 + o), ldstr(ab2 + o + 16 * 352));
    }
    SB();
    f32x4 kv[3];
#pragma unroll
    for (int i = 0; i < 3; ++i) kv[i] = (f32x4){0.f, 0.f, 0.f, 0.f};
#pragma unroll
    for (int ks = 0; ks < 4; ++ks)
#pragma unroll
      for (int i = 0; i < 3; ++i) kv[i] = mfma(pf[ks][i], vf[ks], kv[i]);
    char* pb = ws + WS_PART + (jg * 16 + rt) * GR_BYTES;
    const unsigned po = (mw * 210 + (16 * nw + c16) * 10 + g) * 16;
    if (nw == 0 || c16 < 5) {
      gst128_sc1(po, pb, (u32x4){pk2(kv[0][0], kv[0][1]), tlo, pk2(kv[0][2], kv[0][3]), thi});
      gst128_sc1(po + 64, pb, (u32x4){pk2(kv[1][0], kv[1][1]), tlo, pk2(kv[1][2], kv[1][3]), thi});
      if (g < 2) gst128_sc1(po + 128, pb, (u32x4){pk2(kv[2][0], kv[2][1]), tlo, pk2(kv[2][2], kv[2][3]), thi});
    }
    STAMP(4);
  }

  BAR();
  STAMP(5);
  {
    const char* asrc = ws + WS_A + ((rt64 * 4 + mw) * 74 + nw) * 1024;
    u32x4 af[37];
#define LDA(a) af[a] = gld128_sc1(boff, asrc + (a) * 2048)
#define LDA4(a) LDA(a); LDA(a + 1); LDA(a + 2); LDA(a + 3)
    LDA4(0); LDA4(4); LDA4(8);
    unsigned phq[20];
    if (isq) phi_unit(kq, pbase, phq);
    else {
#pragma unroll
      for (int i = 0; i < 20; ++i) phq[i] = 0;
    }
    STAMP(6);
    f32x4 ob[5], oa[5];
#pragma unroll
    for (int n = 0; n < 5; ++n) { ob[n] = (f32x4){0.f, 0.f, 0.f, 0.f}; oa[n] = (f32x4){0.f, 0.f, 0.f, 0.f}; }
    const unsigned brd = nw * 5120 + boff;
    u32x4 b0[5], b1[5];
#define RDB(B, SL, J) { _Pragma("unroll") for (int n = 0; n < 5; ++n) B[n] = lds128(SL + brd + (J) * 10240 + n * 1024); SB(); }
#define MM(O, B, A) { PIN(af[A]); _Pragma("unroll") for (int n = 0; n < 5; ++n) O[n] = mfma(B[n], af[A], O[n]); SB(); }
#define STAGE4(O, SL, A, WT) RDB(b0, SL, 0) RDB(b1, SL, 1) WT; MM(O, b0, A) RDB(b0, SL, 2) MM(O, b1, A + 1) RDB(b1, SL, 3) MM(O, b0, A + 2) MM(O, b1, A + 3)
    STAGE4(ob, L_S1, 0, WAITV(8)) LDA4(12);
    BAR(); STAGE4(ob, L_S2, 4, WAITV(8)) LDA4(16);
    BAR(); STAGE4(ob, L_S0, 8, WAITV(8)) LDA4(20);
    BAR(); STAGE4(ob, L_S1, 12, WAITV(8)) LDA(24); LDA(25); LDA(26);
    STAMP(7);
    BAR();
    WAITV(7);
    if (isq) {
      const unsigned kb = (q4 == g) ? L_KV + g * 1680 + p4 * 80 : L_ZERO;
      const unsigned zb = (q4 == g) ? L_KV + g * 1680 + 1600 : L_ZERO;
      f32x4 o[6];
#pragma unroll
      for (int t = 0; t < 6; ++t) o[t] = (f32x4){0.f, 0.f, 0.f, 0.f};
      u32x4 ka[2][6];
#pragma unroll
      for (int t = 0; t < 6; ++t) ka[0][t] = t < 5 ? lds128(kb + t * 320) : lds128(zb);
#pragma unroll
      for (int s = 0; s < 5; ++s) {
        if (s < 4) {
#pragma unroll
          for (int t = 0; t < 6; ++t) ka[(s + 1) & 1][t] = t < 5 ? lds128(kb + t * 320 + (s + 1) * 16) : lds128(zb + (s + 1) * 16);
        }
        SB();
        const u32x4 bq = {phq[4 * s], phq[4 * s + 1], phq[4 * s + 2], phq[4 * s + 3]};
#pragma unroll
        for (int t = 0; t < 6; ++t) o[t] = mfma(ka[s & 1][t], bq, o[t]);
        SB();
      }
      char* abase = ws + WS_A + ((rt * 8 + 2 * mw + nw) * 74 + 54 + (jg >> 1) * 5) * 1024;
      const float sc = __builtin_amdgcn_rcpf(o[5][0] + 1e-6f);
      unsigned r[10];
#pragma unroll
      for (int t = 0; t < 5; ++t) { r[2 * t] = pk2(o[t][0] * sc, o[t][1] * sc); r[2 * t + 1] = pk2(o[t][2] * sc, o[t][3] * sc); }
      gst128_sc1(boff, abase + (2 * (jg & 1)) * 1024, (u32x4){r[0], r[1], r[2], r[3]});
      gst128_sc1(boff, abase + (2 * (jg & 1) + 1) * 1024, (u32x4){r[4], r[5], r[6], r[7]});
      gst64_sc1(boff + 8 * (jg & 1), abase + 4 * 1024, (u32x2){r[8], r[9]});
    }
    STAGE4(ob, L_S2, 16, )
    if (isq) {
      WAITV(0);
      if (lane == 0) gst64_sc1(flagoff, ws + WS_OF, tagv);
    }
    STAMP(8);
    BAR(); STAGE4(ob, L_S0, 20, WAITV(3))
    BAR();
    STAMP(9);
    unsigned gbits = gld32_sc1((rt64 * 64 + mw * 16 + c16) * 4, ws + WS_G);
    LDA4(27); LDA4(31); LDA(35); LDA(36);
    RDB(b0, L_S1, 0) RDB(b1, L_S1, 1) WAITV(11); MM(ob, b0, 24) RDB(b0, L_S1, 2) MM(ob, b1, 25) MM(ob, b0, 26)
    BAR(); STAGE4(oa, L_S2, 27, WAITV(6))
    BAR(); STAGE4(oa, L_S0, 31, WAITV(2))
    BAR(); RDB(b0, L_S1, 0) RDB(b1, L_S1, 1) WAITV(0); MM(oa, b0, 35) MM(oa, b1, 36)
    STAMP(10);
    PIN(gbits);
    const float gv = __builtin_bit_cast(float, gbits), omg = 1.f - gv;
#pragma unroll
    for (int n = 0; n < 5; ++n) ob[n] = ob[n] * omg + oa[n] * gv;
    const unsigned rb = L_S2 + mw * 5120 + boff;
    if (nw == 1) {
#pragma unroll
      for (int n = 0; n < 3; ++n) sts128(rb + n * 1024, __builtin_bit_cast(u32x4, ob[n]));
    } else {
#pragma unroll
      for (int n = 3; n < 5; ++n) sts128(rb + n * 1024, __builtin_bit_cast(u32x4, ob[n]));
    }
    BAR();
    const unsigned oo = (rt64 * 64 + mw * 16 + c16) * 1280 + ct * 160 + g * 8;
    if (nw == 0) {
#pragma unroll
      for (int n = 0; n < 3; ++n) {
        const f32x4 r = ob[n] + __builtin_bit_cast(f32x4, lds128(rb + n * 1024));
        gst64(oo + n * 32, out, (u32x2){pk2(r[0], r[1]), pk2(r[2], r[3])});
      }
    } else {
#pragma unroll
      for (int n = 3; n < 5; ++n) {
        const f32x4 r = ob[n] + __builtin_bit_cast(f32x4, lds128(rb + n * 1024));
        gst64(oo + n * 32, out, (u32x2){pk2(r[0], r[1]), pk2(r[2], r[3])});
      }
    }
  }
  while (__builtin_amdgcn_s_memrealtime() - tstart < 5000) __builtin_amdgcn_s_sleep(1);
}

extern "C" void kernel_launch(void* const* d_in, const int* in_sizes, int n_in, void* d_out, int out_size, void* d_ws,
                              size_t ws_size, hipStream_t stream) {
  (void)in_sizes; (void)n_in; (void)out_size; (void)ws_size;

  (void)hipFuncSetAttribute((const void*)fused_kernel, hipFuncAttributeMaxDynamicSharedMemorySize, LDS_BYTES);
  fused_kernel<<<256, 768, LDS_BYTES, stream>>>((const char*)d_in[0], (const char*)d_in[1], (const char*)d_in[2],
                                                (const char*)d_in[3], (const char*)d_in[5], (const char*)d_in[6],
                                                (const char*)d_in[7], (char*)d_out, (char*)d_ws);
}
```

```cpp
#include <hip/hip_runtime.h>
#include <stdint.h>

typedef __attribute__((ext_vector_type(8))) __bf16 bf16x8;
typedef __attribute__((ext_vector_type(2))) __bf16 bf16x2;
typedef __attribute__((ext_vector_type(4))) short s16x4;
typedef __attribute__((ext_vector_type(4))) float f32x4;
typedef __attribute__((ext_vector_type(2))) float f32x2;
typedef __attribute__((ext_vector_type(4))) unsigned u32x4;
typedef __attribute__((ext_vector_type(2))) unsigned u32x2;
typedef unsigned long long u64;
#define LAS __attribute__((address_space(3)))
#define DI __device__ __forceinline__

constexpr unsigned WS_EPOCH = 0;
constexpr unsigned WS_RT = 1024;
constexpr unsigned WS_HF = 4096;
constexpr unsigned WS_OF = 12288;
constexpr unsigned WS_G = 20480;
constexpr unsigned WS_PF = 28672;
constexpr unsigned WS_RED = 49152;
constexpr unsigned GR_BYTES = 896 * 16;
constexpr unsigned WS_PART = WS_RED + 8 * GR_BYTES;
constexpr unsigned WS_B = 2u << 20;
constexpr unsigned WS_A = 5u << 20;
constexpr unsigned WS_P = 15u << 20;
constexpr unsigned WS_PROF = 24u << 20;
static_assert(WS_PART + 128 * GR_BYTES <= WS_B, "ws");

constexpr unsigned SLOT = 47104;
constexpr unsigned L_S1 = 0, L_S2 = SLOT, L_S0 = 2 * SLOT, L_MISC = 3 * SLOT;
constexpr unsigned L_PHIK = L_S2 + 40960;
constexpr unsigned L_V = L_PHIK + 128 * 352;
constexpr unsigned L_BR = 0;
constexpr unsigned L_AR = 81920;
constexpr unsigned L_KV = 147456;
constexpr unsigned L_WPHI = 154240;
constexpr unsigned L_GATE = L_WPHI;
constexpr unsigned L_ZERO = L_WPHI + 4096;
constexpr unsigned L_ONE = L_ZERO + 2048;
constexpr unsigned L_TAB = L_ONE + 32;
constexpr unsigned LDS_BYTES = 163840;
static_assert(L_ONE + 32 + 3360 <= LDS_BYTES, "lds");

#define WPOL ""
#define HOP1_SLEEP 8
#define NQ2 6
#define SG 2
constexpr int NH2 = 27 - NQ2;
constexpr unsigned CODE_BYTES = 25600;
constexpr unsigned SPIN_CAP = 1u << 16;

DI unsigned pk2(float a, float b) {
  bf16x2 v = __builtin_convertvector((f32x2){a, b}, bf16x2);
  return __builtin_bit_cast(unsigned, v);
}
DI f32x4 mfma(u32x4 a, u32x4 b, f32x4 c) {
  return __builtin_amdgcn_mfma_f32_16x16x32_bf16(__builtin_bit_cast(bf16x8, a), __builtin_bit_cast(bf16x8, b), c, 0, 0, 0);
}
DI u32x4 lds128(unsigned a) { return *(LAS u32x4*)(size_t)a; }
DI u32x2 lds64(unsigned a) { return *(LAS u32x2*)(size_t)a; }
DI void sts128(unsigned a, u32x4 v) { *(LAS u32x4*)(size_t)a = v; }
DI void sts64(unsigned a, u32x2 v) { *(LAS u32x2*)(size_t)a = v; }
DI u32x2 ldstr(unsigned a) {
  return __builtin_bit_cast(u32x2, __builtin_amdgcn_ds_read_tr16_b64_v4i16((LAS s16x4*)(size_t)a));
}
DI u32x4 cat(u32x2 a, u32x2 b) { return (u32x4){a[0], a[1], b[0], b[1]}; }
DI void glds(unsigned voff, const void* sbase, unsigned lds) {
  asm volatile("s_mov_b32 m0, %2\n\ts_nop 0\n\tglobal_load_lds_dwordx4 %0, %1" ::"v"(voff), "s"(sbase), "s"(lds) : "memory");
}
DI void glds_m(unsigned voff, const void* sbase, unsigned lds, u64 mask) {
  asm volatile("s_mov_b32 m0, %2\n\ts_mov_b64 exec, %3\n\tglobal_load_lds_dwordx4 %0, %1 nt\n\ts_mov_b64 exec, -1" ::"v"(voff), "s"(sbase), "s"(lds), "s"(mask) : "memory");
}
DI void glds5_sc1(unsigned voff, unsigned voff1k, const void* sbase, unsigned lds) {
  asm volatile(
      "s_mov_b32 m0, %2\n\ts_nop 0\n\t"
      "global_load_lds_dwordx4 %0, %1 sc1\n\t"
      "global_load_lds_dwordx4 %0, %1 offset:1024 sc1\n\t"
      "global_load_lds_dwordx4 %0, %1 offset:2048 sc1\n\t"
      "global_load_lds_dwordx4 %0, %1 offset:3072 sc1\n\t"
      "s_add_u32 m0, m0, 1024\n\ts_nop 0\n\t"
      "global_load_lds_dwordx4 %3, %1 offset:3072 sc1" ::"v"(voff), "s"(sbase), "s"(lds), "v"(voff1k)
      : "memory", "scc");
}
DI u32x4 gld128_sc1(unsigned voff, const void* sbase) {
  u32x4 r;
  asm volatile("global_load_dwordx4 %0, %1, %2 sc1" : "=v"(r) : "v"(voff), "s"(sbase) : "memory");
  return r;
}
DI u32x2 gld64_sc1(unsigned voff, const void* sbase) {
  u32x2 r;
  asm volatile("global_load_dwordx2 %0, %1, %2 sc1" : "=v"(r) : "v"(voff), "s"(sbase) : "memory");
  return r;
}
DI unsigned gld32_sc1(unsigned voff, const void* sbase) {
  unsigned r;
  asm volatile("global_load_dword %0, %1, %2 sc1" : "=v"(r) : "v"(voff), "s"(sbase) : "memory");
  return r;
}
DI unsigned gld32(unsigned voff, const void* sbase) {
  unsigned r;
  asm volatile("global_load_dword %0, %1, %2" : "=v"(r) : "v"(voff), "s"(sbase) : "memory");
  return r;
}
DI void gst128_sc1(unsigned voff, void* sbase, u32x4 v) {
  asm volatile("global_store_dwordx4 %0, %1, %2 sc1\n\ts_nop 1" ::"v"(voff), "v"(v), "s"(sbase) : "memory");
}
DI void gst64_sc1(unsigned voff, void* sbase, u32x2 v) {
  asm volatile("global_store_dwordx2 %0, %1, %2 sc1" ::"v"(voff), "v"(v), "s"(sbase) : "memory");
}
DI void gst32_sc1(unsigned voff, void* sbase, unsigned v) {
  asm volatile("global_store_dword %0, %1, %2 sc1" ::"v"(voff), "v"(v), "s"(sbase) : "memory");
}
DI void gst64(unsigned voff, void* sbase, u32x2 v) {
  asm volatile("global_store_dwordx2 %0, %1, %2" ::"v"(voff), "v"(v), "s"(sbase) : "memory");
}
#define WAITV(n) asm volatile("s_waitcnt vmcnt(" #n ")" ::: "memory")
#define BAR() asm volatile("s_waitcnt lgkmcnt(0)\n\ts_barrier" ::: "memory")
#define PIN(x) asm volatile("" : "+v"(x))
#define SB() __builtin_amdgcn_sched_barrier(0)

DI void poll_words(const char* base, unsigned voff, unsigned tlo, unsigned thi) {
#pragma unroll 1
  for (unsigned spins = 0; spins < SPIN_CAP; ++spins) {
    u32x2 v = gld64_sc1(voff, base);
    asm volatile("s_waitcnt vmcnt(0)" : "+v"(v)::"memory");
    if (__all(v[0] == tlo && v[1] == thi)) break;
    __builtin_amdgcn_s_sleep(1);
  }
}

DI void dual_softmax(const f32x4 (&p)[5], unsigned (&o)[20]) {
  float mx = __builtin_fmaxf(p[0][0], p[0][1]), mn = __builtin_fminf(p[0][0], p[0][1]);
  float mx2 = __builtin_fmaxf(p[0][2], p[0][3]), mn2 = __builtin_fminf(p[0][2], p[0][3]);
#pragma unroll
  for (int t = 1; t < 5; ++t) {
    mx = __builtin_fmaxf(__builtin_fmaxf(mx, p[t][0]), p[t][1]); mn = __builtin_fminf(__builtin_fminf(mn, p[t][0]), p[t][1]);
    mx2 = __builtin_fmaxf(__builtin_fmaxf(mx2, p[t][2]), p[t][3]); mn2 = __builtin_fminf(__builtin_fminf(mn2, p[t][2]), p[t][3]);
  }
  const float L = 1.4426950408889634f;
  const float mxl = __builtin_fmaxf(mx, mx2) * -L, mnl = __builtin_fminf(mn, mn2) * L;
  f32x2 ep[10], en[10];
#pragma unroll
  for (int i = 0; i < 10; ++i) {
    const f32x2 v = {p[i >> 1][2 * (i & 1)], p[i >> 1][2 * (i & 1) + 1]};
    ep[i] = __builtin_elementwise_fma(v, (f32x2){L, L}, (f32x2){mxl, mxl});
    en[i] = __builtin_elementwise_fma(v, (f32x2){-L, -L}, (f32x2){mnl, mnl});
  }
  SB();
  f32x2 sp, sn;
#pragma unroll
  for (int i = 0; i < 10; ++i) {
    ep[i][0] = __builtin_amdgcn_exp2f(ep[i][0]); ep[i][1] = __builtin_amdgcn_exp2f(ep[i][1]);
    en[i][0] = __builtin_amdgcn_exp2f(en[i][0]); en[i][1] = __builtin_amdgcn_exp2f(en[i][1]);
    if (i == 1) { sp = ep[0]; sn = en[0]; }
    if (i > 1) { sp += ep[i - 1]; sn += en[i - 1]; }
    SB();
  }
  sp += ep[9]; sn += en[9];
  const float rp = __builtin_amdgcn_rcpf(sp[0] + sp[1]), rn = __builtin_amdgcn_rcpf(sn[0] + sn[1]);
  f32x2 rp2 = {rp, rp}, rn2 = {rn, rn};
  PIN(rp2); PIN(rn2);
#pragma unroll
  for (int i = 0; i < 10; ++i) { ep[i] *= rp2; en[i] *= rn2; }
  SB();
#pragma unroll
  for (int i = 0; i < 10; ++i) { o[i] = pk2(ep[i][0], ep[i][1]); o[10 + i] = pk2(en[i][0], en[i][1]); }
}
struct WPhi { u32x4 a0[5], a1[5]; u32x2 a2[5]; };
DI void wphi_read(WPhi& f, unsigned pbase) {
#pragma unroll
  for (int t = 0; t < 5; ++t) f.a0[t] = cat(ldstr(pbase + 8 * t), ldstr(pbase + 8 * t + 160));
#pragma unroll
  for (int t = 0; t < 5; ++t) f.a1[t] = cat(ldstr(pbase + 8 * t + 320), ldstr(pbase + 8 * t + 480));
#pragma unroll
  for (int t = 0; t < 5; ++t) f.a2[t] = ldstr(pbase + 8 * t + 640);
}
DI void phi_proj(const WPhi& f, const unsigned (&kq)[10], f32x4 (&pp)[5]) {
  const u32x4 f0 = {kq[0], kq[1], kq[2], kq[3]}, f1 = {kq[4], kq[5], kq[6], kq[7]};
  const u32x2 f2 = {kq[8], kq[9]};
#pragma unroll
  for (int t = 0; t < 5; ++t) pp[t] = mfma(f.a0[t], f0, (f32x4){0.f, 0.f, 0.f, 0.f});
#pragma unroll
  for (int t = 0; t < 5; ++t) pp[t] = mfma(f.a1[t], f1, pp[t]);
#pragma unroll
  for (int t = 0; t < 5; ++t)
    pp[t] = __builtin_amdgcn_mfma_f32_16x16x16bf16_1k(__builtin_bit_cast(s16x4, f.a2[t]), __builtin_bit_cast(s16x4, f2), pp[t], 0, 0, 0);
}
DI u32x4 silu8(f32x4 a, f32x4 c) {
  const float NL = -1.4426950408889634f;
  f32x2 v[4] = {{a[0], a[1]}, {a[2], a[3]}, {c[0], c[1]}, {c[2], c[3]}}, e[4];
#pragma unroll
  for (int i = 0; i < 4; ++i) e[i] = v[i] * (f32x2){NL, NL};
#pragma unroll
  for (int i = 0; i < 4; ++i) { e[i][0] = __builtin_amdgcn_exp2f(e[i][0]); e[i][1] = __builtin_amdgcn_exp2f(e[i][1]); }
#pragma unroll
  for (int i = 0; i < 4; ++i) e[i] += (f32x2){1.f, 1.f};
#pragma unroll
  for (int i = 0; i < 4; ++i) { e[i][0] = __builtin_amdgcn_rcpf(e[i][0]); e[i][1] = __builtin_amdgcn_rcpf(e[i][1]); }
#pragma unroll
  for (int i = 0; i < 4; ++i) v[i] *= e[i];
  return (u32x4){pk2(v[0][0], v[0][1]), pk2(v[1][0], v[1][1]), pk2(v[2][0], v[2][1]), pk2(v[3][0], v[3][1])};
}

DI void amfma(f32x4& c, const u32x4& a, const u32x4& b) { asm volatile("v_mfma_f32_16x16x32_bf16 %0, %1, %2, %0" : "+v"(c) : "v"(a), "v"(b)); }
template <int OFF> DI void trlo(u32x4& f, unsigned a) { u32x2 t; asm volatile("ds_read_b64_tr_b16 %0, %1 offset:%2" : "=v"(t) : "v"(a), "i"(OFF)); f[0] = t[0]; f[1] = t[1]; }
template <int OFF> DI void trhi(u32x4& f, unsigned a) { u32x2 t; asm volatile("ds_read_b64_tr_b16 %0, %1 offset:%2" : "=v"(t) : "v"(a), "i"(OFF)); f[2] = t[0]; f[3] = t[1]; }
template <int OFF> DI void rd128(u32x4& f, unsigned a) { asm volatile("ds_read_b128 %0, %1 offset:%2" : "=v"(f) : "v"(a), "i"(OFF)); }
DI void vadd(unsigned& v, unsigned s) { asm volatile("v_add_u32 %0, %1, %0" : "+v"(v) : "s"(s)); }
#define WL(n) asm volatile("s_waitcnt lgkmcnt(" #n ")")
#define MFMA_DONE() asm volatile("s_waitcnt lgkmcnt(0)\n\ts_nop 7\n\ts_nop 3" ::: "memory")

template <int NT, bool Q, bool GATE>
DI void gemm1_loop(f32x4 (&acc)[8][2], unsigned xa, unsigned wa, unsigned wb, unsigned gk) {
  u32x4 W[8], X0[2], X1[2];
  unsigned xb = xa ^ 64;
#pragma unroll
  for (int t = 0; t < NT; ++t) { acc[t][0] = (f32x4){0.f, 0.f, 0.f, 0.f}; acc[t][1] = (f32x4){0.f, 0.f, 0.f, 0.f}; PIN(acc[t][0]); PIN(acc[t][1]); }
  BAR();
#define TO(i) (Q ? ((i) < 5 ? (NT == 8 ? 0 : 80) + 8 * (i) : 160 + 8 * ((i) - (NT == 8 ? 5 : 2))) : 32 * (i))
#define AD(i) wa
#define RL(i, off) if constexpr (GATE && (i) == 6) { rd128<0>(W[i], wb); } else { trlo<TO(i) + (off)>(W[i], AD(i)); }
#define RH(i, off) if constexpr (GATE && (i) == 6) { gk = gk == 19 ? 0 : gk + 1; vadd(wb, gk == 0 ? -1216 : 64); } else { trhi<TO(i) + (off) + 7680>(W[i], AD(i)); }
#define M0(i, X) amfma(acc[i][0], W[i], X[0]);
#define M1(i, X) amfma(acc[i][1], W[i], X[1]);
#define G1(i, j) M0(i, X0) RL(j, 15360) M1(i, X0) RH(j, 15360)
#define G1X(i, j, h) M0(i, X0) RL(j, 15360) rd128<2048 * (h)>(X1[h], xb); M1(i, X0) RH(j, 15360)
  rd128<0>(X0[0], xa); rd128<2048>(X0[1], xa);
  RL(0, 0) RH(0, 0) RL(1, 0) RH(1, 0) RL(2, 0) RH(2, 0) RL(3, 0) RH(3, 0) RL(4, 0) RH(4, 0)
  if constexpr (NT > 6) { RL(5, 0) RH(5, 0) }
  if constexpr (NT > 7) { RL(6, 0) RH(6, 0) }
  unsigned sb = L_S0;
#pragma unroll 1
  for (int t = 0; t < 10; ++t) {
    if constexpr (NT == 8) {
      WL(10); M0(0, X0) RL(7, 0) M1(0, X0) RH(7, 0) G1(1, 0)
      WL(10); G1(2, 1) G1(3, 2)
      WL(10); G1X(4, 3, 0) G1X(5, 4, 1)
      WL(12); G1(6, 5) G1(7, 6)
      WL(5);
    } else if constexpr (NT == 7) {
      WL(8); M0(0, X0) RL(6, 0) M1(0, X0) RH(6, 0) G1(1, 0)
      WL(7); G1(2, 1) G1X(3, 2, 0)
      WL(8); G1X(4, 3, 1) G1(5, 4)
      WL(12); G1(6, 5)
      WL(5);
    } else {
      WL(6); M0(0, X0) RL(5, 0) M1(0, X0) RH(5, 0) G1(1, 0)
      WL(6); G1X(2, 1, 0) G1X(3, 2, 1)
      WL(8); G1(4, 3) G1(5, 4)
      WL(4);
    }
    M0(0, X1) RL(NT - 1, 15360) M1(0, X1) RH(NT - 1, 15360)
    const unsigned sn = sb == L_S0 ? L_S1 : sb + SLOT, dl = sn - sb;
    sb = sn;
    M0(1, X1) vadd(xa, dl); M1(1, X1) vadd(wa, dl);
    M0(2, X1) vadd(xb, dl); M1(2, X1)
    BAR();
    M0(3, X1) rd128<0>(X0[0], xa); rd128<2048>(X0[1], xa); M1(3, X1) RL(0, 0) RH(0, 0)
    M0(4, X1) RL(1, 0) RH(1, 0) M1(4, X1) RL(2, 0) RH(2, 0)
    if constexpr (NT == 8) {
      M0(5, X1) RL(3, 0) RH(3, 0) M1(5, X1) RL(4, 0)
      M0(6, X1) RH(4, 0) RL(5, 0) M1(6, X1) RH(5, 0)
      M0(7, X1) RL(6, 0) M1(7, X1) RH(6, 0)
    } else if constexpr (NT == 7) {
      M0(5, X1) RL(3, 0) RH(3, 0) M1(5, X1) RL(4, 0) RH(4, 0)
      M0(6, X1) RL(5, 0) M1(6, X1) RH(5, 0)
    } else {
      M0(5, X1) RL(3, 0) RH(3, 0) M1(5, X1) RL(4, 0) RH(4, 0)
    }
  }
  MFMA_DONE();
#undef TO
#undef AD
#undef RL
#undef RH
#undef M0
#undef M1
#undef G1
#undef G1X
}

struct P2 {
  u32x4 A[2], B[3];
  unsigned aa, ba;
  unsigned st;
};
DI void p2_first(P2& p) {
  asm volatile("ds_read_b128 %0, %5\n\tds_read_b128 %2, %6\n\tds_read_b128 %3, %6 offset:1024\n\tds_read_b128 %4, %6 offset:2048\n\t"
               "ds_read_b128 %1, %5 offset:2048\n\ts_waitcnt lgkmcnt(0)"
               : "=&v"(p.A[0]), "=&v"(p.A[1]), "=&v"(p.B[0]), "=&v"(p.B[1]), "=&v"(p.B[2]) : "v"(p.aa), "v"(p.ba) : "memory");
}
#define MF(d, a, b) "v_mfma_f32_16x16x32_bf16 %" #d ", %" #a ", %" #b ", %" #d "\n\t"
#define RD(d, a, o) "ds_read_b128 %" #d ", %" #a " offset:" #o "\n\t"
DI void p2_run(P2& p, f32x4 (&o)[2][3], int n, int nw) {
  u32x4 A0, A1, B0, B1, B2;
  unsigned da, db;
  asm volatile(
      "s_cmp_lg_u32 %22, 0\n\ts_cbranch_scc1 2f\n"
      "1:\n\t"
      "s_add_u32 %18, %18, 1\n\ts_and_b32 %20, %18, 3\n\ts_cselect_b32 %20, 16384, %23\n\t"
      "s_waitcnt lgkmcnt(2)\n\t" MF(0, 8, 6) RD(11, 16, 1024)
      "s_and_b32 %21, %18, 7\n\t" MF(1, 9, 6) RD(13, 17, 5120)
      "s_waitcnt lgkmcnt(2)\n\t" MF(2, 10, 6) RD(14, 17, 6144)
      "s_cselect_b32 %21, 10240, %24\n\t" MF(3, 8, 7) RD(15, 17, 7168)
      MF(4, 9, 7) RD(12, 16, 3072)
      MF(5, 10, 7) "v_add_u32 %16, %20, %16\n\t"
      "s_waitcnt lgkmcnt(2)\n\t" MF(0, 13, 11) "v_add_u32 %17, %21, %17\n\t"
      MF(1, 14, 11)
      "s_waitcnt lgkmcnt(0)\n\ts_barrier\n\t"
      MF(2, 15, 11) RD(6, 16, 0) RD(8, 17, 0)
      MF(3, 13, 12) RD(9, 17, 1024) RD(10, 17, 2048)
      MF(4, 14, 12) RD(7, 16, 2048) "s_sub_u32 %19, %19, 1\n\t"
      MF(5, 15, 12)
      "s_cmp_lg_u32 %19, 0\n\ts_cbranch_scc1 1b\n\t"
      "s_branch 3f\n"
      "2:\n\t"
      "s_add_u32 %18, %18, 1\n\ts_and_b32 %20, %18, 3\n\ts_cselect_b32 %20, 16384, %23\n\t"
      "s_waitcnt lgkmcnt(1)\n\t" MF(0, 8, 6) RD(11, 16, 1024) RD(13, 17, 5120)
      "s_and_b32 %21, %18, 7\n\t" MF(1, 9, 6) RD(14, 17, 6144)
      "s_cselect_b32 %21, 10240, %24\n\ts_waitcnt lgkmcnt(3)\n\t" MF(3, 8, 7) RD(12, 16, 3072)
      MF(4, 9, 7) "v_add_u32 %16, %20, %16\n\tv_add_u32 %17, %21, %17\n\t"
      "s_waitcnt lgkmcnt(1)\n\t" MF(0, 13, 11)
      MF(1, 14, 11)
      "s_waitcnt lgkmcnt(0)\n\ts_barrier\n\t"
      RD(6, 16, 0) RD(8, 17, 0)
      MF(3, 13, 12) RD(9, 17, 1024) RD(7, 16, 2048) "s_sub_u32 %19, %19, 1\n\t"
      MF(4, 14, 12)
      "s_cmp_lg_u32 %19, 0\n\ts_cbranch_scc1 2b\n"
      "3:\n\t"
      "s_waitcnt lgkmcnt(0)\n\ts_nop 7"
      : "+v"(o[0][0]), "+v"(o[0][1]), "+v"(o[0][2]), "+v"(o[1][0]), "+v"(o[1][1]), "+v"(o[1][2]),
        "+v"(p.A[0]), "+v"(p.A[1]), "+v"(p.B[0]), "+v"(p.B[1]), "+v"(p.B[2]),
        "=&v"(A0), "=&v"(A1), "=&v"(B0), "=&v"(B1), "=&v"(B2),
        "+v"(p.aa), "+v"(p.ba), "+s"(p.st), "+s"(n), "=&s"(da), "=&s"(db)
      : "s"(nw), "s"(-49152), "s"(-71680)
      : "memory", "scc");
}
#undef MF
#undef RD

#define TRW 0
#define TRL 0
#define STAMP(i)

__global__ __launch_bounds__(768, 3) void fused_kernel(const char* __restrict__ x, const char* __restrict__ wqkv,
                                                       const char* __restrict__ wphi, const char* __restrict__ wo,
                                                       const char* __restrict__ w1, const char* __restrict__ w2,
                                                       const char* __restrict__ gate, char* out, char* ws) {
  extern __shared__ __attribute__((aligned(16))) char smem[];
  u64 pc;
  asm volatile("s_getpc_b64 %0" : "=s"(pc));
  STAMP(0);
  asm volatile("" ::"s"(x), "s"(wqkv), "s"(wphi), "s"(wo), "s"(w1), "s"(w2), "s"(gate), "s"(out), "s"(ws));
  const int tid = threadIdx.x, lane = tid & 63;
  const int w = __builtin_amdgcn_readfirstlane(tid >> 6);
  const int b = blockIdx.x, xcd = b & 7, idx = b >> 3;
  const int rt = (xcd & 1) * 8 + (idx & 7);
  const int cb = idx >> 3, cq = xcd >> 1;
  const bool isq = cb < 2;
  const int jg = 2 * cq + (cb & 1);
  const int rt2 = (xcd >> 1) * 4 + ((idx >> 2) & 3), ct = (xcd & 1) * 4 + (idx & 3);
  const int g = lane >> 4, c16 = lane & 15, q4 = c16 >> 2, p4 = lane & 3;
  const int hstart = jg * 448 - (jg == 7 ? 64 : 0);
  const unsigned boff = lane * 16;
#define ROTM 7
  const int rot = ROTM == 0 ? 0 : ROTM == 5 ? (((((idx & 7) + cb) & 3) * 5) >> 1) + (xcd & 1) : ((((idx & 7) + cb) % 5) * 2 + (xcd & 1));

  if (__builtin_expect(w >= 8, 1)) {
    const int L = w - 8;
    const int ldw = isq ? 3840 : 3456;
    unsigned xo[4], wv[8];
    {
      const unsigned x0 = (rt * 128 + 8 * L + (lane >> 3)) * 1280 + (((lane & 7) ^ ((4 * L + (lane >> 4)) & 7)) << 4);
#pragma unroll
      for (int i = 0; i < 4; ++i) xo[i] = x0 + i * 32 * 1280;
      const int rs = lane >= 30 ? 1 : 0, pos = lane - 30 * rs;
      const int sec = pos < 10 ? 0 : (pos < 15 ? 2560 - 160 : (pos < 25 ? 1280 - 240 : 2560 - 320));
      const int col = isq ? sec + jg * 160 + pos * 16 : hstart + pos * 16;
      const unsigned w0 = (8 * (L >> 1) + 2 * (L & 1) + rs) * ldw + col;
#pragma unroll
      for (int i = 0; i < 8; ++i) wv[i] = w0 + (32 * (i >> 2) + 16 * (i & 1) + 4 * ((i >> 1) & 1)) * ldw;
    }
    const u64 half = isq ? 0x3fffffffull : (jg < 6 ? 0x0fffffffull : 0x00ffffffull);
    const u64 wmask = half | (half << 30);
    const char* wbase = isq ? wqkv : w1;
    const char* xs = x + rot * 128;
    const char* wsrc = wbase + rot * 64 * ldw;
    int kst = rot;
    const unsigned mx0 = L * 1024, mw0 = 16384 + L * 960;
    auto issue_lo = [&](unsigned sb) {
      asm volatile(
          "s_mov_b32 m0, %10\n\ts_nop 0\n\tglobal_load_lds_dwordx4 %0, %8\n\t"
          "s_add_u32 m0, m0, 4096\n\ts_nop 0\n\tglobal_load_lds_dwordx4 %1, %8\n\t"
          "s_add_u32 m0, m0, 4096\n\ts_nop 0\n\tglobal_load_lds_dwordx4 %2, %8\n\t"
          "s_add_u32 m0, m0, 4096\n\ts_nop 0\n\tglobal_load_lds_dwordx4 %3, %8\n\t"
          "s_mov_b32 m0, %11\n\ts_mov_b64 exec, %12\n\tglobal_load_lds_dwordx4 %4, %9 " WPOL "\n\t"
          "s_add_u32 m0, m0, 3840\n\ts_nop 0\n\tglobal_load_lds_dwordx4 %5, %9 " WPOL "\n\t"
          "s_add_u32 m0, m0, 3840\n\ts_nop 0\n\tglobal_load_lds_dwordx4 %6, %9 " WPOL "\n\t"
          "s_add_u32 m0, m0, 3840\n\ts_nop 0\n\tglobal_load_lds_dwordx4 %7, %9 " WPOL "\n\t"
          "s_mov_b64 exec, -1"
          ::"v"(xo[0]), "v"(xo[1]), "v"(xo[2]), "v"(xo[3]), "v"(wv[0]), "v"(wv[1]), "v"(wv[2]), "v"(wv[3]),
            "s"(xs), "s"(wsrc), "s"(sb + mx0), "s"(sb + mw0), "s"(wmask)
          : "memory", "scc");
    };
    auto issue_hi = [&](unsigned sb) {
      asm volatile(
          "s_mov_b32 m0, %5\n\ts_mov_b64 exec, %6\n\tglobal_load_lds_dwordx4 %0, %4 " WPOL "\n\t"
          "s_add_u32 m0, m0, 3840\n\ts_nop 0\n\tglobal_load_lds_dwordx4 %1, %4 " WPOL "\n\t"
          "s_add_u32 m0, m0, 3840\n\ts_nop 0\n\tglobal_load_lds_dwordx4 %2, %4 " WPOL "\n\t"
          "s_add_u32 m0, m0, 3840\n\ts_nop 0\n\tglobal_load_lds_dwordx4 %3, %4 " WPOL "\n\t"
          "s_mov_b64 exec, -1"
          ::"v"(wv[4]), "v"(wv[5]), "v"(wv[6]), "v"(wv[7]), "s"(wsrc), "s"(sb + mw0 + 15360), "s"(wmask)
          : "memory", "scc");
      const bool wrap = ++kst == 10;
      xs = wrap ? x : xs + 128;
      wsrc = wrap ? wbase : wsrc + 64 * ldw;
    };
    auto issue_stage = [&](unsigned sb) { issue_lo(sb); issue_hi(sb); };
#define LSTAMP()
#define LBAR() asm volatile("s_barrier" ::: "memory")
#define STEP(N, SL) WAITV(N); LSTAMP(); LBAR(); LSTAMP(); issue_stage(SL); LSTAMP();
    LSTAMP();
    issue_stage(L_S0);
    LSTAMP();
    int lane2 = lane, unit = b * 3 + L;
    asm volatile("" : "+v"(lane2), "+s"(unit));
    if (L == 3) {
      const char* src = isq ? wphi + jg * 3200 : gate;
      const unsigned lim = isq ? 3184 : 1264;
#pragma unroll
      for (int n = 0; n < 4; ++n) {
        const unsigned o = n * 1024 + lane2 * 16;
        glds(o < lim ? o : lim, src, L_WPHI + n * 1024);
      }
    }
    issue_lo(L_S1);
    LSTAMP();
    WAITV(8); LSTAMP(); LBAR(); LSTAMP();
    issue_hi(L_S1);
    issue_lo(L_S2);
    WAITV(8); LSTAMP(); LBAR(); LSTAMP();
    issue_hi(L_S2);
    asm volatile("" : "+v"(lane2), "+s"(unit));
    const bool retile = (L < 3) && (unit < 740);
    const int rk = retile ? unit / 10 : 0, rcu = retile ? unit - rk * 10 : 0;
    {
      const char* src = rk < 54 ? w2 : wo;
      const int kp = rk - 54, P = kp / 5, s5 = kp - P * 5;
      const bool lft = rk >= 54 && s5 == 4;
      const int gs = lft ? 40 : 4;
      const int r0 = rk < 54 ? 32 * rk : (lft ? 160 * P + 16 : 160 * P + 40 * s5);
      const int rh = rk < 54 ? 16 : (lft ? 2 : 20);
      const int c = lane2 & 7, e = (lane2 >> 3) & 3;
      const unsigned v0 = (r0 + (lane2 >> 5) * gs + (lft ? 20 * (e & 1) + (e >> 1) : e)) * 1280 + rcu * 128 + ((((c >> 1) ^ ((lane2 >> 4) & 3)) << 5) | ((c & 1) << 4));
      const u64 m = retile ? ~0ull : 1ull;
#pragma unroll
      for (int n = 0; n < 4; ++n)
        glds_m(v0 + ((n & 1) * 2 * gs + (n >> 1) * rh) * 1280, src, (L < 3 ? L_MISC + L * 4096 + n * 1024 : L_ONE + 16), m);
    }
    u64 epoch;
    asm volatile("s_load_dwordx2 %0, %1, 0x0\n\ts_waitcnt lgkmcnt(0)" : "=s"(epoch) : "s"(ws) : "memory");
    const u64 tag = epoch * 6364136223846793005ull + 1442695040888963407ull;
    const unsigned tlo = (unsigned)tag, thi = (unsigned)(tag >> 32);
    const u32x2 tagv = {tlo, thi};
    issue_stage(L_S0);
    STEP(12, L_S1)
    if (retile) {
      const int g2 = lane2 >> 4, q2 = (lane2 >> 2) & 3;
      const unsigned base = L_MISC + L * 4096 + (4 * g2 + q2) * 128 + 8 * (lane2 & 3);
      const int sw = (2 * g2 + (q2 >> 1)) & 3;
      u32x4 f[4];
#pragma unroll
      for (int n = 0; n < 4; ++n) { const unsigned a = base + ((n ^ sw) << 5); f[n] = cat(ldstr(a), ldstr(a + 2048)); }
#pragma unroll
      for (int n = 0; n < 4; ++n) {
        const int N = 4 * rcu + n, c5 = N / 5;
        gst128_sc1(boff, ws + WS_B + ((c5 * 74 + rk) * 5 + (N - c5 * 5)) * 1024, f[n]);
      }
      WAITV(16);
    } else {
      WAITV(12);
    }
    STEP(16, L_S2)
    STEP(12, L_S0)
    if (L == 3 && lane == 0) gst64_sc1(b * 8, ws + WS_RT, tagv);
    STEP(12, L_S1)
    STEP(12, L_S2)
    STEP(12, L_S0)
    WAITV(12);
    LSTAMP();
    LBAR();
    LSTAMP();
    const int k0 = isq ? 54 - 2 * NQ2 : 0;
    if (L == 1) {
      const char* bsrc = ws + WS_B + (ct * 74 + k0) * 5120;
      const unsigned boff1k = boff + 1024;
      unsigned bl = L_BR;
      auto issue_b = [&](int n) {
#pragma unroll 1
        for (int j = 0; j < 2 * n; ++j) { glds5_sc1(boff, boff1k, bsrc, bl); bsrc += 5120; bl = bl == L_BR + 76800 ? L_BR : bl + 5120; }
      };
      auto rt_ok = [&]() {
        u32x4 r0, r1;
        asm volatile("global_load_dwordx4 %0, %2, %3 sc1\n\tglobal_load_dwordx4 %1, %2, %3 offset:16 sc1\n\ts_waitcnt vmcnt(0)"
                     : "=&v"(r0), "=&v"(r1) : "v"(lane * 32), "s"(ws + WS_RT) : "memory");
        const unsigned bad = (r0[0] ^ tlo) | (r0[1] ^ thi) | (r0[2] ^ tlo) | (r0[3] ^ thi) | (r1[0] ^ tlo) | (r1[1] ^ thi) | (r1[2] ^ tlo) | (r1[3] ^ thi);
        return __all(bad == 0);
      };
      __builtin_amdgcn_s_setprio(2);
      if (rt_ok()) {
        issue_b(1);
        LBAR();
        issue_b(3);
        LBAR();
        issue_b(4);
      } else {
        LBAR(); LBAR();
#pragma unroll 1
        for (unsigned spins = 0; spins < SPIN_CAP; ++spins) if (rt_ok()) break;
        issue_b(8);
      }
      LSTAMP();
      if (b == 0 && lane == 0) gst64_sc1(0, ws + WS_EPOCH, tagv);
      LBAR();
      if (isq) LBAR();
      WAITV(0);
      LBAR();
      const int ntot = isq ? NQ2 + 10 : NH2;
#pragma unroll 1
      for (int s = 0; s < ntot - 8; ++s) {
        if (isq && s == NQ2) LBAR();
        WAITV(40);
        LBAR();
        issue_b(1);
      }
      LBAR(); LBAR();
      WAITV(0);
      if (isq) {
        const char* pf = ws + WS_PF + (rt2 * 8 + ct) * 128;
        u32x2 f = gld64_sc1((lane & 7) * 8, pf);
        LBAR();
        asm volatile("s_waitcnt vmcnt(0)" : "+v"(f)::"memory");
        LBAR();
        if (!__all(f[0] == tlo && f[1] == thi)) poll_words(pf, (lane & 7) * 8, tlo, thi);
        bsrc = ws + WS_P + (rt2 * 8 + ct) * 40960;
        issue_b(1); LBAR(); issue_b(1); LBAR(); issue_b(2); LBAR(); LBAR();
      }
      WAITV(0);
      LBAR();
    } else if (L == 0) {
      const char* asrc = ws + WS_A + (rt2 * 8 * 74 + k0) * 1024;
      unsigned al = L_AR;
      unsigned ao[8];
#pragma unroll
      for (int i = 0; i < 8; ++i) ao[i] = boff + i * 74 * 1024;
      auto issue_a = [&]() {
        asm volatile(
            "s_mov_b32 m0, %9\n\ts_nop 0\n\tglobal_load_lds_dwordx4 %0, %8 sc1\n\tglobal_load_lds_dwordx4 %0, %8 offset:1024 sc1\n\t"
            "s_add_u32 m0, m0, 2048\n\ts_nop 0\n\tglobal_load_lds_dwordx4 %1, %8 sc1\n\tglobal_load_lds_dwordx4 %1, %8 offset:1024 sc1\n\t"
            "s_add_u32 m0, m0, 2048\n\ts_nop 0\n\tglobal_load_lds_dwordx4 %2, %8 sc1\n\tglobal_load_lds_dwordx4 %2, %8 offset:1024 sc1\n\t"
            "s_add_u32 m0, m0, 2048\n\ts_nop 0\n\tglobal_load_lds_dwordx4 %3, %8 sc1\n\tglobal_load_lds_dwordx4 %3, %8 offset:1024 sc1\n\t"
            "s_add_u32 m0, m0, 2048\n\ts_nop 0\n\tglobal_load_lds_dwordx4 %4, %8 sc1\n\tglobal_load_lds_dwordx4 %4, %8 offset:1024 sc1\n\t"
            "s_add_u32 m0, m0, 2048\n\ts_nop 0\n\tglobal_load_lds_dwordx4 %5, %8 sc1\n\tglobal_load_lds_dwordx4 %5, %8 offset:1024 sc1\n\t"
            "s_add_u32 m0, m0, 2048\n\ts_nop 0\n\tglobal_load_lds_dwordx4 %6, %8 sc1\n\tglobal_load_lds_dwordx4 %6, %8 offset:1024 sc1\n\t"
            "s_add_u32 m0, m0, 2048\n\ts_nop 0\n\tglobal_load_lds_dwordx4 %7, %8 sc1\n\tglobal_load_lds_dwordx4 %7, %8 offset:1024 sc1"
            ::"v"(ao[0]), "v"(ao[1]), "v"(ao[2]), "v"(ao[3]), "v"(ao[4]), "v"(ao[5]), "v"(ao[6]), "v"(ao[7]), "s"(asrc), "s"(al)
            : "memory", "scc");
        asrc += 2048;
        al = al == L_AR + 49152 ? L_AR : al + 16384;
      };
      WAITV(0);
      LBAR();
      const char* fl = ws + WS_HF + rt2 * 128;
      unsigned fo = (lane & 7) * 8;
      int n = NH2;
      if (isq) {
        unsigned ia[7];
#pragma unroll
        for (int i = 0; i < 7; ++i) ia[i] = *(LAS unsigned*)(size_t)(L_TAB + i * 256 + lane * 4);
        unsigned my1 = *(LAS unsigned*)(size_t)(L_TAB + (rt < 7 ? rt : rt - 7) * 256 + lane * 4) == L_ONE + 16 ? 0u : ~0u;
#pragma unroll
        for (int i = 0; i < 7; ++i) PIN(ia[i]);
        PIN(my1);
        LBAR(); LSTAMP(); LBAR();
        u32x2 f = gld64_sc1(fo, fl);
        __builtin_amdgcn_s_setprio(3);
        LBAR();
        LSTAMP();
        asm volatile("s_waitcnt vmcnt(0)" : "+v"(f)::"memory");
        if (!__all(f[0] == tlo && f[1] == thi)) poll_words(fl, fo, tlo, thi);
        issue_a(); issue_a();
        LSTAMP();
        if (rt < 14) {
          const char* pb = ws + WS_PART + jg * 16 * GR_BYTES + rt * 1024;
          u32x4 pv[16];
#pragma unroll 1
          for (unsigned spins = 0; spins < SPIN_CAP; ++spins) {
#pragma unroll
            for (int i = 0; i < 16; ++i) pv[i] = gld128_sc1(boff, pb + i * GR_BYTES);
            asm volatile("s_waitcnt vmcnt(0)"
                         : "+v"(pv[0]), "+v"(pv[1]), "+v"(pv[2]), "+v"(pv[3]), "+v"(pv[4]), "+v"(pv[5]), "+v"(pv[6]), "+v"(pv[7]),
                           "+v"(pv[8]), "+v"(pv[9]), "+v"(pv[10]), "+v"(pv[11]), "+v"(pv[12]), "+v"(pv[13]), "+v"(pv[14]), "+v"(pv[15])::"memory");
            LSTAMP();
            unsigned bad = 0;
#pragma unroll
            for (int i = 0; i < 16; ++i) bad |= (pv[i][1] ^ tlo) | (pv[i][3] ^ thi);
            if (__all((bad & my1) == 0)) break;
          }
          float s[4] = {0.f, 0.f, 0.f, 0.f};
#pragma unroll
          for (int i = 0; i < 16; ++i) {
            s[0] += __builtin_bit_cast(float, pv[i][0] << 16);
            s[1] += __builtin_bit_cast(float, pv[i][0] & 0xffff0000u);
            s[2] += __builtin_bit_cast(float, pv[i][2] << 16);
            s[3] += __builtin_bit_cast(float, pv[i][2] & 0xffff0000u);
          }
          gst128_sc1(boff, ws + WS_RED + jg * GR_BYTES + rt * 1024, (u32x4){pk2(s[0], s[1]), tlo, pk2(s[2], s[3]), thi});
        }
        LSTAMP();
        issue_a(); issue_a();
        LSTAMP();
        {
          const char* rb = ws + WS_RED + jg * GR_BYTES;
          u32x4 pv[14];
#pragma unroll 1
          for (unsigned spins = 0; spins < SPIN_CAP; ++spins) {
#pragma unroll
            for (int i = 0; i < 14; ++i) pv[i] = gld128_sc1(boff, rb + i * 1024);
            asm volatile("s_waitcnt vmcnt(0)"
                         : "+v"(pv[0]), "+v"(pv[1]), "+v"(pv[2]), "+v"(pv[3]), "+v"(pv[4]), "+v"(pv[5]), "+v"(pv[6]), "+v"(pv[7]),
                           "+v"(pv[8]), "+v"(pv[9]), "+v"(pv[10]), "+v"(pv[11]), "+v"(pv[12]), "+v"(pv[13])::"memory");
            LSTAMP();
            unsigned bad = 0;
#pragma unroll
            for (int i = 0; i < 14; ++i) {
              asm volatile("ds_write2_b32 %0, %1, %2 offset0:%3 offset1:%4" ::"v"(ia[i % 7] + (i / 7) * 3360), "v"(pv[i][0]), "v"(pv[i][2]), "i"(0), "i"(1) : "memory");
              bad |= (pv[i][1] ^ tlo) | (pv[i][3] ^ thi);
            }
            if (__all(bad == 0)) break;
          }
        }
        BAR();
        LSTAMP();
#pragma unroll 1
        for (int s = 0; s < NQ2 - 4; ++s) { WAITV(32); LBAR(); issue_a(); }
        WAITV(32); LBAR();
        WAITV(16); LBAR();
        WAITV(0); LBAR();
        LBAR();
        fl = ws + WS_OF + rt2 * 512;
        fo = lane * 8;
        n = 10;
      } else {
        LBAR(); LSTAMP(); LBAR();
        if (lane == 0) gst64_sc1(rt * 128 + jg * 8, ws + WS_HF, tagv);
      }
      LSTAMP();
      poll_words(fl, fo, tlo, thi);
      LSTAMP();
#pragma unroll 1
      for (int s = -4; s < n - 4; ++s) {
        if (s >= -1) { WAITV(32); LBAR(); LSTAMP(); }
        issue_a();
      }
      WAITV(32); LBAR(); LSTAMP();
      WAITV(16); LBAR(); LSTAMP();
      WAITV(0); LBAR(); LSTAMP();
    } else {
      WAITV(0);
      LBAR();
    }
    return;
  }

  const int mw = w & 3, nw = w >> 2;
  unsigned warm = 0;
  if (w == 0) {
    const unsigned o = ((idx * 8 + (lane & 7)) * 128);
    asm volatile("global_load_dword %0, %1, %2" : "=v"(warm) : "v"(o < CODE_BYTES ? o : 0), "s"(pc & ~127ull) : "memory");
  }
  if (tid < 128) sts128(L_ZERO + tid * 16, (u32x4){0u, 0u, 0u, 0u});
  if (tid < 448) {
    const int hd = tid >= 224 ? 1 : 0, j = tid - 224 * hd, r = j - 160;
    const int ft = r >= 40 ? 2 : (r >= 20 ? 1 : 0), r2 = r - 20 * ft, gq = (r2 * 13) >> 6;
    const int d = j < 160 ? (j & 15) : 16 + r2 - 5 * gq, quad = j < 160 ? (j >> 4) : 4 * ft + gq;
    *(LAS unsigned*)(size_t)(L_TAB + tid * 4) = j < 210 ? L_KV + hd * 1680 + d * 80 + quad * 8 : L_ONE + 16;
  }
  if (tid == 128) sts128(L_ONE, (u32x4){0x3f803f80u, 0x3f803f80u, 0x3f803f80u, 0x3f803f80u});
  u64 epoch;
  asm volatile("s_load_dwordx2 %0, %1, 0x0\n\ts_waitcnt lgkmcnt(0)" : "=s"(epoch) : "s"(ws) : "memory");
  const u64 tag = epoch * 6364136223846793005ull + 1442695040888963407ull;
  const unsigned tlo = (unsigned)tag, thi = (unsigned)(tag >> 32);
  const u32x2 tagv = {tlo, thi};

  const unsigned xa = L_S0 + (32 * mw + c16) * 128 + ((g ^ ((c16 >> 1) & 7)) << 4);
  const unsigned wrd = L_S0 + 16384 + (4 * g + q4) * 480 + (isq ? 40 * (p4 & 1) + 240 * (p4 >> 1) : 8 * p4);
  const bool gatew = !isq && jg == 7 && nw == 1;
  const bool wide = nw == 0 && jg < 6;
  f32x4 acc[8][2];
  unsigned pbase = (p4 == g) ? L_WPHI + (2 * nw + (g & 1)) * 800 + q4 * 40 : L_ZERO;
  unsigned phiw = L_PHIK + (32 * mw + 16 * (g >> 1) + c16) * 352 + 80 * (2 * nw + (g & 1));
  unsigned vw = L_V + (32 * mw + c16) * 160 + 40 * g + 24 * nw;
  unsigned ab = L_PHIK + (4 * g + q4) * 352 + 80 * mw + 8 * p4;
  const bool vreal = nw == 0 || p4 == 0;
  unsigned vb = vreal ? L_V + (4 * g + q4) * 160 + 40 * mw + 32 * nw + 8 * p4 : L_ONE;
  unsigned po = (mw * 224 + (nw == 0 ? lane : 160 + 5 * g + c16)) * 16;
  unsigned ab2 = ab + 64 - 8 * (p4 & 2), vs = vreal ? 2560 : 0;
  PIN(pbase); PIN(phiw); PIN(vw); PIN(ab); PIN(vb); PIN(po); PIN(ab2); PIN(vs);
  STAMP(1);
  if (isq) {
    if (nw == 0) gemm1_loop<8, true, false>(acc, xa, wrd, 0, 0);
    else gemm1_loop<7, true, false>(acc, xa, wrd, 0, 0);
  } else {
    const unsigned wh = wrd + (nw == 0 ? 0 : (jg < 6 ? 256 : 192));
    if (wide) gemm1_loop<8, false, false>(acc, xa, wh, 0, 0);
    else if (gatew) gemm1_loop<7, false, true>(acc, xa, wh, L_GATE + g * 16 + rot * 128, 2 * rot);
    else gemm1_loop<6, false, false>(acc, xa, wh, 0, 0);
  }
  STAMP(2);
  asm volatile("s_waitcnt vmcnt(0)" : "+v"(warm)::"memory");

  unsigned kq[10];
  if (!isq) {
    const int k0 = (hstart >> 6) + (nw == 0 ? 0 : (jg < 6 ? 4 : 3));
    char* abase = ws + WS_A + ((rt * 8 + 2 * mw) * 74 + k0) * 1024;
#pragma unroll
    for (int pr = 0; pr < 4; ++pr) {
      if (pr < 3 || wide) {
        const u32x4 f0 = silu8(acc[2 * pr][0], acc[2 * pr + 1][0]), f1 = silu8(acc[2 * pr][1], acc[2 * pr + 1][1]);
        gst128_sc1(boff, abase + pr * 1024, f0);
        gst128_sc1(boff, abase + (74 + pr) * 1024, f1);
      }
    }
    if (gatew && g == 0) {
#pragma unroll
      for (int mt = 0; mt < 2; ++mt) {
        const float gv = __builtin_amdgcn_rcpf(1.f + __builtin_amdgcn_exp2f(acc[6][mt][0] * -1.4426950408889634f));
        gst32_sc1((rt * 128 + 32 * mw + 16 * mt + c16) * 4, ws + WS_G, __builtin_bit_cast(unsigned, gv));
      }
    }
    STAMP(3);
    WAITV(0);
    BAR();
    STAMP(4);
#pragma unroll
    for (int i = 0; i < 10; ++i) kq[i] = 0;
  } else {
    WPhi wf;
    wphi_read(wf, pbase);
    SB();
    unsigned kk[10];
#pragma unroll
    for (int i = 0; i < 10; ++i) {
      const f32x4 a = acc[i >> 1][0], c = acc[i >> 1][1];
      kq[i] = pk2(a[2 * (i & 1)], a[2 * (i & 1) + 1]); kk[i] = pk2(c[2 * (i & 1)], c[2 * (i & 1) + 1]);
    }
    SB();
#pragma unroll
    for (int i = 0; i < 10; ++i) {
      const auto r = __builtin_amdgcn_permlane32_swap(kq[i], kk[i], false, false);
      kq[i] = r[0]; kk[i] = r[1];
    }
    SB();
    f32x4 pp[5];
    phi_proj(wf, kk, pp);
    SB();
#pragma unroll
    for (int i = 5; i < 8; ++i) {
      if (i < 7 || nw == 0) {
#pragma unroll
        for (int mt = 0; mt < 2; ++mt) {
          const f32x4 a = acc[i][mt];
          sts64(vw + mt * 2560 + 8 * (i - 5), (u32x2){pk2(a[0], a[1]), pk2(a[2], a[3])});
        }
      }
    }
    SB();
    unsigned phi[20];
    dual_softmax(pp, phi);
#pragma unroll
    for (int i = 0; i < 5; ++i) sts128(phiw + 16 * i, (u32x4){phi[4 * i], phi[4 * i + 1], phi[4 * i + 2], phi[4 * i + 3]});
    unsigned va[8];
    va[0] = vb;
#pragma unroll
    for (int i = 1; i < 8; ++i) { va[i] = va[i - 1] + vs; PIN(va[i]); }
    BAR();
    STAMP(3);
    u32x4 vf[4], pf[4][3];
#pragma unroll
    for (int ks = 0; ks < 4; ++ks) {
      const unsigned o = ks * 32 * 352;
      vf[ks] = cat(ldstr(va[2 * ks]), ldstr(va[2 * ks + 1]));
      pf[ks][0] = cat(ldstr(ab + o), ldstr(ab + o + 16 * 352));
      pf[ks][1] = cat(ldstr(ab + o + 32), ldstr(ab + o + 32 + 16 * 352));
      pf[ks][2] = cat(ldstr(ab2 + o), ldstr(ab2 + o + 16 * 352));
      SB();
    }
    f32x4 kv[3];
#pragma unroll
    for (int i = 0; i < 3; ++i) kv[i] = (f32x4){0.f, 0.f, 0.f, 0.f};
#pragma unroll
    for (int ks = 0; ks < 4; ++ks)
#pragma unroll
      for (int i = 0; i < 3; ++i) kv[i] = mfma(pf[ks][i], vf[ks], kv[i]);
    char* pb = ws + WS_PART + (jg * 16 + rt) * GR_BYTES;
    const unsigned fs = nw == 0 ? 1024 : 320;
    if (nw == 0 || c16 < 5) {
      gst128_sc1(po, pb, (u32x4){pk2(kv[0][0], kv[0][1]), tlo, pk2(kv[0][2], kv[0][3]), thi});
      gst128_sc1(po, pb + fs, (u32x4){pk2(kv[1][0], kv[1][1]), tlo, pk2(kv[1][2], kv[1][3]), thi});
      if (g < 2) gst128_sc1(po, pb + 2 * fs, (u32x4){pk2(kv[2][0], kv[2][1]), tlo, pk2(kv[2][2], kv[2][3]), thi});
    }
    STAMP(4);
  }

  {
    P2 p;
    p.aa = L_AR + mw * 4096 + boff;
    p.ba = L_BR + nw * 3072 + boff;
    p.st = 0;
    f32x4 ob[2][3], oa[2][3];
#pragma unroll
    for (int i = 0; i < 6; ++i) { ob[i / 3][i % 3] = (f32x4){0.f, 0.f, 0.f, 0.f}; oa[i / 3][i % 3] = (f32x4){0.f, 0.f, 0.f, 0.f}; }
#define P2_FIRST() p2_first(p)
#define P2_RUN(O, N) p2_run(p, O, N, nw)
    const unsigned pofs = nw == 0 ? mw * 6 : 24 + mw * 4;
    if (!isq) {
#pragma unroll
      for (int i = 0; i < 6; ++i) PIN(ob[i / 3][i % 3]);
      BAR();
      STAMP(5);
      P2_FIRST();
      P2_RUN(ob, NH2);
      STAMP(9);
#pragma unroll
      for (int i = 0; i < 6; ++i) {
        PIN(ob[i / 3][i % 3]);
        if (i % 3 < 2 || nw == 0)
          gst128_sc1(boff, ws + WS_P + (rt2 * 8 + ct) * 40960 + (pofs + (i / 3) * (3 - nw) + i % 3) * 1024, __builtin_bit_cast(u32x4, ob[i / 3][i % 3]));
      }
      WAITV(0);
      if (lane == 0) gst64_sc1((rt2 * 8 + ct) * 128 + w * 8, ws + WS_PF, tagv);
      STAMP(10);
    } else {
      BAR();
      STAMP(5);
      unsigned phq[20];
      {
        WPhi wf;
        wphi_read(wf, pbase);
        SB();
        f32x4 pp[5];
        phi_proj(wf, kq, pp);
        dual_softmax(pp, phq);
      }
#pragma unroll
      for (int i = 0; i < 10; ++i) {
        const int a = i < 8 ? i : 8 + i, c = i < 8 ? i + 8 : 10 + i;
        const auto r = __builtin_amdgcn_permlane32_swap(phq[a], phq[c], false, false);
        phq[a] = r[0]; phq[c] = r[1];
      }
#pragma unroll
      for (int i = 0; i < 10; ++i) {
        const int a = i < 8 ? (i & 3) + 8 * (i >> 2) : 2 * i, c = i < 8 ? a + 4 : a + 1;
        const auto r = __builtin_amdgcn_permlane16_swap(phq[a], phq[c], false, false);
        phq[a] = r[0]; phq[c] = r[1];
      }
      unsigned k0a = L_KV + 2 * nw * 1680 + c16 * 80, k1a = L_KV + 2 * nw * 1680 + (p4 == 0 ? 16 + q4 : 20) * 80;
      unsigned k0b = k0a + 64 + 4 * g, k1b = k1a + 64 + 4 * g;
      k0a += 16 * g; k1a += 16 * g;
      unsigned ow = (g & 1) * 74 * 1024 + ((2 * (jg & 1) + nw) * 16 + c16) * 16 + (g >> 1) * 8;
      PIN(k0a); PIN(k1a); PIN(k0b); PIN(k1b); PIN(ow);
#pragma unroll
      for (int i = 0; i < 20; ++i) PIN(phq[i]);
#pragma unroll
      for (int i = 0; i < 6; ++i) { PIN(ob[i / 3][i % 3]); PIN(oa[i / 3][i % 3]); }
      STAMP(6);
      BAR();
      STAMP(7);
      {
        u32x4 a32[2][2];
        unsigned a16[2][2];
#pragma unroll
        for (int h = 0; h < 2; ++h) {
          a32[h][1] = lds128(k1a + h * 1680); a16[h][1] = *(LAS unsigned*)(size_t)(k1b + h * 1680);
          a32[h][0] = lds128(k0a + h * 1680); a16[h][0] = *(LAS unsigned*)(size_t)(k0b + h * 1680);
        }
        SB();
        f32x4 o[4][2];
#pragma unroll
        for (int t = 1; t >= 0; --t) {
#pragma unroll
          for (int u = 0; u < 4; ++u)
            o[u][t] = mfma(a32[u & 1][t], (u32x4){phq[4 * u], phq[4 * u + 1], phq[4 * u + 2], phq[4 * u + 3]}, (f32x4){0.f, 0.f, 0.f, 0.f});
#pragma unroll
          for (int u = 0; u < 4; ++u)
            o[u][t] = __builtin_amdgcn_mfma_f32_16x16x16bf16_1k(__builtin_bit_cast(s16x4, (u32x2){a16[u & 1][t], 0u}),
                                                                 __builtin_bit_cast(s16x4, (u32x2){phq[16 + u], 0u}), o[u][t], 0, 0, 0);
        }
        char* abase = ws + WS_A + ((rt * 8 + 2 * mw) * 74 + 54 + (jg >> 1) * 5) * 1024;
        float sc[4];
#pragma unroll
        for (int u = 0; u < 4; ++u) sc[u] = __builtin_amdgcn_rcpf(o[u][1][1] + 1e-6f);
        unsigned l0 = pk2(o[0][1][0] * sc[0], o[1][1][0] * sc[1]), l1 = pk2(o[2][1][0] * sc[2], o[3][1][0] * sc[3]);
#pragma unroll
        for (int mt = 0; mt < 2; ++mt) {
          const f32x4 a = o[2 * mt][0] * sc[2 * mt], c = o[2 * mt + 1][0] * sc[2 * mt + 1];
          gst128_sc1(boff, abase + (mt * 74 + 2 * (jg & 1) + nw) * 1024, (u32x4){pk2(a[0], a[1]), pk2(a[2], a[3]), pk2(c[0], c[1]), pk2(c[2], c[3])});
        }
        const auto r = __builtin_amdgcn_permlane16_swap(l0, l1, false, false);
        gst64_sc1(ow, abase + 4 * 1024, (u32x2){r[0], r[1]});
      }
      P2_FIRST();
      P2_RUN(ob, 1);
      WAITV(0);
      if (lane == 0) gst64_sc1(rt * 512 + (jg * 8 + w) * 8, ws + WS_OF, tagv);
      STAMP(8);
      unsigned gb[2];
#pragma unroll
      for (int mt = 0; mt < 2; ++mt) gb[mt] = gld32_sc1((rt2 * 128 + 32 * mw + 16 * mt + c16) * 4, ws + WS_G);
      P2_RUN(ob, NQ2 - 1);
      BAR();
      STAMP(9);
      P2_FIRST();
      P2_RUN(oa, 10);
      asm volatile("s_waitcnt vmcnt(0)" : "+v"(gb[0]), "+v"(gb[1])::"memory");
      BAR();
      STAMP(10);
      u32x4 pt[6];
#pragma unroll
      for (int i = 0; i < 6; ++i) {
        const unsigned tl = (NQ2 + 10) % 8 * 10 + pofs + ((i % 3 < 2 || nw == 0) ? (i / 3) * (3 - nw) + i % 3 : 0);
        pt[i] = lds128(L_BR + (tl >= 80 ? tl - 80 : tl) * 1024 + boff);
      }
      const unsigned oo = (rt2 * 128 + 32 * mw + c16) * 1280 + ct * 160 + nw * 96 + g * 8;
#pragma unroll
      for (int i = 0; i < 6; ++i) {
        const int mt = i / 3, n = i % 3;
        PIN(ob[mt][n]); PIN(oa[mt][n]);
        if (n < 2 || nw == 0) {
          const float gv = __builtin_bit_cast(float, gb[mt]), omg = 1.f - gv;
          const f32x4 r = (ob[mt][n] + __builtin_bit_cast(f32x4, pt[i])) * omg + oa[mt][n] * gv;
          gst64(oo + mt * 16 * 1280 + n * 32, out, (u32x2){pk2(r[0], r[1]), pk2(r[2], r[3])});
        }
      }
    }
  }
}

extern "C" void kernel_launch(void* const* d_in, const int* in_sizes, int n_in, void* d_out, int out_size, void* d_ws,
                              size_t ws_size, hipStream_t stream) {
  (void)in_sizes; (void)n_in; (void)out_size; (void)ws_size;
  (void)hipFuncSetAttribute((const void*)fused_kernel, hipFuncAttributeMaxDynamicSharedMemorySize, LDS_BYTES);
  fused_kernel<<<256, 768, LDS_BYTES, stream>>>((const char*)d_in[0], (const char*)d_in[1], (const char*)d_in[2],
                                                (const char*)d_in[3], (const char*)d_in[5], (const char*)d_in[6],
                                                (const char*)d_in[7], (char*)d_out, (char*)d_ws);
}
```

```cpp
#include <hip/hip_runtime.h>
#include <stdint.h>

typedef __attribute__((ext_vector_type(8))) __bf16 bf16x8;
typedef __attribute__((ext_vector_type(2))) __bf16 bf16x2;
typedef __attribute__((ext_vector_type(4))) short s16x4;
typedef __attribute__((ext_vector_type(4))) float f32x4;
typedef __attribute__((ext_vector_type(2))) float f32x2;
typedef __attribute__((ext_vector_type(4))) unsigned u32x4;
typedef __attribute__((ext_vector_type(2))) unsigned u32x2;
typedef unsigned long long u64;
#define LAS __attribute__((address_space(3)))
#define DI __device__ __forceinline__

constexpr unsigned WS_EPOCH = 0;
constexpr unsigned WS_RT = 1024;
constexpr unsigned WS_HF = 4096;
constexpr unsigned WS_OF = 12288;
constexpr unsigned WS_G = 20480;
constexpr unsigned WS_PF = 28672;
constexpr unsigned WS_RED = 49152;
constexpr unsigned GR_BYTES = 896 * 16;
constexpr unsigned WS_PART = WS_RED + 8 * GR_BYTES;
constexpr unsigned WS_B = 2u << 20;
constexpr unsigned WS_A = 5u << 20;
constexpr unsigned WS_P = 15u << 20;
constexpr unsigned WS_PROF = 24u << 20;
static_assert(WS_PART + 128 * GR_BYTES <= WS_B, "ws");

constexpr unsigned SLOT = 47104;
constexpr unsigned L_S1 = 0, L_S2 = SLOT, L_S0 = 2 * SLOT, L_MISC = 3 * SLOT;
constexpr unsigned L_PHIK = L_S2 + 40960;
constexpr unsigned L_V = L_PHIK + 128 * 352;
constexpr unsigned L_BR = 0;
constexpr unsigned L_AR = 81920;
constexpr unsigned L_KV = 147456;
constexpr unsigned L_WPHI = 154240;
constexpr unsigned L_GATE = L_WPHI;
constexpr unsigned L_ZERO = L_WPHI + 4096;
constexpr unsigned L_ONE = L_ZERO + 2048;
constexpr unsigned L_TAB = L_ONE + 32;
constexpr unsigned LDS_BYTES = 163840;
static_assert(L_ONE + 32 + 3360 <= LDS_BYTES, "lds");

#define WPOL ""
#define HOP1_SLEEP 8
#define HOP2_SLEEP 8
#define RT_SLEEP 0
#define OFL 0
#define TLBM 0
#define NQ2 7
#define SG 2
constexpr int NH2 = 27 - NQ2;
constexpr unsigned CODE_BYTES = 22528;
constexpr unsigned SPIN_CAP = 1u << 16;

DI unsigned pk2(float a, float b) {
  bf16x2 v = __builtin_convertvector((f32x2){a, b}, bf16x2);
  return __builtin_bit_cast(unsigned, v);
}
DI unsigned fbits(float f) { return __builtin_bit_cast(unsigned, f); }
DI float bitsf(unsigned u) { return __builtin_bit_cast(float, u); }
DI f32x4 mfma(u32x4 a, u32x4 b, f32x4 c) {
  return __builtin_amdgcn_mfma_f32_16x16x32_bf16(__builtin_bit_cast(bf16x8, a), __builtin_bit_cast(bf16x8, b), c, 0, 0, 0);
}
DI u32x4 lds128(unsigned a) { return *(LAS u32x4*)(size_t)a; }
DI u32x2 lds64(unsigned a) { return *(LAS u32x2*)(size_t)a; }
DI void sts128(unsigned a, u32x4 v) { *(LAS u32x4*)(size_t)a = v; }
DI void sts64(unsigned a, u32x2 v) { *(LAS u32x2*)(size_t)a = v; }
DI u32x2 ldstr(unsigned a) {
  return __builtin_bit_cast(u32x2, __builtin_amdgcn_ds_read_tr16_b64_v4i16((LAS s16x4*)(size_t)a));
}
DI u32x4 cat(u32x2 a, u32x2 b) { return (u32x4){a[0], a[1], b[0], b[1]}; }
DI void glds(unsigned voff, const void* sbase, unsigned lds) {
  asm volatile("s_mov_b32 m0, %2\n\ts_nop 0\n\tglobal_load_lds_dwordx4 %0, %1" ::"v"(voff), "s"(sbase), "s"(lds) : "memory");
}
DI void glds_nt(unsigned voff, const void* sbase, unsigned lds) {
  asm volatile("s_mov_b32 m0, %2\n\ts_nop 0\n\tglobal_load_lds_dwordx4 %0, %1 nt" ::"v"(voff), "s"(sbase), "s"(lds) : "memory");
}
DI void glds5_sc1(unsigned voff, unsigned voff1k, const void* sbase, unsigned lds) {
  asm volatile(
      "s_mov_b32 m0, %2\n\ts_nop 0\n\t"
      "global_load_lds_dwordx4 %0, %1 sc1\n\t"
      "global_load_lds_dwordx4 %0, %1 offset:1024 sc1\n\t"
      "global_load_lds_dwordx4 %0, %1 offset:2048 sc1\n\t"
      "global_load_lds_dwordx4 %0, %1 offset:3072 sc1\n\t"
      "s_add_u32 m0, m0, 1024\n\ts_nop 0\n\t"
      "global_load_lds_dwordx4 %3, %1 offset:3072 sc1" ::"v"(voff), "s"(sbase), "s"(lds), "v"(voff1k)
      : "memory", "scc");
}
DI u32x4 gld128_sc1(unsigned voff, const void* sbase) {
  u32x4 r;
  asm volatile("global_load_dwordx4 %0, %1, %2 sc1" : "=v"(r) : "v"(voff), "s"(sbase) : "memory");
  return r;
}
DI u32x2 gld64_sc1(unsigned voff, const void* sbase) {
  u32x2 r;
  asm volatile("global_load_dwordx2 %0, %1, %2 sc1" : "=v"(r) : "v"(voff), "s"(sbase) : "memory");
  return r;
}
DI unsigned gld32_sc1(unsigned voff, const void* sbase) {
  unsigned r;
  asm volatile("global_load_dword %0, %1, %2 sc1" : "=v"(r) : "v"(voff), "s"(sbase) : "memory");
  return r;
}
DI unsigned gld32(unsigned voff, const void* sbase) {
  unsigned r;
  asm volatile("global_load_dword %0, %1, %2" : "=v"(r) : "v"(voff), "s"(sbase) : "memory");
  return r;
}
DI void gst128_sc1(unsigned voff, void* sbase, u32x4 v) {
  asm volatile("global_store_dwordx4 %0, %1, %2 sc1\n\ts_nop 1" ::"v"(voff), "v"(v), "s"(sbase) : "memory");
}
DI void gst64_sc1(unsigned voff, void* sbase, u32x2 v) {
  asm volatile("global_store_dwordx2 %0, %1, %2 sc1" ::"v"(voff), "v"(v), "s"(sbase) : "memory");
}
DI void gst32_sc1(unsigned voff, void* sbase, unsigned v) {
  asm volatile("global_store_dword %0, %1, %2 sc1" ::"v"(voff), "v"(v), "s"(sbase) : "memory");
}
DI void gst64(unsigned voff, void* sbase, u32x2 v) {
  asm volatile("global_store_dwordx2 %0, %1, %2" ::"v"(voff), "v"(v), "s"(sbase) : "memory");
}
#define WAITV(n) asm volatile("s_waitcnt vmcnt(" #n ")" ::: "memory")
#define BAR() asm volatile("s_waitcnt lgkmcnt(0)\n\ts_barrier" ::: "memory")
#define PIN(x) asm volatile("" : "+v"(x))
#define SB() __builtin_amdgcn_sched_barrier(0)

DI void poll_words(const char* base, unsigned voff, unsigned tlo, unsigned thi) {
#pragma unroll 1
  for (unsigned spins = 0; spins < SPIN_CAP; ++spins) {
    u32x2 v = gld64_sc1(voff, base);
    asm volatile("s_waitcnt vmcnt(0)" : "+v"(v)::"memory");
    if (__all(v[0] == tlo && v[1] == thi)) break;
    __builtin_amdgcn_s_sleep(1);
  }
}

DI void dual_softmax(const f32x4 (&p)[5], unsigned (&o)[20]) {
  float mx = __builtin_fmaxf(p[0][0], p[0][1]), mn = __builtin_fminf(p[0][0], p[0][1]);
  float mx2 = __builtin_fmaxf(p[0][2], p[0][3]), mn2 = __builtin_fminf(p[0][2], p[0][3]);
#pragma unroll
  for (int t = 1; t < 5; ++t) {
    mx = __builtin_fmaxf(__builtin_fmaxf(mx, p[t][0]), p[t][1]); mn = __builtin_fminf(__builtin_fminf(mn, p[t][0]), p[t][1]);
    mx2 = __builtin_fmaxf(__builtin_fmaxf(mx2, p[t][2]), p[t][3]); mn2 = __builtin_fminf(__builtin_fminf(mn2, p[t][2]), p[t][3]);
  }
  const float L = 1.4426950408889634f;
  const float mxl = __builtin_fmaxf(mx, mx2) * -L, mnl = __builtin_fminf(mn, mn2) * L;
  f32x2 ep[10], en[10];
#pragma unroll
  for (int i = 0; i < 10; ++i) {
    const f32x2 v = {p[i >> 1][2 * (i & 1)], p[i >> 1][2 * (i & 1) + 1]};
    ep[i] = __builtin_elementwise_fma(v, (f32x2){L, L}, (f32x2){mxl, mxl});
    en[i] = __builtin_elementwise_fma(v, (f32x2){-L, -L}, (f32x2){mnl, mnl});
  }
  SB();
  f32x2 sp, sn;
#pragma unroll
  for (int i = 0; i < 10; ++i) {
    ep[i][0] = __builtin_amdgcn_exp2f(ep[i][0]); ep[i][1] = __builtin_amdgcn_exp2f(ep[i][1]);
    en[i][0] = __builtin_amdgcn_exp2f(en[i][0]); en[i][1] = __builtin_amdgcn_exp2f(en[i][1]);
    if (i == 1) { sp = ep[0]; sn = en[0]; }
    if (i > 1) { sp += ep[i - 1]; sn += en[i - 1]; }
    SB();
  }
  sp += ep[9]; sn += en[9];
  const float rp = __builtin_amdgcn_rcpf(sp[0] + sp[1]), rn = __builtin_amdgcn_rcpf(sn[0] + sn[1]);
  f32x2 rp2 = {rp, rp}, rn2 = {rn, rn};
  PIN(rp2); PIN(rn2);
#pragma unroll
  for (int i = 0; i < 10; ++i) { ep[i] *= rp2; en[i] *= rn2; }
  SB();
#pragma unroll
  for (int i = 0; i < 10; ++i) { o[i] = pk2(ep[i][0], ep[i][1]); o[10 + i] = pk2(en[i][0], en[i][1]); }
}
struct WPhi { u32x4 a0[5], a1[5]; u32x2 a2[5]; };
DI void wphi_read(WPhi& f, unsigned pbase) {
#pragma unroll
  for (int t = 0; t < 5; ++t) f.a0[t] = cat(ldstr(pbase + 8 * t), ldstr(pbase + 8 * t + 160));
#pragma unroll
  for (int t = 0; t < 5; ++t) f.a1[t] = cat(ldstr(pbase + 8 * t + 320), ldstr(pbase + 8 * t + 480));
#pragma unroll
  for (int t = 0; t < 5; ++t) f.a2[t] = ldstr(pbase + 8 * t + 640);
}
DI void phi_proj(const WPhi& f, const unsigned (&kq)[10], f32x4 (&pp)[5]) {
  const u32x4 f0 = {kq[0], kq[1], kq[2], kq[3]}, f1 = {kq[4], kq[5], kq[6], kq[7]};
  const u32x2 f2 = {kq[8], kq[9]};
#pragma unroll
  for (int t = 0; t < 5; ++t) pp[t] = mfma(f.a0[t], f0, (f32x4){0.f, 0.f, 0.f, 0.f});
#pragma unroll
  for (int t = 0; t < 5; ++t) pp[t] = mfma(f.a1[t], f1, pp[t]);
#pragma unroll
  for (int t = 0; t < 5; ++t)
    pp[t] = __builtin_amdgcn_mfma_f32_16x16x16bf16_1k(__builtin_bit_cast(s16x4, f.a2[t]), __builtin_bit_cast(s16x4, f2), pp[t], 0, 0, 0);
}
DI u32x4 silu8(f32x4 a, f32x4 c) {
  const float NL = -1.4426950408889634f;
  f32x2 v[4] = {{a[0], a[1]}, {a[2], a[3]}, {c[0], c[1]}, {c[2], c[3]}}, e[4];
#pragma unroll
  for (int i = 0; i < 4; ++i) e[i] = v[i] * (f32x2){NL, NL};
#pragma unroll
  for (int i = 0; i < 4; ++i) { e[i][0] = __builtin_amdgcn_exp2f(e[i][0]); e[i][1] = __builtin_amdgcn_exp2f(e[i][1]); }
#pragma unroll
  for (int i = 0; i < 4; ++i) e[i] += (f32x2){1.f, 1.f};
#pragma unroll
  for (int i = 0; i < 4; ++i) { e[i][0] = __builtin_amdgcn_rcpf(e[i][0]); e[i][1] = __builtin_amdgcn_rcpf(e[i][1]); }
#pragma unroll
  for (int i = 0; i < 4; ++i) v[i] *= e[i];
  return (u32x4){pk2(v[0][0], v[0][1]), pk2(v[1][0], v[1][1]), pk2(v[2][0], v[2][1]), pk2(v[3][0], v[3][1])};
}

DI void amfma(f32x4& c, const u32x4& a, const u32x4& b) { asm volatile("v_mfma_f32_16x16x32_bf16 %0, %1, %2, %0" : "+v"(c) : "v"(a), "v"(b)); }
template <int OFF> DI void trlo(u32x4& f, unsigned a) { u32x2 t; asm volatile("ds_read_b64_tr_b16 %0, %1 offset:%2" : "=v"(t) : "v"(a), "i"(OFF)); f[0] = t[0]; f[1] = t[1]; }
template <int OFF> DI void trhi(u32x4& f, unsigned a) { u32x2 t; asm volatile("ds_read_b64_tr_b16 %0, %1 offset:%2" : "=v"(t) : "v"(a), "i"(OFF)); f[2] = t[0]; f[3] = t[1]; }
template <int OFF> DI void rd128(u32x4& f, unsigned a) { asm volatile("ds_read_b128 %0, %1 offset:%2" : "=v"(f) : "v"(a), "i"(OFF)); }
DI void vadd(unsigned& v, unsigned s) { asm volatile("v_add_u32 %0, %1, %0" : "+v"(v) : "s"(s)); }
#define WL(n) asm volatile("s_waitcnt lgkmcnt(" #n ")")
#define MFMA_DONE() asm volatile("s_waitcnt lgkmcnt(0)\n\ts_nop 7\n\ts_nop 3" ::: "memory")

template <int NT, bool Q, bool GATE>
DI void gemm1_loop(f32x4 (&acc)[8][2], unsigned xa, unsigned wa, unsigned wb, unsigned gk) {
  u32x4 W[8], X0[2], X1[2];
  unsigned xb = xa ^ 64;
#pragma unroll
  for (int t = 0; t < NT; ++t) { acc[t][0] = (f32x4){0.f, 0.f, 0.f, 0.f}; acc[t][1] = (f32x4){0.f, 0.f, 0.f, 0.f}; PIN(acc[t][0]); PIN(acc[t][1]); }
  BAR();
#define TO(i) (Q ? ((i) < 5 ? (NT == 8 ? 0 : 80) + 8 * (i) : 160 + 8 * ((i) - (NT == 8 ? 5 : 2))) : 32 * (i))
#define AD(i) wa
#define RL(i, off) if constexpr (GATE && (i) == 6) { rd128<0>(W[i], wb); } else { trlo<TO(i) + (off)>(W[i], AD(i)); }
#define RH(i, off) if constexpr (GATE && (i) == 6) { gk = gk == 19 ? 0 : gk + 1; vadd(wb, gk == 0 ? -1216 : 64); } else { trhi<TO(i) + (off) + 7680>(W[i], AD(i)); }
#define M0(i, X) amfma(acc[i][0], W[i], X[0]);
#define M1(i, X) amfma(acc[i][1], W[i], X[1]);
#define G1(i, j) M0(i, X0) RL(j, 15360) M1(i, X0) RH(j, 15360)
#define G1X(i, j, h) M0(i, X0) RL(j, 15360) rd128<2048 * (h)>(X1[h], xb); M1(i, X0) RH(j, 15360)
  rd128<0>(X0[0], xa); rd128<2048>(X0[1], xa);
  RL(0, 0) RH(0, 0) RL(1, 0) RH(1, 0) RL(2, 0) RH(2, 0) RL(3, 0) RH(3, 0) RL(4, 0) RH(4, 0)
  if constexpr (NT > 6) { RL(5, 0) RH(5, 0) }
  if constexpr (NT > 7) { RL(6, 0) RH(6, 0) }
  unsigned sb = L_S0;
#pragma unroll 1
  for (int t = 0; t < 10; ++t) {
    if constexpr (NT == 8) {
      WL(10); M0(0, X0) RL(7, 0) M1(0, X0) RH(7, 0) G1(1, 0)
      WL(10); G1(2, 1) G1(3, 2)
      WL(10); G1X(4, 3, 0) G1X(5, 4, 1)
      WL(12); G1(6, 5) G1(7, 6)
      WL(5);
    } else if constexpr (NT == 7) {
      WL(8); M0(0, X0) RL(6, 0) M1(0, X0) RH(6, 0) G1(1, 0)
      WL(7); G1(2, 1) G1X(3, 2, 0)
      WL(8); G1X(4, 3, 1) G1(5, 4)
      WL(12); G1(6, 5)
      WL(5);
    } else {
      WL(6); M0(0, X0) RL(5, 0) M1(0, X0) RH(5, 0) G1(1, 0)
      WL(6); G1X(2, 1, 0) G1X(3, 2, 1)
      WL(8); G1(4, 3) G1(5, 4)
      WL(4);
    }
    M0(0, X1) RL(NT - 1, 15360) M1(0, X1) RH(NT - 1, 15360)
    const unsigned sn = sb == L_S0 ? L_S1 : sb + SLOT, dl = sn - sb;
    sb = sn;
    M0(1, X1) vadd(xa, dl); M1(1, X1) vadd(wa, dl);
    M0(2, X1) vadd(xb, dl); M1(2, X1)
    BAR();
    M0(3, X1) rd128<0>(X0[0], xa); rd128<2048>(X0[1], xa); M1(3, X1) RL(0, 0) RH(0, 0)
    M0(4, X1) RL(1, 0) RH(1, 0) M1(4, X1) RL(2, 0) RH(2, 0)
    if constexpr (NT == 8) {
      M0(5, X1) RL(3, 0) RH(3, 0) M1(5, X1) RL(4, 0)
      M0(6, X1) RH(4, 0) RL(5, 0) M1(6, X1) RH(5, 0)
      M0(7, X1) RL(6, 0) M1(7, X1) RH(6, 0)
    } else if constexpr (NT == 7) {
      M0(5, X1) RL(3, 0) RH(3, 0) M1(5, X1) RL(4, 0) RH(4, 0)
      M0(6, X1) RL(5, 0) M1(6, X1) RH(5, 0)
    } else {
      M0(5, X1) RL(3, 0) RH(3, 0) M1(5, X1) RL(4, 0) RH(4, 0)
    }
  }
  MFMA_DONE();
#undef TO
#undef AD
#undef RL
#undef RH
#undef M0
#undef M1
#undef G1
#undef G1X
}

struct P2 {
  u32x4 A[2], B[3];
  unsigned aa, ba;
  unsigned st;
};
DI void p2_first(P2& p) {
  asm volatile("ds_read_b128 %0, %5\n\tds_read_b128 %2, %6\n\tds_read_b128 %3, %6 offset:1024\n\tds_read_b128 %4, %6 offset:2048\n\t"
               "ds_read_b128 %1, %5 offset:2048\n\ts_waitcnt lgkmcnt(0)"
               : "=&v"(p.A[0]), "=&v"(p.A[1]), "=&v"(p.B[0]), "=&v"(p.B[1]), "=&v"(p.B[2]) : "v"(p.aa), "v"(p.ba) : "memory");
}
#define MF(d, a, b) "v_mfma_f32_16x16x32_bf16 %" #d ", %" #a ", %" #b ", %" #d "\n\t"
#define RD(d, a, o) "ds_read_b128 %" #d ", %" #a " offset:" #o "\n\t"
DI void p2_run(P2& p, f32x4 (&o)[2][3], int n, int nw) {
  u32x4 A0, A1, B0, B1, B2;
  unsigned da, db;
  asm volatile(
      "s_cmp_lg_u32 %22, 0\n\ts_cbranch_scc1 2f\n"
      "1:\n\t"
      "s_add_u32 %18, %18, 1\n\ts_and_b32 %20, %18, 3\n\ts_cselect_b32 %20, 16384, %23\n\t"
      "s_waitcnt lgkmcnt(2)\n\t" MF(0, 8, 6) RD(11, 16, 1024)
      "s_and_b32 %21, %18, 7\n\t" MF(1, 9, 6) RD(13, 17, 5120)
      "s_waitcnt lgkmcnt(2)\n\t" MF(2, 10, 6) RD(14, 17, 6144)
      "s_cselect_b32 %21, 10240, %24\n\t" MF(3, 8, 7) RD(15, 17, 7168)
      MF(4, 9, 7) RD(12, 16, 3072)
      MF(5, 10, 7) "v_add_u32 %16, %20, %16\n\t"
      "s_waitcnt lgkmcnt(2)\n\t" MF(0, 13, 11) "v_add_u32 %17, %21, %17\n\t"
      MF(1, 14, 11)
      "s_waitcnt lgkmcnt(0)\n\ts_barrier\n\t"
      MF(2, 15, 11) RD(6, 16, 0) RD(8, 17, 0)
      MF(3, 13, 12) RD(9, 17, 1024) RD(10, 17, 2048)
      MF(4, 14, 12) RD(7, 16, 2048) "s_sub_u32 %19, %19, 1\n\t"
      MF(5, 15, 12)
      "s_cmp_lg_u32 %19, 0\n\ts_cbranch_scc1 1b\n\t"
      "s_branch 3f\n"
      "2:\n\t"
      "s_add_u32 %18, %18, 1\n\ts_and_b32 %20, %18, 3\n\ts_cselect_b32 %20, 16384, %23\n\t"
      "s_waitcnt lgkmcnt(1)\n\t" MF(0, 8, 6) RD(11, 16, 1024) RD(13, 17, 5120)
      "s_and_b32 %21, %18, 7\n\t" MF(1, 9, 6) RD(14, 17, 6144)
      "s_cselect_b32 %21, 10240, %24\n\ts_waitcnt lgkmcnt(3)\n\t" MF(3, 8, 7) RD(12, 16, 3072)
      MF(4, 9, 7) "v_add_u32 %16, %20, %16\n\tv_add_u32 %17, %21, %17\n\t"
      "s_waitcnt lgkmcnt(1)\n\t" MF(0, 13, 11)
      MF(1, 14, 11)
      "s_waitcnt lgkmcnt(0)\n\ts_barrier\n\t"
      RD(6, 16, 0) RD(8, 17, 0)
      MF(3, 13, 12) RD(9, 17, 1024) RD(7, 16, 2048) "s_sub_u32 %19, %19, 1\n\t"
      MF(4, 14, 12)
      "s_cmp_lg_u32 %19, 0\n\ts_cbranch_scc1 2b\n"
      "3:\n\t"
      "s_waitcnt lgkmcnt(0)\n\ts_nop 7"
      : "+v"(o[0][0]), "+v"(o[0][1]), "+v"(o[0][2]), "+v"(o[1][0]), "+v"(o[1][1]), "+v"(o[1][2]),
        "+v"(p.A[0]), "+v"(p.A[1]), "+v"(p.B[0]), "+v"(p.B[1]), "+v"(p.B[2]),
        "=&v"(A0), "=&v"(A1), "=&v"(B0), "=&v"(B1), "=&v"(B2),
        "+v"(p.aa), "+v"(p.ba), "+s"(p.st), "+s"(n), "=&s"(da), "=&s"(db)
      : "s"(nw), "s"(-49152), "s"(-71680)
      : "memory", "scc");
}
#undef MF
#undef RD

#define TRW 0
#define TRL 0
#define STAMP(i)

typedef __attribute__((ext_vector_type(16))) unsigned u32x16;
DI const char* mkptr(unsigned lo, unsigned hi) { return (const char*)(((u64)hi << 32) | lo); }

__global__ __launch_bounds__(768, 3) void fused_kernel(const char* a_x, const char* a_wqkv, const char* a_wphi, const char* a_wo,
                                                       const char* a_w1, const char* a_w2, const char* a_gate, char* a_out, char* a_ws) {
  extern __shared__ __attribute__((aligned(16))) char smem[];
  u32x16 ka;
  u64 kws, pc;
  asm volatile("s_load_dwordx16 %0, %3, 0x0\n\ts_load_dwordx2 %1, %3, 0x40\n\ts_getpc_b64 %2"
               : "=&s"(ka), "=&s"(kws), "=&s"(pc) : "s"(__builtin_amdgcn_kernarg_segment_ptr()));
  __builtin_amdgcn_sched_barrier(0);
#define ARGS()                                                                                                   \
  asm volatile("s_waitcnt lgkmcnt(0)" : "+s"(ka), "+s"(kws));                                                    \
  const char *x = mkptr(ka[0], ka[1]), *wqkv = mkptr(ka[2], ka[3]), *wphi = mkptr(ka[4], ka[5]), *wo = mkptr(ka[6], ka[7]), \
             *w1 = mkptr(ka[8], ka[9]), *w2 = mkptr(ka[10], ka[11]), *gate = mkptr(ka[12], ka[13]);              \
  char *out = (char*)mkptr(ka[14], ka[15]), *ws = (char*)kws;                                                    \
  (void)x; (void)wqkv; (void)wphi; (void)wo; (void)w1; (void)w2; (void)gate; (void)out;                          \
  u64 epoch;                                                                                                     \
  asm volatile("s_load_dwordx2 %0, %1, 0x0" : "=s"(epoch) : "s"(ws) : "memory")
#define TAG()                                                                                                    \
  asm volatile("s_waitcnt lgkmcnt(0)" : "+s"(epoch));                                                            \
  const u64 tag = epoch * 6364136223846793005ull + 1442695040888963407ull;                                       \
  const unsigned tlo = (unsigned)tag, thi = (unsigned)(tag >> 32);                                               \
  const u32x2 tagv = {tlo, thi}
  const int tid = threadIdx.x, lane = tid & 63;
  const int w = __builtin_amdgcn_readfirstlane(tid >> 6);
  const int b = blockIdx.x, xcd = b & 7, idx = b >> 3;
  const int rt = (xcd & 1) * 8 + (idx & 7);
  const int cb = idx >> 3, cq = xcd >> 1;
  const bool isq = cb < 2;
  const int jg = 2 * cq + (cb & 1);
  const int rt2 = (xcd >> 1) * 4 + ((idx >> 2) & 3), ct = (xcd & 1) * 4 + (idx & 3);
  const int g = lane >> 4, c16 = lane & 15, q4 = c16 >> 2, p4 = lane & 3;
  const int hstart = jg * 448 - (jg == 7 ? 64 : 0);
  const unsigned boff = lane * 16;
#define ROTM 6
  const int rl = idx & 7, xr = xcd & 1;
  const int rot = ROTM == 0 ? 0
                : ROTM == 1 ? ((rl ^ cb) & 1) * 5
                : ROTM == 3 ? ((rl ^ cb) & 1) * 5 + xr * 2 + (cq & 1)
                : ROTM == 4 ? ((((rl + cb) % 3) * 10) / 3 + xr * 2) % 10
                : ROTM == 5 ? ((((rl + cb) & 3) * 5) >> 1) + xr
                : ROTM == 6 ? ((rl ^ cb) & 1) * 5 + xr * 3
                : ROTM == 8 ? (xr * 5 + cq) % 10
                : ((rl + cb) % 5) * 2 + xr;

  if (__builtin_expect(w >= 8, 1)) {
    const int L = w - 8;
    const int ldw = isq ? 3840 : 3456;
    unsigned xo[4], wv[8];
    {
      const unsigned x0 = (rt * 128 + 8 * L + (lane >> 3)) * 1280 + (((lane & 7) ^ ((4 * L + (lane >> 4)) & 7)) << 4);
#pragma unroll
      for (int i = 0; i < 4; ++i) xo[i] = x0 + i * 32 * 1280;
      const int rs = lane >= 30 ? 1 : 0, pos = lane - 30 * rs;
      const int sec = pos < 10 ? 0 : (pos < 15 ? 2560 - 160 : (pos < 25 ? 1280 - 240 : 2560 - 320));
      const int col = isq ? sec + jg * 160 + pos * 16 : hstart + pos * 16;
      const unsigned w0 = (8 * (L >> 1) + 2 * (L & 1) + rs) * ldw + col;
#pragma unroll
      for (int i = 0; i < 8; ++i) wv[i] = w0 + (32 * (i >> 2) + 16 * (i & 1) + 4 * ((i >> 1) & 1)) * ldw;
    }
    const u64 half = isq ? 0x3fffffffull : (jg < 6 ? 0x0fffffffull : 0x00ffffffull);
    const u64 wmask = half | (half << 30);
#pragma unroll
    for (int i = 0; i < 4; ++i) PIN(xo[i]);
#pragma unroll
    for (int i = 0; i < 8; ++i) PIN(wv[i]);
    ARGS();
    const char* wbase = isq ? wqkv : w1;
    const char* xs = x + rot * 128;
    const char* wsrc = wbase + rot * 64 * ldw;
    int kst = rot;
    const unsigned mx0 = L * 1024, mw0 = 16384 + L * 960;
    auto issue_lo = [&](unsigned sb) {
      asm volatile(
          "s_mov_b32 m0, %10\n\ts_nop 0\n\tglobal_load_lds_dwordx4 %0, %8\n\t"
          "s_add_u32 m0, m0, 4096\n\ts_nop 0\n\tglobal_load_lds_dwordx4 %1, %8\n\t"
          "s_add_u32 m0, m0, 4096\n\ts_nop 0\n\tglobal_load_lds_dwordx4 %2, %8\n\t"
          "s_add_u32 m0, m0, 4096\n\ts_nop 0\n\tglobal_load_lds_dwordx4 %3, %8\n\t"
          "s_mov_b32 m0, %11\n\ts_mov_b64 exec, %12\n\tglobal_load_lds_dwordx4 %4, %9 " WPOL "\n\t"
          "s_add_u32 m0, m0, 3840\n\ts_nop 0\n\tglobal_load_lds_dwordx4 %5, %9 " WPOL "\n\t"
          "s_add_u32 m0, m0, 3840\n\ts_nop 0\n\tglobal_load_lds_dwordx4 %6, %9 " WPOL "\n\t"
          "s_add_u32 m0, m0, 3840\n\ts_nop 0\n\tglobal_load_lds_dwordx4 %7, %9 " WPOL "\n\t"
          "s_mov_b64 exec, -1"
          ::"v"(xo[0]), "v"(xo[1]), "v"(xo[2]), "v"(xo[3]), "v"(wv[0]), "v"(wv[1]), "v"(wv[2]), "v"(wv[3]),
            "s"(xs), "s"(wsrc), "s"(sb + mx0), "s"(sb + mw0), "s"(wmask)
          : "memory", "scc");
    };
    auto issue_hi = [&](unsigned sb) {
      asm volatile(
          "s_mov_b32 m0, %5\n\ts_mov_b64 exec, %6\n\tglobal_load_lds_dwordx4 %0, %4 " WPOL "\n\t"
          "s_add_u32 m0, m0, 3840\n\ts_nop 0\n\tglobal_load_lds_dwordx4 %1, %4 " WPOL "\n\t"
          "s_add_u32 m0, m0, 3840\n\ts_nop 0\n\tglobal_load_lds_dwordx4 %2, %4 " WPOL "\n\t"
          "s_add_u32 m0, m0, 3840\n\ts_nop 0\n\tglobal_load_lds_dwordx4 %3, %4 " WPOL "\n\t"
          "s_mov_b64 exec, -1"
          ::"v"(wv[4]), "v"(wv[5]), "v"(wv[6]), "v"(wv[7]), "s"(wsrc), "s"(sb + mw0 + 15360), "s"(wmask)
          : "memory", "scc");
      const bool wrap = ++kst == 10;
      xs = wrap ? x : xs + 128;
      wsrc = wrap ? wbase : wsrc + 64 * ldw;
    };
    auto issue_stage = [&](unsigned sb) { issue_lo(sb); issue_hi(sb); };
#define LSTAMP()
#define LBAR() asm volatile("s_barrier" ::: "memory")
#define STEP(N, SL) WAITV(N); LSTAMP(); LBAR(); LSTAMP(); issue_stage(SL); LSTAMP();
    LSTAMP();
    __builtin_amdgcn_s_setprio(3);
    issue_stage(L_S0);
    LSTAMP();
    __builtin_amdgcn_s_setprio(1);
    int lane2 = lane, unit = b * 3 + L;
    asm volatile("" : "+v"(lane2), "+s"(unit));
    const bool retile = (L < 3) && (unit < 740);
    const int rk = retile ? unit / 10 : 0, rcu = retile ? unit - rk * 10 : 0;
    const char* rsrc = rk < 54 ? w2 : wo;
    unsigned rv[4], ra[4], rd[4];
    {
      const int kp = rk - 54, P = kp / 5, s5 = kp - P * 5;
      const bool lft = rk >= 54 && s5 == 4;
      const int gs = lft ? 40 : 4;
      const int r0 = rk < 54 ? 32 * rk : (lft ? 160 * P + 16 : 160 * P + 40 * s5);
      const int rh = rk < 54 ? 16 : (lft ? 2 : 20);
      const int c = lane2 & 7, e = (lane2 >> 3) & 3, g2 = lane2 >> 4, q2 = (lane2 >> 2) & 3;
      const unsigned v0 = (r0 + (lane2 >> 5) * gs + (lft ? 20 * (e & 1) + (e >> 1) : e)) * 1280 + rcu * 128 + ((((c >> 1) ^ g2) << 5) | ((c & 1) << 4));
      const unsigned base = L_MISC + L * 4096 + (4 * g2 + q2) * 128 + 8 * (lane2 & 3);
      const int sw = (2 * g2 + (q2 >> 1)) & 3;
#pragma unroll
      for (int n = 0; n < 4; ++n) {
        const int N = 4 * rcu + n, c5 = N / 5;
        rv[n] = v0 + ((n & 1) * 2 * gs + (n >> 1) * rh) * 1280;
        ra[n] = base + ((n ^ sw) << 5);
        rd[n] = WS_B + ((c5 * 74 + rk) * 5 + (N - c5 * 5)) * 1024 + lane2 * 16;
        PIN(rv[n]); PIN(ra[n]); PIN(rd[n]);
      }
    }
    TAG();
    LSTAMP();
    issue_lo(L_S1);
    LSTAMP();
    WAITV(8); LSTAMP(); LBAR(); LSTAMP();
    __builtin_amdgcn_s_setprio(3);
    issue_hi(L_S1);
    __builtin_amdgcn_s_setprio(1);
    asm volatile("s_mov_b32 m0, %5\n\ts_mov_b64 exec, %6\n\tglobal_load_lds_dwordx4 %0, %4 nt\n\t"
                 "s_add_u32 m0, m0, %7\n\ts_nop 0\n\tglobal_load_lds_dwordx4 %1, %4 nt\n\t"
                 "s_add_u32 m0, m0, %7\n\ts_nop 0\n\tglobal_load_lds_dwordx4 %2, %4 nt\n\t"
                 "s_add_u32 m0, m0, %7\n\ts_nop 0\n\tglobal_load_lds_dwordx4 %3, %4 nt\n\ts_mov_b64 exec, -1"
                 ::"v"(rv[0]), "v"(rv[1]), "v"(rv[2]), "v"(rv[3]), "s"(rsrc), "s"(retile ? L_MISC + L * 4096 : L_ONE + 16),
                   "s"(retile ? ~0ull : 1ull), "s"(retile ? 1024 : 0) : "memory", "scc");
    issue_lo(L_S2);
#define HALF(PREV, NEXT) LSTAMP(); LBAR(); LSTAMP(); __builtin_amdgcn_s_setprio(3); issue_hi(PREV); __builtin_amdgcn_s_setprio(1); issue_lo(NEXT); LSTAMP();
    WAITV(12);
    HALF(L_S2, L_S0)
    WAITV(8);
    {
      u32x4 f[4];
#pragma unroll
      for (int n = 0; n < 4; ++n) f[n] = cat(ldstr(ra[n]), ldstr(ra[n] + 2048));
      LSTAMP(); LBAR(); LSTAMP();
      __builtin_amdgcn_s_setprio(3);
      issue_hi(L_S0);
      if (retile) {
#pragma unroll
        for (int n = 0; n < 4; ++n) gst128_sc1(rd[n], ws, f[n]);
      }
      __builtin_amdgcn_s_setprio(1);
      issue_lo(L_S1);
      LSTAMP();
      if (retile) { WAITV(12); } else { WAITV(8); }
      unsigned sp = L_S1, sb = L_S2;
#pragma unroll 1
      for (int t = 2; t < 7; ++t) {
        HALF(sp, sb)
        sp = sb;
        sb = sb == L_S0 ? L_S1 : sb + SLOT;
        if (t == 3 && L == 3 && lane == 0) gst64_sc1(b * 8, ws + WS_RT, tagv);
        WAITV(8);
      }
      LSTAMP(); LBAR(); LSTAMP();
      issue_hi(sp);
    }
    const int k0 = isq ? 54 - 2 * NQ2 : 0;
    const char* bsrc = ws + WS_B + (ct * 74 + k0) * 5120;
    const unsigned boff1k = boff + 1024;
    unsigned bl = L_BR;
    auto issue_b = [&](int n) {
#pragma unroll 1
      for (int j = 0; j < 2 * n; ++j) { glds5_sc1(boff, boff1k, bsrc, bl); bsrc += 5120; bl = bl == L_BR + 76800 ? L_BR : bl + 5120; }
    };
    __builtin_amdgcn_s_setprio(3);
    if (L == 1) {
      u32x4 r0, r1;
      asm volatile("global_load_dwordx4 %0, %2, %3 sc1\n\tglobal_load_dwordx4 %1, %2, %3 offset:16 sc1\n\ts_waitcnt vmcnt(2)\n\ts_barrier\n\ts_waitcnt vmcnt(0)"
                   : "=&v"(r0), "=&v"(r1) : "v"(lane * 32), "s"(ws + WS_RT) : "memory");
      const unsigned bad = (r0[0] ^ tlo) | (r0[1] ^ thi) | (r0[2] ^ tlo) | (r0[3] ^ thi) | (r1[0] ^ tlo) | (r1[1] ^ thi) | (r1[2] ^ tlo) | (r1[3] ^ thi);
      if (__all(bad == 0)) {
        issue_b(3);
        LBAR();
        issue_b(5);
      } else {
        LBAR();
#pragma unroll 1
        for (int i = 0; i < 4; ++i) poll_words(ws + WS_RT + i * 512, lane * 8, tlo, thi);
        issue_b(8);
      }
      if (b == 0 && lane == 0) gst64_sc1(0, ws + WS_EPOCH, tagv);
    } else {
      WAITV(0);
      LBAR();
      if (!isq && L >= 2) return;
      LBAR();
    }
    LSTAMP();
    const char* fl = ws + WS_HF + rt2 * 128;
    unsigned fo = (lane & 7) * 8;
    bool ok = false;
    if (isq) {
      unsigned ia[4];
#pragma unroll
      for (int i = 0; i < 4; ++i) {
        const int sl = L + 4 * i < 14 ? L + 4 * i : L;
        ia[i] = *(LAS unsigned*)(size_t)(L_TAB + (sl < 7 ? sl : sl - 7) * 256 + lane * 4) + (sl < 7 ? 0 : 3360);
        PIN(ia[i]);
      }
      unsigned my1 = *(LAS unsigned*)(size_t)(L_TAB + (rt < 7 ? rt : rt - 7) * 256 + (16 * L + c16) * 4) == L_ONE + 16 ? 0u : ~0u;
      unsigned h1o = g * 4 * GR_BYTES + c16 * 16;
      unsigned h2o = c16 * 16 + (g & 1) * 8, htag = (g & 1) ? thi : tlo;
      PIN(my1); PIN(h1o); PIN(h2o); PIN(htag);
      LBAR();
      WAITV(0);
      LBAR();
      LSTAMP();
      u32x4 pv[4];
      if (rt < 14) {
        const char* pb = ws + WS_PART + jg * 16 * GR_BYTES + rt * 1024 + L * 256;
        __builtin_amdgcn_s_sleep(HOP1_SLEEP);
#pragma unroll 1
        for (unsigned spins = 0; spins < SPIN_CAP; ++spins) {
#pragma unroll
          for (int i = 0; i < 4; ++i) pv[i] = gld128_sc1(h1o, pb + i * GR_BYTES);
          asm volatile("s_waitcnt vmcnt(0)" : "+v"(pv[0]), "+v"(pv[1]), "+v"(pv[2]), "+v"(pv[3])::"memory");
          LSTAMP();
          unsigned bad = 0;
#pragma unroll
          for (int i = 0; i < 4; ++i) bad |= (pv[i][1] ^ tlo) | (pv[i][3] ^ thi);
          if (__all((bad & my1) == 0)) break;
        }
        float s[4];
#pragma unroll
        for (int e = 0; e < 4; ++e) {
          float t[4];
#pragma unroll
          for (int i = 0; i < 4; ++i) t[i] = __builtin_bit_cast(float, (e & 1) ? pv[i][e & 2] & 0xffff0000u : pv[i][e & 2] << 16);
          s[e] = (t[0] + t[1]) + (t[2] + t[3]);
        }
        const auto ra = __builtin_amdgcn_permlane32_swap(fbits(s[0]), fbits(s[1]), false, false);
        const auto rc = __builtin_amdgcn_permlane32_swap(fbits(s[2]), fbits(s[3]), false, false);
        const auto rd = __builtin_amdgcn_permlane16_swap(fbits(bitsf(ra[0]) + bitsf(ra[1])), fbits(bitsf(rc[0]) + bitsf(rc[1])), false, false);
        unsigned u0 = fbits(bitsf(rd[0]) + bitsf(rd[1])), u1 = u0;
        PIN(u1);
        const auto re = __builtin_amdgcn_permlane32_swap(u0, u1, false, false);
        if (lane < 32) gst64_sc1(h2o, ws + WS_RED + jg * GR_BYTES + rt * 1024 + L * 256, (u32x2){pk2(bitsf(re[0]), bitsf(re[1])), htag});
      }
      LSTAMP();
      u32x2 f = gld64_sc1(fo, fl);
      __builtin_amdgcn_s_sleep(HOP2_SLEEP);
      const char* rb = ws + WS_RED + jg * GR_BYTES + L * 1024;
#pragma unroll 1
      for (unsigned spins = 0; spins < SPIN_CAP; ++spins) {
#pragma unroll
        for (int i = 0; i < 4; ++i) pv[i] = gld128_sc1(boff, rb + ((i < 3 || L < 2) ? i * 4096 : 0));
        asm volatile("s_waitcnt vmcnt(0)" : "+v"(pv[0]), "+v"(pv[1]), "+v"(pv[2]), "+v"(pv[3])::"memory");
        LSTAMP();
        unsigned bad = 0;
#pragma unroll
        for (int i = 0; i < 4; ++i) {
          asm volatile("ds_write2_b32 %0, %1, %2 offset0:0 offset1:1" ::"v"(ia[i]), "v"(pv[i][0]), "v"(pv[i][2]) : "memory");
          bad |= (pv[i][1] ^ tlo) | (pv[i][3] ^ thi);
        }
        if (__all(bad == 0)) break;
      }
      BAR();
      LSTAMP();
      PIN(f);
      ok = __all(f[0] == tlo && f[1] == thi);
    } else {
      LBAR();
      if (L == 0 && lane == 0) gst64_sc1(rt * 128 + jg * 8, ws + WS_HF, tagv);
    }
    if (L == 1) {
      WAITV(0);
      LBAR();
      const int ntot = isq ? NQ2 + 10 : NH2;
#pragma unroll 1
      for (int s = 0; s < ntot - 8; ++s) {
        if (isq && s == NQ2) LBAR();
        WAITV(40);
        LBAR();
        issue_b(1);
      }
      LBAR(); LBAR();
      WAITV(0);
      if (isq) {
        const char* pf = ws + WS_PF + (rt2 * 8 + ct) * 128;
        u32x2 f = gld64_sc1((lane & 7) * 8, pf);
        LBAR(); LBAR();
        asm volatile("s_waitcnt vmcnt(0)" : "+v"(f)::"memory");
        if (!__all(f[0] == tlo && f[1] == thi)) poll_words(pf, (lane & 7) * 8, tlo, thi);
        bsrc = ws + WS_P + (rt2 * 8 + ct) * 40960;
        issue_b(1); LBAR(); issue_b(1); LBAR(); issue_b(2); LBAR(); LBAR();
      }
      WAITV(0);
      LBAR();
    } else if (L == 0) {
      const char* asrc = ws + WS_A + (rt2 * 8 * 74 + k0) * 1024;
      unsigned al = L_AR;
      unsigned ao[8];
#pragma unroll
      for (int i = 0; i < 8; ++i) ao[i] = boff + i * 74 * 1024;
      int n = isq ? NQ2 : NH2, s0 = -4;
#pragma unroll 1
      for (int seg = isq ? 0 : 1; seg < 2; ++seg) {
        LSTAMP();
        if (!ok) poll_words(fl, fo, tlo, thi);
        LSTAMP();
#pragma unroll 1
        for (int s = -4; s < n - 4; ++s) {
          if (s == -2) {
            if (s0 == -2) LBAR();
            WAITV(16); LBAR(); LSTAMP();
          }
          if (s >= 0) { WAITV(32); LBAR(); LSTAMP(); }
          asm volatile(
              "s_mov_b32 m0, %9\n\ts_nop 0\n\tglobal_load_lds_dwordx4 %0, %8 sc1\n\tglobal_load_lds_dwordx4 %0, %8 offset:1024 sc1\n\t"
              "s_add_u32 m0, m0, 2048\n\ts_nop 0\n\tglobal_load_lds_dwordx4 %1, %8 sc1\n\tglobal_load_lds_dwordx4 %1, %8 offset:1024 sc1\n\t"
              "s_add_u32 m0, m0, 2048\n\ts_nop 0\n\tglobal_load_lds_dwordx4 %2, %8 sc1\n\tglobal_load_lds_dwordx4 %2, %8 offset:1024 sc1\n\t"
              "s_add_u32 m0, m0, 2048\n\ts_nop 0\n\tglobal_load_lds_dwordx4 %3, %8 sc1\n\tglobal_load_lds_dwordx4 %3, %8 offset:1024 sc1\n\t"
              "s_add_u32 m0, m0, 2048\n\ts_nop 0\n\tglobal_load_lds_dwordx4 %4, %8 sc1\n\tglobal_load_lds_dwordx4 %4, %8 offset:1024 sc1\n\t"
              "s_add_u32 m0, m0, 2048\n\ts_nop 0\n\tglobal_load_lds_dwordx4 %5, %8 sc1\n\tglobal_load_lds_dwordx4 %5, %8 offset:1024 sc1\n\t"
              "s_add_u32 m0, m0, 2048\n\ts_nop 0\n\tglobal_load_lds_dwordx4 %6, %8 sc1\n\tglobal_load_lds_dwordx4 %6, %8 offset:1024 sc1\n\t"
              "s_add_u32 m0, m0, 2048\n\ts_nop 0\n\tglobal_load_lds_dwordx4 %7, %8 sc1\n\tglobal_load_lds_dwordx4 %7, %8 offset:1024 sc1"
              ::"v"(ao[0]), "v"(ao[1]), "v"(ao[2]), "v"(ao[3]), "v"(ao[4]), "v"(ao[5]), "v"(ao[6]), "v"(ao[7]), "s"(asrc), "s"(al)
              : "memory", "scc");
          asrc += 2048;
          al = al == L_AR + 49152 ? L_AR : al + 16384;
        }
        fl = ws + WS_OF + rt2 * 512;
        fo = lane * 8;
        n = 10;
        u32x2 f;
        asm volatile("s_waitcnt vmcnt(32)\n\ts_barrier\n\tglobal_load_dwordx2 %0, %1, %2 sc1\n\ts_waitcnt vmcnt(17)\n\ts_barrier\n\t"
                     "s_waitcnt vmcnt(1)\n\ts_barrier\n\ts_waitcnt vmcnt(0)"
                     : "=&v"(f) : "v"(fo), "s"(fl) : "memory");
        LSTAMP();
        ok = __all(f[0] == tlo && f[1] == thi);
        if (ok) s0 = -2; else LBAR();
      }
    }
    return;
  }

  asm volatile("s_waitcnt lgkmcnt(0)" ::"s"(ka), "s"(kws));
  const int mw = w & 3, nw = w >> 2;
  const char *wphi = a_wphi, *gate = a_gate;
  char *out = a_out, *ws = a_ws;
  u64 epoch;
  asm volatile("s_load_dwordx2 %0, %1, 0x0\n\ts_waitcnt lgkmcnt(0)" : "=s"(epoch) : "s"(ws) : "memory");
  unsigned warm = 0;
  if (w == 4) {
    const unsigned o = ((idx * 8 + (lane & 7)) * 128);
    asm volatile("global_load_dword %0, %1, %2" : "=v"(warm) : "v"(o < CODE_BYTES ? o : 0), "s"(pc & ~127ull) : "memory");
  }
  {
    if (w == 3) {
      __builtin_amdgcn_s_setprio(3);
      const char* src = isq ? wphi + jg * 3200 : gate;
      const unsigned lim = isq ? 3184 : 1264;
#pragma unroll
      for (int n = 0; n < 4; ++n) {
        const unsigned o = n * 1024 + boff;
        glds(o < lim ? o : lim, src, L_WPHI + n * 1024);
      }
      __builtin_amdgcn_s_setprio(0);
    }
  }
  if (tid < 128) sts128(L_ZERO + tid * 16, (u32x4){0u, 0u, 0u, 0u});
  if (tid < 448) {
    const int hd = tid >= 224 ? 1 : 0, j = tid - 224 * hd, r = j - 160;
    const int ft = r >= 40 ? 2 : (r >= 20 ? 1 : 0), r2 = r - 20 * ft, gq = (r2 * 13) >> 6;
    const int d = j < 160 ? (j & 15) : 16 + r2 - 5 * gq, quad = j < 160 ? (j >> 4) : 4 * ft + gq;
    *(LAS unsigned*)(size_t)(L_TAB + tid * 4) = j < 210 ? L_KV + hd * 1680 + d * 80 + quad * 8 : L_ONE + 16;
  }
  if (tid == 128) sts128(L_ONE, (u32x4){0x3f803f80u, 0x3f803f80u, 0x3f803f80u, 0x3f803f80u});
  TAG();

  const unsigned xa = L_S0 + (32 * mw + c16) * 128 + ((g ^ ((c16 >> 1) & 7)) << 4);
  const unsigned wrd = L_S0 + 16384 + (4 * g + q4) * 480 + (isq ? 40 * (p4 & 1) + 240 * (p4 >> 1) : 8 * p4);
  const bool gatew = !isq && jg == 7 && nw == 1;
  const bool wide = nw == 0 && jg < 6;
  f32x4 acc[8][2];
  unsigned pbase = (p4 == g) ? L_WPHI + (2 * nw + (g & 1)) * 800 + q4 * 40 : L_ZERO;
  unsigned phiw = L_PHIK + (32 * mw + 16 * (g >> 1) + c16) * 352 + 80 * (2 * nw + (g & 1));
  unsigned vw = L_V + (32 * mw + c16) * 160 + 40 * g + 24 * nw;
  unsigned ab = L_PHIK + (4 * g + q4) * 352 + 80 * mw + 8 * p4;
  const bool vreal = nw == 0 || p4 == 0;
  unsigned vb = vreal ? L_V + (4 * g + q4) * 160 + 40 * mw + 32 * nw + 8 * p4 : L_ONE;
  unsigned po = (mw * 224 + (nw == 0 ? lane : 160 + 5 * g + c16)) * 16;
  unsigned ab2 = ab + 64 - 8 * (p4 & 2), vs = vreal ? 2560 : 0;
  PIN(pbase); PIN(phiw); PIN(vw); PIN(ab); PIN(vb); PIN(po); PIN(ab2); PIN(vs);
  STAMP(1);
  WAITV(0);
  if (isq) {
    if (nw == 0) gemm1_loop<8, true, false>(acc, xa, wrd, 0, 0);
    else gemm1_loop<7, true, false>(acc, xa, wrd, 0, 0);
  } else {
    const unsigned wh = wrd + (nw == 0 ? 0 : (jg < 6 ? 256 : 192));
    if (wide) gemm1_loop<8, false, false>(acc, xa, wh, 0, 0);
    else if (gatew) gemm1_loop<7, false, true>(acc, xa, wh, L_GATE + g * 16 + rot * 128, 2 * rot);
    else gemm1_loop<6, false, false>(acc, xa, wh, 0, 0);
  }
  STAMP(2);
  PIN(warm);

  unsigned kq[10];
  if (!isq) {
    const int k0 = (hstart >> 6) + (nw == 0 ? 0 : (jg < 6 ? 4 : 3));
    char* abase = ws + WS_A + ((rt * 8 + 2 * mw) * 74 + k0) * 1024;
#pragma unroll
    for (int pr = 0; pr < 4; ++pr) {
      if (pr < 3 || wide) {
        const u32x4 f0 = silu8(acc[2 * pr][0], acc[2 * pr + 1][0]), f1 = silu8(acc[2 * pr][1], acc[2 * pr + 1][1]);
        gst128_sc1(boff, abase + pr * 1024, f0);
        gst128_sc1(boff, abase + (74 + pr) * 1024, f1);
      }
    }
    if (gatew && g == 0) {
#pragma unroll
      for (int mt = 0; mt < 2; ++mt) {
        const float gv = __builtin_amdgcn_rcpf(1.f + __builtin_amdgcn_exp2f(acc[6][mt][0] * -1.4426950408889634f));
        gst32_sc1((rt * 128 + 32 * mw + 16 * mt + c16) * 4, ws + WS_G, __builtin_bit_cast(unsigned, gv));
      }
    }
    STAMP(3);
    WAITV(0);
    BAR();
    STAMP(4);
#pragma unroll
    for (int i = 0; i < 10; ++i) kq[i] = 0;
  } else {
    WPhi wf;
    wphi_read(wf, pbase);
    SB();
    unsigned kk[10];
#pragma unroll
    for (int i = 0; i < 10; ++i) {
      const f32x4 a = acc[i >> 1][0], c = acc[i >> 1][1];
      kq[i] = pk2(a[2 * (i & 1)], a[2 * (i & 1) + 1]); kk[i] = pk2(c[2 * (i & 1)], c[2 * (i & 1) + 1]);
    }
    SB();
#pragma unroll
    for (int i = 0; i < 10; ++i) {
      const auto r = __builtin_amdgcn_permlane32_swap(kq[i], kk[i], false, false);
      kq[i] = r[0]; kk[i] = r[1];
    }
    SB();
    f32x4 pp[5];
    phi_proj(wf, kk, pp);
    SB();
#pragma unroll
    for (int i = 5; i < 8; ++i) {
      if (i < 7 || nw == 0) {
#pragma unroll
        for (int mt = 0; mt < 2; ++mt) {
          const f32x4 a = acc[i][mt];
          sts64(vw + mt * 2560 + 8 * (i - 5), (u32x2){pk2(a[0], a[1]), pk2(a[2], a[3])});
        }
      }
    }
    SB();
    unsigned phi[20];
    dual_softmax(pp, phi);
#pragma unroll
    for (int i = 0; i < 5; ++i) sts128(phiw + 16 * i, (u32x4){phi[4 * i], phi[4 * i + 1], phi[4 * i + 2], phi[4 * i + 3]});
    unsigned va[8];
    va[0] = vb;
#pragma unroll
    for (int i = 1; i < 8; ++i) { va[i] = va[i - 1] + vs; PIN(va[i]); }
    BAR();
    STAMP(3);
    u32x4 vf[4], pf[4][3];
#pragma unroll
    for (int ks = 0; ks < 4; ++ks) {
      const unsigned o = ks * 32 * 352;
      vf[ks] = cat(ldstr(va[2 * ks]), ldstr(va[2 * ks + 1]));
      pf[ks][0] = cat(ldstr(ab + o), ldstr(ab + o + 16 * 352));
      pf[ks][1] = cat(ldstr(ab + o + 32), ldstr(ab + o + 32 + 16 * 352));
      pf[ks][2] = cat(ldstr(ab2 + o), ldstr(ab2 + o + 16 * 352));
      SB();
    }
    f32x4 kv[3];
#pragma unroll
    for (int i = 0; i < 3; ++i) kv[i] = (f32x4){0.f, 0.f, 0.f, 0.f};
#pragma unroll
    for (int ks = 0; ks < 4; ++ks)
#pragma unroll
      for (int i = 0; i < 3; ++i) kv[i] = mfma(pf[ks][i], vf[ks], kv[i]);
    char* pb = ws + WS_PART + (jg * 16 + rt) * GR_BYTES;
    const unsigned fs = nw == 0 ? 1024 : 320;
    if (nw == 0 || c16 < 5) {
      gst128_sc1(po, pb, (u32x4){pk2(kv[0][0], kv[0][1]), tlo, pk2(kv[0][2], kv[0][3]), thi});
      gst128_sc1(po, pb + fs, (u32x4){pk2(kv[1][0], kv[1][1]), tlo, pk2(kv[1][2], kv[1][3]), thi});
      if (g < 2) gst128_sc1(po, pb + 2 * fs, (u32x4){pk2(kv[2][0], kv[2][1]), tlo, pk2(kv[2][2], kv[2][3]), thi});
    }
  }

  {
    P2 p;
    p.aa = L_AR + mw * 4096 + boff;
    p.ba = L_BR + nw * 3072 + boff;
    p.st = 0;
    f32x4 ob[2][3], oa[2][3];
#pragma unroll
    for (int i = 0; i < 6; ++i) { ob[i / 3][i % 3] = (f32x4){0.f, 0.f, 0.f, 0.f}; oa[i / 3][i % 3] = (f32x4){0.f, 0.f, 0.f, 0.f}; }
#define P2_FIRST() p2_first(p)
#define P2_RUN(O, N) p2_run(p, O, N, nw)
    const unsigned pofs = nw == 0 ? mw * 6 : 24 + mw * 4;
    if (!isq) {
#pragma unroll
      for (int i = 0; i < 6; ++i) PIN(ob[i / 3][i % 3]);
      BAR();
      STAMP(5);
      P2_FIRST();
      P2_RUN(ob, NH2);
      STAMP(9);
#pragma unroll
      for (int i = 0; i < 6; ++i) {
        PIN(ob[i / 3][i % 3]);
        if (i % 3 < 2 || nw == 0)
          gst128_sc1(boff, ws + WS_P + (rt2 * 8 + ct) * 40960 + (pofs + (i / 3) * (3 - nw) + i % 3) * 1024, __builtin_bit_cast(u32x4, ob[i / 3][i % 3]));
      }
      WAITV(0);
      if (lane == 0) gst64_sc1((rt2 * 8 + ct) * 128 + w * 8, ws + WS_PF, tagv);
      STAMP(10);
    } else {
      BAR();
      STAMP(5);
      unsigned phq[20];
      {
        WPhi wf;
        wphi_read(wf, pbase);
        SB();
        f32x4 pp[5];
        phi_proj(wf, kq, pp);
        dual_softmax(pp, phq);
      }
#pragma unroll
      for (int i = 0; i < 10; ++i) {
        const int a = i < 8 ? i : 8 + i, c = i < 8 ? i + 8 : 10 + i;
        const auto r = __builtin_amdgcn_permlane32_swap(phq[a], phq[c], false, false);
        phq[a] = r[0]; phq[c] = r[1];
      }
#pragma unroll
      for (int i = 0; i < 10; ++i) {
        const int a = i < 8 ? (i & 3) + 8 * (i >> 2) : 2 * i, c = i < 8 ? a + 4 : a + 1;
        const auto r = __builtin_amdgcn_permlane16_swap(phq[a], phq[c], false, false);
        phq[a] = r[0]; phq[c] = r[1];
      }
      unsigned k0a = L_KV + 2 * nw * 1680 + c16 * 80, k1a = L_KV + 2 * nw * 1680 + (p4 == 0 ? 16 + q4 : 20) * 80;
      unsigned k0b = k0a + 64 + 4 * g, k1b = k1a + 64 + 4 * g;
      k0a += 16 * g; k1a += 16 * g;
      unsigned ow = (g & 1) * 74 * 1024 + ((2 * (jg & 1) + nw) * 16 + c16) * 16 + (g >> 1) * 8;
      PIN(k0a); PIN(k1a); PIN(k0b); PIN(k1b); PIN(ow);
#pragma unroll
      for (int i = 0; i < 20; ++i) PIN(phq[i]);
#pragma unroll
      for (int i = 0; i < 6; ++i) { PIN(ob[i / 3][i % 3]); PIN(oa[i / 3][i % 3]); }
      STAMP(6);
      BAR();
      STAMP(7);
      {
        u32x4 a32[2][2];
        unsigned a16[2][2];
#pragma unroll
        for (int h = 0; h < 2; ++h) {
          a32[h][1] = lds128(k1a + h * 1680); a16[h][1] = *(LAS unsigned*)(size_t)(k1b + h * 1680);
          a32[h][0] = lds128(k0a + h * 1680); a16[h][0] = *(LAS unsigned*)(size_t)(k0b + h * 1680);
        }
        SB();
        f32x4 o[4][2];
#pragma unroll
        for (int t = 1; t >= 0; --t) {
#pragma unroll
          for (int u = 0; u < 4; ++u)
            o[u][t] = mfma(a32[u & 1][t], (u32x4){phq[4 * u], phq[4 * u + 1], phq[4 * u + 2], phq[4 * u + 3]}, (f32x4){0.f, 0.f, 0.f, 0.f});
#pragma unroll
          for (int u = 0; u < 4; ++u)
            o[u][t] = __builtin_amdgcn_mfma_f32_16x16x16bf16_1k(__builtin_bit_cast(s16x4, (u32x2){a16[u & 1][t], 0u}),
                                                                 __builtin_bit_cast(s16x4, (u32x2){phq[16 + u], 0u}), o[u][t], 0, 0, 0);
        }
        char* abase = ws + WS_A + ((rt * 8 + 2 * mw) * 74 + 54 + (jg >> 1) * 5) * 1024;
        float sc[4];
#pragma unroll
        for (int u = 0; u < 4; ++u) sc[u] = __builtin_amdgcn_rcpf(o[u][1][1] + 1e-6f);
        unsigned l0 = pk2(o[0][1][0] * sc[0], o[1][1][0] * sc[1]), l1 = pk2(o[2][1][0] * sc[2], o[3][1][0] * sc[3]);
#pragma unroll
        for (int mt = 0; mt < 2; ++mt) {
          const f32x4 a = o[2 * mt][0] * sc[2 * mt], c = o[2 * mt + 1][0] * sc[2 * mt + 1];
          gst128_sc1(boff, abase + (mt * 74 + 2 * (jg & 1) + nw) * 1024, (u32x4){pk2(a[0], a[1]), pk2(a[2], a[3]), pk2(c[0], c[1]), pk2(c[2], c[3])});
        }
        const auto r = __builtin_amdgcn_permlane16_swap(l0, l1, false, false);
        gst64_sc1(ow, abase + 4 * 1024, (u32x2){r[0], r[1]});
      }
      STAMP(4);
      WAITV(0);
      if (lane == 0) gst64_sc1(rt * 512 + (jg * 8 + w) * 8, ws + WS_OF, tagv);
      STAMP(8);
      BAR();
      P2_FIRST();
      unsigned gb[2];
#pragma unroll
      for (int mt = 0; mt < 2; ++mt) gb[mt] = gld32_sc1((rt2 * 128 + 32 * mw + 16 * mt + c16) * 4, ws + WS_G);
      P2_RUN(ob, NQ2 - OFL);
      BAR();
      STAMP(9);
      P2_FIRST();
      P2_RUN(oa, 10);
      asm volatile("s_waitcnt vmcnt(0)" : "+v"(gb[0]), "+v"(gb[1])::"memory");
      BAR();
      STAMP(10);
      u32x4 pt[6];
#pragma unroll
      for (int i = 0; i < 6; ++i) {
        const unsigned tl = (NQ2 + 10) % 8 * 10 + pofs + ((i % 3 < 2 || nw == 0) ? (i / 3) * (3 - nw) + i % 3 : 0);
        pt[i] = lds128(L_BR + (tl >= 80 ? tl - 80 : tl) * 1024 + boff);
      }
      const unsigned oo = (rt2 * 128 + 32 * mw + c16) * 1280 + ct * 160 + nw * 96 + g * 8;
#pragma unroll
      for (int i = 0; i < 6; ++i) {
        const int mt = i / 3, n = i % 3;
        PIN(ob[mt][n]); PIN(oa[mt][n]);
        if (n < 2 || nw == 0) {
          const float gv = __builtin_bit_cast(float, gb[mt]), omg = 1.f - gv;
          const f32x4 r = (ob[mt][n] + __builtin_bit_cast(f32x4, pt[i])) * omg + oa[mt][n] * gv;
          gst64(oo + mt * 16 * 1280 + n * 32, out, (u32x2){pk2(r[0], r[1]), pk2(r[2], r[3])});
        }
      }
    }
  }
}

extern "C" void kernel_launch(void* const* d_in, const int* in_sizes, int n_in, void* d_out, int out_size, void* d_ws,
                              size_t ws_size, hipStream_t stream) {
  (void)in_sizes; (void)n_in; (void)out_size; (void)ws_size;
  (void)hipFuncSetAttribute((const void*)fused_kernel, hipFuncAttributeMaxDynamicSharedMemorySize, LDS_BYTES);
  fused_kernel<<<256, 768, LDS_BYTES, stream>>>((const char*)d_in[0], (const char*)d_in[1], (const char*)d_in[2],
                                                (const char*)d_in[3], (const char*)d_in[5], (const char*)d_in[6],
                                                (const char*)d_in[7], (char*)d_out, (char*)d_ws);
}
```

```cpp
#include <hip/hip_runtime.h>
#include <stdint.h>

typedef __attribute__((ext_vector_type(8))) __bf16 bf16x8;
typedef __attribute__((ext_vector_type(2))) __bf16 bf16x2;
typedef __attribute__((ext_vector_type(4))) short s16x4;
typedef __attribute__((ext_vector_type(4))) float f32x4;
typedef __attribute__((ext_vector_type(2))) float f32x2;
typedef __attribute__((ext_vector_type(4))) unsigned u32x4;
typedef __attribute__((ext_vector_type(2))) unsigned u32x2;
typedef unsigned long long u64;
#define LAS __attribute__((address_space(3)))
#define DI __device__ __forceinline__

constexpr unsigned WS_EPOCH = 0;
constexpr unsigned WS_RT = 1024;
constexpr unsigned WS_HF = 4096;
constexpr unsigned WS_OF = 12288;
constexpr unsigned WS_G = 20480;
constexpr unsigned WS_RED = 49152;
constexpr unsigned GR_BYTES = 896 * 16;
constexpr unsigned WS_PART = WS_RED + 8 * GR_BYTES;
constexpr unsigned WS_B = 2u << 20;
constexpr unsigned WS_A = 5u << 20;
constexpr unsigned WS_P = 15u << 20;
constexpr unsigned WS_PROF = 24u << 20;
static_assert(WS_PART + 128 * GR_BYTES <= WS_B, "ws");

constexpr unsigned SLOT = 47104;
constexpr unsigned L_S1 = 0, L_S2 = SLOT, L_S0 = 2 * SLOT, L_MISC = 3 * SLOT;
constexpr unsigned L_PHIK = L_S2 + 40960;
constexpr unsigned L_V = L_PHIK + 128 * 352;
constexpr unsigned L_BR = 0;
constexpr unsigned L_AR = 81920;
constexpr unsigned L_KV = 147456;
constexpr unsigned L_WPHI = 154240;
constexpr unsigned L_GATE = L_WPHI;
constexpr unsigned L_ZERO = L_WPHI + 4096;
constexpr unsigned L_ONE = L_ZERO + 2048;
constexpr unsigned L_TAB = L_ONE + 32;
constexpr unsigned LDS_BYTES = 163840;
static_assert(L_ONE + 32 + 3360 <= LDS_BYTES, "lds");

#define WPOL ""
#define HOP1_SLEEP 8
#define HOP2_SLEEP 10
#define RT_SLEEP 0
#define OFL 2
#define TLBM 0
#define SPF 0
#define P2ROT 2
#define RETRY_SLEEP 2
#define POLL_SLEEP 2
#define APRE 2
#define NQ2 7
#define SG 2
constexpr int NH2 = 27 - NQ2;
constexpr unsigned CODE_BYTES = 22528;
constexpr unsigned SPIN_CAP = 1u << 16;

DI unsigned pk2(float a, float b) {
  bf16x2 v = __builtin_convertvector((f32x2){a, b}, bf16x2);
  return __builtin_bit_cast(unsigned, v);
}
DI unsigned fbits(float f) { return __builtin_bit_cast(unsigned, f); }
DI float bitsf(unsigned u) { return __builtin_bit_cast(float, u); }
DI f32x4 mfma(u32x4 a, u32x4 b, f32x4 c) {
  return __builtin_amdgcn_mfma_f32_16x16x32_bf16(__builtin_bit_cast(bf16x8, a), __builtin_bit_cast(bf16x8, b), c, 0, 0, 0);
}
DI u32x4 lds128(unsigned a) { return *(LAS u32x4*)(size_t)a; }
DI u32x2 lds64(unsigned a) { return *(LAS u32x2*)(size_t)a; }
DI void sts128(unsigned a, u32x4 v) { *(LAS u32x4*)(size_t)a = v; }
DI void sts64(unsigned a, u32x2 v) { *(LAS u32x2*)(size_t)a = v; }
DI u32x2 ldstr(unsigned a) {
  return __builtin_bit_cast(u32x2, __builtin_amdgcn_ds_read_tr16_b64_v4i16((LAS s16x4*)(size_t)a));
}
DI u32x4 cat(u32x2 a, u32x2 b) { return (u32x4){a[0], a[1], b[0], b[1]}; }
DI void glds(unsigned voff, const void* sbase, unsigned lds) {
  asm volatile("s_mov_b32 m0, %2\n\ts_nop 0\n\tglobal_load_lds_dwordx4 %0, %1" ::"v"(voff), "s"(sbase), "s"(lds) : "memory");
}
DI void glds_nt(unsigned voff, const void* sbase, unsigned lds) {
  asm volatile("s_mov_b32 m0, %2\n\ts_nop 0\n\tglobal_load_lds_dwordx4 %0, %1 nt" ::"v"(voff), "s"(sbase), "s"(lds) : "memory");
}
DI void glds5_sc1(unsigned voff, unsigned voff1k, const void* sbase, unsigned lds) {
  asm volatile(
      "s_mov_b32 m0, %2\n\ts_nop 0\n\t"
      "global_load_lds_dwordx4 %0, %1 sc1\n\t"
      "global_load_lds_dwordx4 %0, %1 offset:1024 sc1\n\t"
      "global_load_lds_dwordx4 %0, %1 offset:2048 sc1\n\t"
      "global_load_lds_dwordx4 %0, %1 offset:3072 sc1\n\t"
      "s_add_u32 m0, m0, 1024\n\ts_nop 0\n\t"
      "global_load_lds_dwordx4 %3, %1 offset:3072 sc1" ::"v"(voff), "s"(sbase), "s"(lds), "v"(voff1k)
      : "memory", "scc");
}
DI u32x4 gld128_sc1(unsigned voff, const void* sbase) {
  u32x4 r;
  asm volatile("global_load_dwordx4 %0, %1, %2 sc1" : "=v"(r) : "v"(voff), "s"(sbase) : "memory");
  return r;
}
DI u32x2 gld64_sc1(unsigned voff, const void* sbase) {
  u32x2 r;
  asm volatile("global_load_dwordx2 %0, %1, %2 sc1" : "=v"(r) : "v"(voff), "s"(sbase) : "memory");
  return r;
}
DI unsigned gld32_sc1(unsigned voff, const void* sbase) {
  unsigned r;
  asm volatile("global_load_dword %0, %1, %2 sc1" : "=v"(r) : "v"(voff), "s"(sbase) : "memory");
  return r;
}
DI unsigned gld32(unsigned voff, const void* sbase) {
  unsigned r;
  asm volatile("global_load_dword %0, %1, %2" : "=v"(r) : "v"(voff), "s"(sbase) : "memory");
  return r;
}
DI void gst128_sc1(unsigned voff, void* sbase, u32x4 v) {
  asm volatile("global_store_dwordx4 %0, %1, %2 sc1\n\ts_nop 1" ::"v"(voff), "v"(v), "s"(sbase) : "memory");
}
DI void gst64_sc1(unsigned voff, void* sbase, u32x2 v) {
  asm volatile("global_store_dwordx2 %0, %1, %2 sc1" ::"v"(voff), "v"(v), "s"(sbase) : "memory");
}
DI void gst32_sc1(unsigned voff, void* sbase, unsigned v) {
  asm volatile("global_store_dword %0, %1, %2 sc1" ::"v"(voff), "v"(v), "s"(sbase) : "memory");
}
DI void gst64(unsigned voff, void* sbase, u32x2 v) {
  asm volatile("global_store_dwordx2 %0, %1, %2" ::"v"(voff), "v"(v), "s"(sbase) : "memory");
}
#define WAITV(n) asm volatile("s_waitcnt vmcnt(" #n ")" ::: "memory")
#define BAR() asm volatile("s_waitcnt lgkmcnt(0)\n\ts_barrier" ::: "memory")
#define PIN(x) asm volatile("" : "+v"(x))
#define SB() __builtin_amdgcn_sched_barrier(0)

DI void poll_words(const char* base, unsigned voff, unsigned tlo, unsigned thi) {
#pragma unroll 1
  for (unsigned spins = 0; spins < SPIN_CAP; ++spins) {
    u32x2 v = gld64_sc1(voff, base);
    asm volatile("s_waitcnt vmcnt(0)" : "+v"(v)::"memory");
    if (__all(v[0] == tlo && v[1] == thi)) break;
    __builtin_amdgcn_s_sleep(POLL_SLEEP);
  }
}

DI void dual_softmax(const f32x4 (&p)[5], unsigned (&o)[20]) {
  float mx = __builtin_fmaxf(p[0][0], p[0][1]), mn = __builtin_fminf(p[0][0], p[0][1]);
  float mx2 = __builtin_fmaxf(p[0][2], p[0][3]), mn2 = __builtin_fminf(p[0][2], p[0][3]);
#pragma unroll
  for (int t = 1; t < 5; ++t) {
    mx = __builtin_fmaxf(__builtin_fmaxf(mx, p[t][0]), p[t][1]); mn = __builtin_fminf(__builtin_fminf(mn, p[t][0]), p[t][1]);
    mx2 = __builtin_fmaxf(__builtin_fmaxf(mx2, p[t][2]), p[t][3]); mn2 = __builtin_fminf(__builtin_fminf(mn2, p[t][2]), p[t][3]);
  }
  const float L = 1.4426950408889634f;
  const float mxl = __builtin_fmaxf(mx, mx2) * -L, mnl = __builtin_fminf(mn, mn2) * L;
  f32x2 ep[10], en[10];
#pragma unroll
  for (int i = 0; i < 10; ++i) {
    const f32x2 v = {p[i >> 1][2 * (i & 1)], p[i >> 1][2 * (i & 1) + 1]};
    ep[i] = __builtin_elementwise_fma(v, (f32x2){L, L}, (f32x2){mxl, mxl});
    en[i] = __builtin_elementwise_fma(v, (f32x2){-L, -L}, (f32x2){mnl, mnl});
  }
  SB();
  f32x2 sp, sn;
#pragma unroll
  for (int i = 0; i < 10; ++i) {
    ep[i][0] = __builtin_amdgcn_exp2f(ep[i][0]); ep[i][1] = __builtin_amdgcn_exp2f(ep[i][1]);
    en[i][0] = __builtin_amdgcn_exp2f(en[i][0]); en[i][1] = __builtin_amdgcn_exp2f(en[i][1]);
    if (i == 1) { sp = ep[0]; sn = en[0]; }
    if (i > 1) { sp += ep[i - 1]; sn += en[i - 1]; }
    SB();
  }
  sp += ep[9]; sn += en[9];
  const float rp = __builtin_amdgcn_rcpf(sp[0] + sp[1]), rn = __builtin_amdgcn_rcpf(sn[0] + sn[1]);
  f32x2 rp2 = {rp, rp}, rn2 = {rn, rn};
  PIN(rp2); PIN(rn2);
#pragma unroll
  for (int i = 0; i < 10; ++i) { ep[i] *= rp2; en[i] *= rn2; }
  SB();
#pragma unroll
  for (int i = 0; i < 10; ++i) { o[i] = pk2(ep[i][0], ep[i][1]); o[10 + i] = pk2(en[i][0], en[i][1]); }
}
struct WPhi { u32x4 a0[5], a1[5]; u32x2 a2[5]; };
DI void wphi_read(WPhi& f, unsigned pbase) {
#pragma unroll
  for (int t = 0; t < 5; ++t) f.a0[t] = cat(ldstr(pbase + 8 * t), ldstr(pbase + 8 * t + 160));
#pragma unroll
  for (int t = 0; t < 5; ++t) f.a1[t] = cat(ldstr(pbase + 8 * t + 320), ldstr(pbase + 8 * t + 480));
#pragma unroll
  for (int t = 0; t < 5; ++t) f.a2[t] = ldstr(pbase + 8 * t + 640);
}
DI void phi_proj(const WPhi& f, const unsigned (&kq)[10], f32x4 (&pp)[5]) {
  const u32x4 f0 = {kq[0], kq[1], kq[2], kq[3]}, f1 = {kq[4], kq[5], kq[6], kq[7]};
  const u32x2 f2 = {kq[8], kq[9]};
#pragma unroll
  for (int t = 0; t < 5; ++t) pp[t] = mfma(f.a0[t], f0, (f32x4){0.f, 0.f, 0.f, 0.f});
#pragma unroll
  for (int t = 0; t < 5; ++t) pp[t] = mfma(f.a1[t], f1, pp[t]);
#pragma unroll
  for (int t = 0; t < 5; ++t)
    pp[t] = __builtin_amdgcn_mfma_f32_16x16x16bf16_1k(__builtin_bit_cast(s16x4, f.a2[t]), __builtin_bit_cast(s16x4, f2), pp[t], 0, 0, 0);
}
DI u32x4 silu8(f32x4 a, f32x4 c) {
  const float NL = -1.4426950408889634f;
  f32x2 v[4] = {{a[0], a[1]}, {a[2], a[3]}, {c[0], c[1]}, {c[2], c[3]}}, e[4];
#pragma unroll
  for (int i = 0; i < 4; ++i) e[i] = v[i] * (f32x2){NL, NL};
#pragma unroll
  for (int i = 0; i < 4; ++i) { e[i][0] = __builtin_amdgcn_exp2f(e[i][0]); e[i][1] = __builtin_amdgcn_exp2f(e[i][1]); }
#pragma unroll
  for (int i = 0; i < 4; ++i) e[i] += (f32x2){1.f, 1.f};
#pragma unroll
  for (int i = 0; i < 4; ++i) { e[i][0] = __builtin_amdgcn_rcpf(e[i][0]); e[i][1] = __builtin_amdgcn_rcpf(e[i][1]); }
#pragma unroll
  for (int i = 0; i < 4; ++i) v[i] *= e[i];
  return (u32x4){pk2(v[0][0], v[0][1]), pk2(v[1][0], v[1][1]), pk2(v[2][0], v[2][1]), pk2(v[3][0], v[3][1])};
}

DI void amfma(f32x4& c, const u32x4& a, const u32x4& b) { asm volatile("v_mfma_f32_16x16x32_bf16 %0, %1, %2, %0" : "+v"(c) : "v"(a), "v"(b)); }
template <int OFF> DI void trlo(u32x4& f, unsigned a) { u32x2 t; asm volatile("ds_read_b64_tr_b16 %0, %1 offset:%2" : "=v"(t) : "v"(a), "i"(OFF)); f[0] = t[0]; f[1] = t[1]; }
template <int OFF> DI void trhi(u32x4& f, unsigned a) { u32x2 t; asm volatile("ds_read_b64_tr_b16 %0, %1 offset:%2" : "=v"(t) : "v"(a), "i"(OFF)); f[2] = t[0]; f[3] = t[1]; }
template <int OFF> DI void rd128(u32x4& f, unsigned a) { asm volatile("ds_read_b128 %0, %1 offset:%2" : "=v"(f) : "v"(a), "i"(OFF)); }
DI void vadd(unsigned& v, unsigned s) { asm volatile("v_add_u32 %0, %1, %0" : "+v"(v) : "s"(s)); }
#define WL(n) asm volatile("s_waitcnt lgkmcnt(" #n ")")
#define MFMA_DONE() asm volatile("s_waitcnt lgkmcnt(0)\n\ts_nop 7\n\ts_nop 3" ::: "memory")

template <int NT, bool Q, bool GATE>
DI void gemm1_loop(f32x4 (&acc)[8][2], unsigned xa, unsigned wa, unsigned wb, unsigned gk) {
  u32x4 W[8], X0[2], X1[2];
  unsigned xb = xa ^ 64;
#pragma unroll
  for (int t = 0; t < NT; ++t) { acc[t][0] = (f32x4){0.f, 0.f, 0.f, 0.f}; acc[t][1] = (f32x4){0.f, 0.f, 0.f, 0.f}; PIN(acc[t][0]); PIN(acc[t][1]); }
  BAR();
#define TO(i) (Q ? ((i) < 5 ? (NT == 8 ? 0 : 80) + 8 * (i) : 160 + 8 * ((i) - (NT == 8 ? 5 : 2))) : 32 * (i))
#define AD(i) wa
#define RL(i, off) if constexpr (GATE && (i) == 6) { rd128<0>(W[i], wb); } else { trlo<TO(i) + (off)>(W[i], AD(i)); }
#define RH(i, off) if constexpr (GATE && (i) == 6) { gk = gk == 19 ? 0 : gk + 1; vadd(wb, gk == 0 ? -1216 : 64); } else { trhi<TO(i) + (off) + 7680>(W[i], AD(i)); }
#define M0(i, X) amfma(acc[i][0], W[i], X[0]);
#define M1(i, X) amfma(acc[i][1], W[i], X[1]);
#define G1(i, j) M0(i, X0) RL(j, 15360) M1(i, X0) RH(j, 15360)
#define G1X(i, j, h) M0(i, X0) RL(j, 15360) rd128<2048 * (h)>(X1[h], xb); M1(i, X0) RH(j, 15360)
  rd128<0>(X0[0], xa); rd128<2048>(X0[1], xa);
  RL(0, 0) RH(0, 0) RL(1, 0) RH(1, 0) RL(2, 0) RH(2, 0) RL(3, 0) RH(3, 0) RL(4, 0) RH(4, 0)
  if constexpr (NT > 6) { RL(5, 0) RH(5, 0) }
  if constexpr (NT > 7) { RL(6, 0) RH(6, 0) }
  unsigned sb = L_S0;
#pragma unroll 1
  for (int t = 0; t < 10; ++t) {
    if constexpr (NT == 8) {
      WL(10); M0(0, X0) RL(7, 0) M1(0, X0) RH(7, 0) G1(1, 0)
      WL(10); G1(2, 1) G1(3, 2)
      WL(10); G1X(4, 3, 0) G1X(5, 4, 1)
      WL(12); G1(6, 5) G1(7, 6)
      WL(5);
    } else if constexpr (NT == 7) {
      WL(8); M0(0, X0) RL(6, 0) M1(0, X0) RH(6, 0) G1(1, 0)
      WL(7); G1(2, 1) G1X(3, 2, 0)
      WL(8); G1X(4, 3, 1) G1(5, 4)
      WL(12); G1(6, 5)
      WL(5);
    } else {
      WL(6); M0(0, X0) RL(5, 0) M1(0, X0) RH(5, 0) G1(1, 0)
      WL(6); G1X(2, 1, 0) G1X(3, 2, 1)
      WL(8); G1(4, 3) G1(5, 4)
      WL(4);
    }
    M0(0, X1) RL(NT - 1, 15360) M1(0, X1) RH(NT - 1, 15360)
    const unsigned sn = sb == L_S0 ? L_S1 : sb + SLOT, dl = sn - sb;
    sb = sn;
    M0(1, X1) vadd(xa, dl); M1(1, X1) vadd(wa, dl);
    M0(2, X1) vadd(xb, dl); M1(2, X1)
    BAR();
    M0(3, X1) rd128<0>(X0[0], xa); rd128<2048>(X0[1], xa); M1(3, X1) RL(0, 0) RH(0, 0)
    M0(4, X1) RL(1, 0) RH(1, 0) M1(4, X1) RL(2, 0) RH(2, 0)
    if constexpr (NT == 8) {
      M0(5, X1) RL(3, 0) RH(3, 0) M1(5, X1) RL(4, 0)
      M0(6, X1) RH(4, 0) RL(5, 0) M1(6, X1) RH(5, 0)
      M0(7, X1) RL(6, 0) M1(7, X1) RH(6, 0)
    } else if constexpr (NT == 7) {
      M0(5, X1) RL(3, 0) RH(3, 0) M1(5, X1) RL(4, 0) RH(4, 0)
      M0(6, X1) RL(5, 0) M1(6, X1) RH(5, 0)
    } else {
      M0(5, X1) RL(3, 0) RH(3, 0) M1(5, X1) RL(4, 0) RH(4, 0)
    }
  }
  MFMA_DONE();
#undef TO
#undef AD
#undef RL
#undef RH
#undef M0
#undef M1
#undef G1
#undef G1X
}

struct P2 {
  u32x4 A[2], B[3];
  unsigned aa, ba;
  unsigned st, sa;
};
DI void p2_first(P2& p) {
  asm volatile("ds_read_b128 %0, %5\n\tds_read_b128 %2, %6\n\tds_read_b128 %3, %6 offset:1024\n\tds_read_b128 %4, %6 offset:2048\n\t"
               "ds_read_b128 %1, %5 offset:2048\n\ts_waitcnt lgkmcnt(0)"
               : "=&v"(p.A[0]), "=&v"(p.A[1]), "=&v"(p.B[0]), "=&v"(p.B[1]), "=&v"(p.B[2]) : "v"(p.aa), "v"(p.ba) : "memory");
}
#define MF(d, a, b) "v_mfma_f32_16x16x32_bf16 %" #d ", %" #a ", %" #b ", %" #d "\n\t"
#define RD(d, a, o) "ds_read_b128 %" #d ", %" #a " offset:" #o "\n\t"
DI void p2_run(P2& p, f32x4 (&o)[2][3], int n, int nw) {
  u32x4 A0, A1, B0, B1, B2;
  unsigned da, db;
  asm volatile(
      "s_cmp_lg_u32 %22, 0\n\ts_cbranch_scc1 2f\n"
      "1:\n\t"
      "s_add_u32 %18, %18, 1\n\ts_and_b32 %20, %18, 3\n\ts_cselect_b32 %20, 16384, %23\n\t"
      "s_waitcnt lgkmcnt(2)\n\t" MF(0, 8, 6) RD(11, 16, 1024)
      "s_and_b32 %21, %18, 7\n\t" MF(1, 9, 6) RD(13, 17, 5120)
      "s_waitcnt lgkmcnt(2)\n\t" MF(2, 10, 6) RD(14, 17, 6144)
      "s_cselect_b32 %21, 10240, %24\n\t" MF(3, 8, 7) RD(15, 17, 7168)
      MF(4, 9, 7) RD(12, 16, 3072)
      MF(5, 10, 7) "v_add_u32 %16, %20, %16\n\t"
      "s_waitcnt lgkmcnt(2)\n\t" MF(0, 13, 11) "v_add_u32 %17, %21, %17\n\t"
      MF(1, 14, 11)
      "s_waitcnt lgkmcnt(0)\n\ts_barrier\n\t"
      MF(2, 15, 11) RD(6, 16, 0) RD(8, 17, 0)
      MF(3, 13, 12) RD(9, 17, 1024) RD(10, 17, 2048)
      MF(4, 14, 12) RD(7, 16, 2048) "s_sub_u32 %19, %19, 1\n\t"
      MF(5, 15, 12)
      "s_cmp_lg_u32 %19, 0\n\ts_cbranch_scc1 1b\n\t"
      "s_branch 3f\n"
      "2:\n\t"
      "s_add_u32 %18, %18, 1\n\ts_and_b32 %20, %18, 3\n\ts_cselect_b32 %20, 16384, %23\n\t"
      "s_waitcnt lgkmcnt(1)\n\t" MF(0, 8, 6) RD(11, 16, 1024) RD(13, 17, 5120)
      "s_and_b32 %21, %18, 7\n\t" MF(1, 9, 6) RD(14, 17, 6144)
      "s_cselect_b32 %21, 10240, %24\n\ts_waitcnt lgkmcnt(3)\n\t" MF(3, 8, 7) RD(12, 16, 3072)
      MF(4, 9, 7) "v_add_u32 %16, %20, %16\n\tv_add_u32 %17, %21, %17\n\t"
      "s_waitcnt lgkmcnt(1)\n\t" MF(0, 13, 11)
      MF(1, 14, 11)
      "s_waitcnt lgkmcnt(0)\n\ts_barrier\n\t"
      RD(6, 16, 0) RD(8, 17, 0)
      MF(3, 13, 12) RD(9, 17, 1024) RD(7, 16, 2048) "s_sub_u32 %19, %19, 1\n\t"
      MF(4, 14, 12)
      "s_cmp_lg_u32 %19, 0\n\ts_cbranch_scc1 2b\n"
      "3:\n\t"
      "s_waitcnt lgkmcnt(0)\n\ts_nop 7"
      : "+v"(o[0][0]), "+v"(o[0][1]), "+v"(o[0][2]), "+v"(o[1][0]), "+v"(o[1][1]), "+v"(o[1][2]),
        "+v"(p.A[0]), "+v"(p.A[1]), "+v"(p.B[0]), "+v"(p.B[1]), "+v"(p.B[2]),
        "=&v"(A0), "=&v"(A1), "=&v"(B0), "=&v"(B1), "=&v"(B2),
        "+v"(p.aa), "+v"(p.ba), "+s"(p.st), "+s"(n), "=&s"(da), "=&s"(db)
      : "s"(nw), "s"(-49152), "s"(-71680)
      : "memory", "scc");
}
DI void p2a_run(P2& p, f32x4 (&o)[3], unsigned& aa, int n, int nw) {
  u32x4 A0, A1, A2, A3, B0, B1, B2;
  unsigned da, db;
#define ADVB "s_add_u32 %15, %15, 1\n\ts_and_b32 %18, %15, 7\n\ts_cselect_b32 %18, 10240, %22\n\tv_add_u32 %14, %18, %14\n\t"
#define ADVA "s_add_u32 %19, %19, 1\n\ts_and_b32 %17, %19, 3\n\ts_cselect_b32 %17, 16384, %21\n\tv_add_u32 %13, %17, %13\n\t"
  asm volatile(
      "s_cmp_lg_u32 %20, 0\n\ts_cbranch_scc1 2f\n"
      "1:\n\t"
      "s_barrier\n\t" RD(6, 13, 0) RD(10, 14, 5120) RD(11, 14, 6144) RD(12, 14, 7168) RD(7, 13, 1024) RD(8, 13, 2048) RD(9, 13, 3072)
      ADVB
      "s_waitcnt lgkmcnt(6)\n\t" MF(0, 3, 6) MF(1, 4, 6) MF(2, 5, 6)
      RD(3, 14, 0) RD(4, 14, 1024) RD(5, 14, 2048) ADVA
      "s_waitcnt lgkmcnt(5)\n\t" MF(0, 10, 7) MF(1, 11, 7) MF(2, 12, 7)
      RD(10, 14, 5120) RD(11, 14, 6144) RD(12, 14, 7168) ADVB
      "s_waitcnt lgkmcnt(3)\n\t" MF(0, 3, 8) MF(1, 4, 8) MF(2, 5, 8)
      RD(3, 14, 0) RD(4, 14, 1024) RD(5, 14, 2048) "s_sub_u32 %16, %16, 1\n\t"
      "s_waitcnt lgkmcnt(3)\n\t" MF(0, 10, 9) MF(1, 11, 9) MF(2, 12, 9)
      "s_cmp_lg_u32 %16, 0\n\ts_cbranch_scc1 1b\n\t"
      "s_branch 3f\n"
      "2:\n\t"
      "s_barrier\n\t" RD(6, 13, 0) RD(10, 14, 5120) RD(11, 14, 6144) RD(7, 13, 1024) RD(8, 13, 2048) RD(9, 13, 3072)
      ADVB
      "s_waitcnt lgkmcnt(5)\n\t" MF(0, 3, 6) MF(1, 4, 6)
      RD(3, 14, 0) RD(4, 14, 1024) ADVA
      "s_waitcnt lgkmcnt(4)\n\t" MF(0, 10, 7) MF(1, 11, 7)
      RD(10, 14, 5120) RD(11, 14, 6144) ADVB
      "s_waitcnt lgkmcnt(2)\n\t" MF(0, 3, 8) MF(1, 4, 8)
      RD(3, 14, 0) RD(4, 14, 1024) "s_sub_u32 %16, %16, 1\n\t"
      "s_waitcnt lgkmcnt(2)\n\t" MF(0, 10, 9) MF(1, 11, 9)
      "s_cmp_lg_u32 %16, 0\n\ts_cbranch_scc1 2b\n"
      "3:\n\t"
      "s_waitcnt lgkmcnt(0)\n\ts_nop 7"
      : "+v"(o[0]), "+v"(o[1]), "+v"(o[2]), "+v"(p.B[0]), "+v"(p.B[1]), "+v"(p.B[2]),
        "=&v"(A0), "=&v"(A1), "=&v"(A2), "=&v"(A3), "=&v"(B0), "=&v"(B1), "=&v"(B2),
        "+v"(aa), "+v"(p.ba), "+s"(p.st), "+s"(n), "=&s"(da), "=&s"(db), "+s"(p.sa)
      : "s"(nw), "s"(-49152), "s"(-71680)
      : "memory", "scc");
#undef ADVA
#undef ADVB
}
#undef MF
#undef RD

#define TRW 0
#define TRL 0
#define STAMP(i)

#define SL2(r, o) "s_load_dword s95, %" #r ", " #o "\n\ts_load_dword s95, %" #r ", " #o "+64\n\t"
DI void spf(int kind, int st, const char* x, const char* wqkv, const char* w1, int xcd, int idx) {
  const int xr = xcd & 1, cq = xcd >> 1, krow = st * 64 + xr * 32 + idx;
  const char* px = x + (xr * 1024 + cq * 256 + idx * 8) * 1280 + st * 128;
  const char* pq = wqkv + krow * 3840 + ((cq * 320) & ~127);
  const char* ph = w1 + krow * 3456 + cq * 896 - (cq == 3 ? 128 : 0);
  asm volatile("s_cmp_lt_u32 %3, 2\n\ts_cbranch_scc0 2f\n\ts_cmp_eq_u32 %3, 0\n\ts_cbranch_scc0 1f\n\t"
               SL2(0, 0) SL2(0, 1280) SL2(0, 2560) SL2(0, 3840) SL2(0, 5120) SL2(0, 6400) "s_branch 4f\n"
               "1:\n\t" SL2(0, 7680) SL2(0, 8960) SL2(2, 0) SL2(2, 128) SL2(2, 256) SL2(2, 384) "s_branch 4f\n"
               "2:\n\ts_cmp_eq_u32 %3, 2\n\ts_cbranch_scc0 3f\n\t"
               SL2(1, 0) SL2(1, 128) SL2(1, 256) SL2(1, 1280) SL2(1, 1408) SL2(1, 1536) "s_branch 4f\n"
               "3:\n\t" SL2(1, 2560) SL2(1, 2688) SL2(1, 2816) SL2(2, 512) SL2(2, 640) SL2(2, 768)
               "\n4:" ::"s"(px), "s"(pq), "s"(ph), "s"(kind) : "s95", "scc");
}
typedef __attribute__((ext_vector_type(16))) unsigned u32x16;
DI const char* mkptr(unsigned lo, unsigned hi) { return (const char*)(((u64)hi << 32) | lo); }

__global__ __launch_bounds__(768, 3) void fused_kernel(const char* a_x, const char* a_wqkv, const char* a_wphi, const char* a_wo,
                                                       const char* a_w1, const char* a_w2, const char* a_gate, char* a_out, char* a_ws) {
  extern __shared__ __attribute__((aligned(16))) char smem[];
  u32x16 ka;
  u64 kws, pc;
  asm volatile("s_load_dwordx16 %0, %3, 0x0\n\ts_load_dwordx2 %1, %3, 0x40\n\ts_getpc_b64 %2"
               : "=&s"(ka), "=&s"(kws), "=&s"(pc) : "s"(__builtin_amdgcn_kernarg_segment_ptr()));
  __builtin_amdgcn_sched_barrier(0);
#define ARGS()                                                                                                   \
  asm volatile("s_waitcnt lgkmcnt(0)" : "+s"(ka), "+s"(kws));                                                    \
  const char *x = mkptr(ka[0], ka[1]), *wqkv = mkptr(ka[2], ka[3]), *wphi = mkptr(ka[4], ka[5]), *wo = mkptr(ka[6], ka[7]), \
             *w1 = mkptr(ka[8], ka[9]), *w2 = mkptr(ka[10], ka[11]), *gate = mkptr(ka[12], ka[13]);              \
  char *out = (char*)mkptr(ka[14], ka[15]), *ws = (char*)kws;                                                    \
  (void)x; (void)wqkv; (void)wphi; (void)wo; (void)w1; (void)w2; (void)gate; (void)out;                          \
  u64 epoch;                                                                                                     \
  asm volatile("s_load_dwordx2 %0, %1, 0x0" : "=s"(epoch) : "s"(ws) : "memory")
#define TAG()                                                                                                    \
  asm volatile("s_waitcnt lgkmcnt(0)" : "+s"(epoch));                                                            \
  const u64 tag = epoch * 6364136223846793005ull + 1442695040888963407ull;                                       \
  const unsigned tlo = (unsigned)tag, thi = (unsigned)(tag >> 32);                                               \
  const u32x2 tagv = {tlo, thi}
  const int tid = threadIdx.x, lane = tid & 63;
  const int w = __builtin_amdgcn_readfirstlane(tid >> 6);
  const int b = blockIdx.x, xcd = b & 7, idx = b >> 3;
  const int rt = (xcd & 1) * 8 + (idx & 7);
  const int cb = idx >> 3, cq = xcd >> 1;
  const bool isq = cb < 2;
  const int jg = 2 * cq + (cb & 1);
  const int rt2 = ((xcd & 1) ^ 1) * 8 + ((cq & 1) ^ (isq ? 0 : 1)) * 4 + ((idx >> 2) & 3), ct = (cq >> 1) * 4 + (idx & 3);
  const int g = lane >> 4, c16 = lane & 15, q4 = c16 >> 2, p4 = lane & 3;
  const int hstart = jg * 448 - (jg == 7 ? 64 : 0);
  const unsigned boff = lane * 16;
#define ROTM 7
  const int rl = idx & 7, xr = xcd & 1;
  const int rot = ROTM == 0 ? 0
                : ROTM == 1 ? ((rl ^ cb) & 1) * 5
                : ROTM == 3 ? ((rl ^ cb) & 1) * 5 + xr * 2 + (cq & 1)
                : ROTM == 4 ? ((((rl + cb) % 3) * 10) / 3 + xr * 2) % 10
                : ROTM == 5 ? ((((rl + cb) & 3) * 5) >> 1) + xr
                : ROTM == 6 ? ((rl ^ cb) & 1) * 5 + xr * 3
                : ROTM == 8 ? (xr * 5 + cq) % 10
                : ((rl + cb) % 5) * 2 + xr;

  if (__builtin_expect(w >= 8, 1)) {
    const int L = w - 8;
    const int ldw = isq ? 3840 : 3456;
    unsigned xo[4], wv[8];
    {
      const unsigned x0 = (rt * 128 + 8 * L + (lane >> 3)) * 1280 + (((lane & 7) ^ ((4 * L + (lane >> 4)) & 7)) << 4);
#pragma unroll
      for (int i = 0; i < 4; ++i) xo[i] = x0 + i * 32 * 1280;
      const int rs = lane >= 30 ? 1 : 0, pos = lane - 30 * rs;
      const int sec = pos < 10 ? 0 : (pos < 15 ? 2560 - 160 : (pos < 25 ? 1280 - 240 : 2560 - 320));
      const int col = isq ? sec + jg * 160 + pos * 16 : hstart + pos * 16;
      const unsigned w0 = (8 * (L >> 1) + 2 * (L & 1) + rs) * ldw + col;
#pragma unroll
      for (int i = 0; i < 8; ++i) wv[i] = w0 + (32 * (i >> 2) + 16 * (i & 1) + 4 * ((i >> 1) & 1)) * ldw;
    }
    const u64 half = isq ? 0x3fffffffull : (jg < 6 ? 0x0fffffffull : 0x00ffffffull);
    const u64 wmask = half | (half << 30);
#pragma unroll
    for (int i = 0; i < 4; ++i) PIN(xo[i]);
#pragma unroll
    for (int i = 0; i < 8; ++i) PIN(wv[i]);
    ARGS();
    const char* wbase = isq ? wqkv : w1;
    const char* xs = x + rot * 128;
    const char* wsrc = wbase + rot * 64 * ldw;
    int kst = rot;
    const unsigned mx0 = L * 1024, mw0 = 16384 + L * 960;
    auto issue_lo = [&](unsigned sb) {
      asm volatile(
          "s_mov_b32 m0, %10\n\ts_nop 0\n\tglobal_load_lds_dwordx4 %0, %8\n\t"
          "s_add_u32 m0, m0, 4096\n\ts_nop 0\n\tglobal_load_lds_dwordx4 %1, %8\n\t"
          "s_add_u32 m0, m0, 4096\n\ts_nop 0\n\tglobal_load_lds_dwordx4 %2, %8\n\t"
          "s_add_u32 m0, m0, 4096\n\ts_nop 0\n\tglobal_load_lds_dwordx4 %3, %8\n\t"
          "s_mov_b32 m0, %11\n\ts_mov_b64 exec, %12\n\tglobal_load_lds_dwordx4 %4, %9 " WPOL "\n\t"
          "s_add_u32 m0, m0, 3840\n\ts_nop 0\n\tglobal_load_lds_dwordx4 %5, %9 " WPOL "\n\t"
          "s_add_u32 m0, m0, 3840\n\ts_nop 0\n\tglobal_load_lds_dwordx4 %6, %9 " WPOL "\n\t"
          "s_add_u32 m0, m0, 3840\n\ts_nop 0\n\tglobal_load_lds_dwordx4 %7, %9 " WPOL "\n\t"
          "s_mov_b64 exec, -1"
          ::"v"(xo[0]), "v"(xo[1]), "v"(xo[2]), "v"(xo[3]), "v"(wv[0]), "v"(wv[1]), "v"(wv[2]), "v"(wv[3]),
            "s"(xs), "s"(wsrc), "s"(sb + mx0), "s"(sb + mw0), "s"(wmask)
          : "memory", "scc");
    };
    auto issue_hi = [&](unsigned sb) {
      asm volatile(
          "s_mov_b32 m0, %5\n\ts_mov_b64 exec, %6\n\tglobal_load_lds_dwordx4 %0, %4 " WPOL "\n\t"
          "s_add_u32 m0, m0, 3840\n\ts_nop 0\n\tglobal_load_lds_dwordx4 %1, %4 " WPOL "\n\t"
          "s_add_u32 m0, m0, 3840\n\ts_nop 0\n\tglobal_load_lds_dwordx4 %2, %4 " WPOL "\n\t"
          "s_add_u32 m0, m0, 3840\n\ts_nop 0\n\tglobal_load_lds_dwordx4 %3, %4 " WPOL "\n\t"
          "s_mov_b64 exec, -1"
          ::"v"(wv[4]), "v"(wv[5]), "v"(wv[6]), "v"(wv[7]), "s"(wsrc), "s"(sb + mw0 + 15360), "s"(wmask)
          : "memory", "scc");
      const bool wrap = ++kst == 10;
      xs = wrap ? x : xs + 128;
      wsrc = wrap ? wbase : wsrc + 64 * ldw;
    };
    auto issue_stage = [&](unsigned sb) { issue_lo(sb); issue_hi(sb); };
#define LSTAMP()
#define LBAR() asm volatile("s_barrier" ::: "memory")
#define STEP(N, SL) WAITV(N); LSTAMP(); LBAR(); LSTAMP(); issue_stage(SL); LSTAMP();
    LSTAMP();
    __builtin_amdgcn_s_setprio(3);
    issue_stage(L_S0);
    LSTAMP();
    __builtin_amdgcn_s_setprio(1);
    int lane2 = lane, unit = b * 3 + L;
    asm volatile("" : "+v"(lane2), "+s"(unit));
    const bool retile = (L < 3) && (unit < 740);
    const int rk = retile ? unit / 10 : 0, rcu = retile ? unit - rk * 10 : 0;
    const char* rsrc = rk < 54 ? w2 : wo;
    unsigned rv[4], ra[4], rd[4];
    {
      const int kp = rk - 54, P = kp / 5, s5 = kp - P * 5;
      const bool lft = rk >= 54 && s5 == 4;
      const int gs = lft ? 40 : 4;
      const int r0 = rk < 54 ? 32 * rk : (lft ? 160 * P + 16 : 160 * P + 40 * s5);
      const int rh = rk < 54 ? 16 : (lft ? 2 : 20);
      const int c = lane2 & 7, e = (lane2 >> 3) & 3, g2 = lane2 >> 4, q2 = (lane2 >> 2) & 3;
      const unsigned v0 = (r0 + (lane2 >> 5) * gs + (lft ? 20 * (e & 1) + (e >> 1) : e)) * 1280 + rcu * 128 + ((((c >> 1) ^ g2) << 5) | ((c & 1) << 4));
      const unsigned base = L_MISC + L * 4096 + (4 * g2 + q2) * 128 + 8 * (lane2 & 3);
      const int sw = (2 * g2 + (q2 >> 1)) & 3;
#pragma unroll
      for (int n = 0; n < 4; ++n) {
        const int N = 4 * rcu + n, c5 = N / 5;
        rv[n] = v0 + ((n & 1) * 2 * gs + (n >> 1) * rh) * 1280;
        ra[n] = base + ((n ^ sw) << 5);
        rd[n] = WS_B + ((c5 * 74 + rk) * 5 + (N - c5 * 5)) * 1024 + lane2 * 16;
        PIN(rv[n]); PIN(ra[n]); PIN(rd[n]);
      }
    }
    TAG();
    int pst = SPF > 1 ? 5 : 3;
#define PF()
    PF();
    LSTAMP();
    issue_lo(L_S1);
    LSTAMP();
    WAITV(8); LSTAMP(); LBAR(); LSTAMP();
    __builtin_amdgcn_s_setprio(3);
    issue_hi(L_S1);
    PF();
    __builtin_amdgcn_s_setprio(1);
    asm volatile("s_mov_b32 m0, %5\n\ts_mov_b64 exec, %6\n\tglobal_load_lds_dwordx4 %0, %4 nt\n\t"
                 "s_add_u32 m0, m0, %7\n\ts_nop 0\n\tglobal_load_lds_dwordx4 %1, %4 nt\n\t"
                 "s_add_u32 m0, m0, %7\n\ts_nop 0\n\tglobal_load_lds_dwordx4 %2, %4 nt\n\t"
                 "s_add_u32 m0, m0, %7\n\ts_nop 0\n\tglobal_load_lds_dwordx4 %3, %4 nt\n\ts_mov_b64 exec, -1"
                 ::"v"(rv[0]), "v"(rv[1]), "v"(rv[2]), "v"(rv[3]), "s"(rsrc), "s"(retile ? L_MISC + L * 4096 : L_ONE + 16),
                   "s"(retile ? ~0ull : 1ull), "s"(retile ? 1024 : 0) : "memory", "scc");
    issue_lo(L_S2);
#define HALF(PREV, NEXT) LSTAMP(); LBAR(); LSTAMP(); __builtin_amdgcn_s_setprio(3); issue_hi(PREV); PF(); __builtin_amdgcn_s_setprio(1); issue_lo(NEXT); LSTAMP();
    WAITV(12);
    HALF(L_S2, L_S0)
    WAITV(8);
    {
      u32x4 f[4];
#pragma unroll
      for (int n = 0; n < 4; ++n) f[n] = cat(ldstr(ra[n]), ldstr(ra[n] + 2048));
      LSTAMP(); LBAR(); LSTAMP();
      __builtin_amdgcn_s_setprio(3);
      issue_hi(L_S0);
      if (retile) {
#pragma unroll
        for (int n = 0; n < 4; ++n) gst128_sc1(rd[n], ws, f[n]);
      }
      PF();
      __builtin_amdgcn_s_setprio(1);
      issue_lo(L_S1);
      LSTAMP();
      if (retile) { WAITV(12); } else { WAITV(8); }
      unsigned sp = L_S1, sb = L_S2;
#pragma unroll 1
      for (int t = 2; t < 7; ++t) {
        HALF(sp, sb)
        sp = sb;
        sb = sb == L_S0 ? L_S1 : sb + SLOT;
        if (t == 3 && L == 3 && lane == 0) gst64_sc1(b * 8, ws + WS_RT, tagv);
        WAITV(8);
      }
      LSTAMP(); LBAR(); LSTAMP();
      issue_hi(sp);
    }
    const int k0 = isq ? 54 - 2 * NQ2 : 0, nb = isq ? NQ2 : NH2;
    const int rot2 = P2ROT == 1 ? (((idx + (idx >> 2)) & 3) * nb) >> 2 : P2ROT == 2 ? (((idx + (idx >> 2)) & 1) * nb) >> 1 : 0;
    const char* bbase = ws + WS_B + ct * 74 * 5120;
    const unsigned boff1k = boff + 1024;
    unsigned bl = L_BR;
    int bs = 0, bk = k0 + 2 * rot2;
    auto issue_b = [&](int n) {
#pragma unroll 1
      for (int j = 0; j < n; ++j) {
        glds5_sc1(boff, boff1k, bbase + bk * 5120, bl);
        glds5_sc1(boff, boff1k, bbase + bk * 5120 + 5120, bl + 5120);
        bl = bl == L_BR + 71680 ? L_BR : bl + 10240;
        ++bs;
        bk += 2;
        if (bs < nb && bk == k0 + 2 * nb) bk = k0;
        if (bs == nb) bk = 54;
      }
    };
    __builtin_amdgcn_s_setprio(3);
    if (L == 1) {
      u32x4 r0, r1;
      asm volatile("global_load_dwordx4 %0, %2, %3 sc1\n\tglobal_load_dwordx4 %1, %2, %3 offset:16 sc1\n\ts_waitcnt vmcnt(2)\n\ts_barrier\n\ts_waitcnt vmcnt(0)"
                   : "=&v"(r0), "=&v"(r1) : "v"(lane * 32), "s"(ws + WS_RT) : "memory");
      const unsigned bad = (r0[0] ^ tlo) | (r0[1] ^ thi) | (r0[2] ^ tlo) | (r0[3] ^ thi) | (r1[0] ^ tlo) | (r1[1] ^ thi) | (r1[2] ^ tlo) | (r1[3] ^ thi);
      if (__all(bad == 0)) {
        issue_b(3);
        LBAR();
        issue_b(5);
      } else {
        LBAR();
#pragma unroll 1
        for (int i = 0; i < 4; ++i) poll_words(ws + WS_RT + i * 512, lane * 8, tlo, thi);
        issue_b(8);
      }
      if (b == 0 && lane == 0) gst64_sc1(0, ws + WS_EPOCH, tagv);
    } else {
      WAITV(0);
      LBAR();
      if (!isq && L >= 2) return;
      LBAR();
    }
    LSTAMP();
    const char* abase = ws + WS_A + rt2 * 8 * 74 * 1024;
    unsigned al = L_AR;
    unsigned ao[8];
#pragma unroll
    for (int i = 0; i < 8; ++i) ao[i] = boff + (i ^ (isq ? 0 : 1)) * 74 * 1024;
    int ak = k0 + 2 * rot2;
    auto issue_a = [&]() {
        asm volatile(
            "s_mov_b32 m0, %9\n\ts_nop 0\n\tglobal_load_lds_dwordx4 %0, %8 sc1\n\tglobal_load_lds_dwordx4 %0, %8 offset:1024 sc1\n\t"
            "s_add_u32 m0, m0, 2048\n\ts_nop 0\n\tglobal_load_lds_dwordx4 %1, %8 sc1\n\tglobal_load_lds_dwordx4 %1, %8 offset:1024 sc1\n\t"
            "s_add_u32 m0, m0, 2048\n\ts_nop 0\n\tglobal_load_lds_dwordx4 %2, %8 sc1\n\tglobal_load_lds_dwordx4 %2, %8 offset:1024 sc1\n\t"
            "s_add_u32 m0, m0, 2048\n\ts_nop 0\n\tglobal_load_lds_dwordx4 %3, %8 sc1\n\tglobal_load_lds_dwordx4 %3, %8 offset:1024 sc1\n\t"
            "s_add_u32 m0, m0, 2048\n\ts_nop 0\n\tglobal_load_lds_dwordx4 %4, %8 sc1\n\tglobal_load_lds_dwordx4 %4, %8 offset:1024 sc1\n\t"
            "s_add_u32 m0, m0, 2048\n\ts_nop 0\n\tglobal_load_lds_dwordx4 %5, %8 sc1\n\tglobal_load_lds_dwordx4 %5, %8 offset:1024 sc1\n\t"
            "s_add_u32 m0, m0, 2048\n\ts_nop 0\n\tglobal_load_lds_dwordx4 %6, %8 sc1\n\tglobal_load_lds_dwordx4 %6, %8 offset:1024 sc1\n\t"
            "s_add_u32 m0, m0, 2048\n\ts_nop 0\n\tglobal_load_lds_dwordx4 %7, %8 sc1\n\tglobal_load_lds_dwordx4 %7, %8 offset:1024 sc1"
            ::"v"(ao[0]), "v"(ao[1]), "v"(ao[2]), "v"(ao[3]), "v"(ao[4]), "v"(ao[5]), "v"(ao[6]), "v"(ao[7]), "s"(abase + ak * 1024), "s"(al)
            : "memory", "scc");
        ak += 2;
        if (ak == k0 + 2 * nb) ak = k0;
        al = al == L_AR + 49152 ? L_AR : al + 16384;
    };
    const char* fl = ws + WS_HF + rt2 * 128;
    unsigned fo = (lane & 7) * 8;
    bool ok = false;
    if (isq) {
      unsigned ia[4];
#pragma unroll
      for (int i = 0; i < 4; ++i) {
        const int sl = L + 4 * i < 14 ? L + 4 * i : L;
        ia[i] = *(LAS unsigned*)(size_t)(L_TAB + (sl < 7 ? sl : sl - 7) * 256 + lane * 4) + (sl < 7 ? 0 : 3360);
        PIN(ia[i]);
      }
      unsigned my1 = *(LAS unsigned*)(size_t)(L_TAB + (rt < 7 ? rt : rt - 7) * 256 + (16 * L + c16) * 4) == L_ONE + 16 ? 0u : ~0u;
      unsigned h1o = g * 4 * GR_BYTES + c16 * 16;
      unsigned h2o = c16 * 16 + (g & 1) * 8, htag = (g & 1) ? thi : tlo;
      PIN(my1); PIN(h1o); PIN(h2o); PIN(htag);
      LBAR();
      WAITV(0);
      LBAR();
      LSTAMP();
      u32x2 f = gld64_sc1(fo, fl);
      u32x4 pv[4];
      if (rt < 14) {
        const char* pb = ws + WS_PART + (jg ^ 2) * 16 * GR_BYTES + rt * 1024 + L * 256;
        __builtin_amdgcn_s_sleep(HOP1_SLEEP);
#pragma unroll 1
        for (unsigned spins = 0; spins < SPIN_CAP; ++spins) {
#pragma unroll
          for (int i = 0; i < 4; ++i) pv[i] = gld128_sc1(h1o, pb + i * GR_BYTES);
          asm volatile("s_waitcnt vmcnt(0)" : "+v"(pv[0]), "+v"(pv[1]), "+v"(pv[2]), "+v"(pv[3])::"memory");
          LSTAMP();
          unsigned bad = 0;
#pragma unroll
          for (int i = 0; i < 4; ++i) bad |= (pv[i][1] ^ tlo) | (pv[i][3] ^ thi);
          if (__all((bad & my1) == 0)) break;
          __builtin_amdgcn_s_sleep(RETRY_SLEEP);
        }
        float s[4];
#pragma unroll
        for (int e = 0; e < 4; ++e) {
          float t[4];
#pragma unroll
          for (int i = 0; i < 4; ++i) t[i] = __builtin_bit_cast(float, (e & 1) ? pv[i][e & 2] & 0xffff0000u : pv[i][e & 2] << 16);
          s[e] = (t[0] + t[1]) + (t[2] + t[3]);
        }
        const auto ra = __builtin_amdgcn_permlane32_swap(fbits(s[0]), fbits(s[1]), false, false);
        const auto rc = __builtin_amdgcn_permlane32_swap(fbits(s[2]), fbits(s[3]), false, false);
        const auto rd = __builtin_amdgcn_permlane16_swap(fbits(bitsf(ra[0]) + bitsf(ra[1])), fbits(bitsf(rc[0]) + bitsf(rc[1])), false, false);
        unsigned u0 = fbits(bitsf(rd[0]) + bitsf(rd[1])), u1 = u0;
        PIN(u1);
        const auto re = __builtin_amdgcn_permlane32_swap(u0, u1, false, false);
        if (lane < 32) gst64_sc1(h2o, ws + WS_RED + (jg ^ 2) * GR_BYTES + rt * 1024 + L * 256, (u32x2){pk2(bitsf(re[0]), bitsf(re[1])), htag});
      }
      LSTAMP();
      if (rt >= 14) WAITV(0);
      PIN(f);
      if (L == 0) {
        if (!__all(f[0] == tlo && f[1] == thi)) poll_words(fl, fo, tlo, thi);
#pragma unroll 1
        for (int i = 0; i < APRE; ++i) issue_a();
      } else {
        __builtin_amdgcn_s_sleep(HOP2_SLEEP);
      }
      const char* rb = ws + WS_RED + jg * GR_BYTES + L * 1024;
#pragma unroll 1
      for (unsigned spins = 0; spins < SPIN_CAP; ++spins) {
#pragma unroll
        for (int i = 0; i < 4; ++i) pv[i] = gld128_sc1(boff, rb + ((i < 3 || L < 2) ? i * 4096 : 0));
        asm volatile("s_waitcnt vmcnt(0)" : "+v"(pv[0]), "+v"(pv[1]), "+v"(pv[2]), "+v"(pv[3])::"memory");
        LSTAMP();
        unsigned bad = 0;
#pragma unroll
        for (int i = 0; i < 4; ++i) {
          asm volatile("ds_write2_b32 %0, %1, %2 offset0:0 offset1:1" ::"v"(ia[i]), "v"(pv[i][0]), "v"(pv[i][2]) : "memory");
          bad |= (pv[i][1] ^ tlo) | (pv[i][3] ^ thi);
        }
        if (__all(bad == 0)) break;
        __builtin_amdgcn_s_sleep(RETRY_SLEEP);
      }
      BAR();
      LSTAMP();
    } else {
      LBAR();
      if (L == 0 && lane == 0) gst64_sc1(rt * 128 + jg * 8, ws + WS_HF, tagv);
    }
    __builtin_amdgcn_s_setprio(0);
    if (L == 1) {
      WAITV(0);
      LBAR();
#pragma unroll 1
      for (int s = 0; s < nb; ++s) {
        WAITV(40);
        LBAR();
        issue_b(1);
      }
      WAITV(40); LBAR();
      WAITV(30); LBAR();
      issue_b(2);
      WAITV(30); LBAR();
      WAITV(10); LBAR();
      WAITV(0); LBAR();
    } else if (L == 0) {
      LSTAMP();
      if (!isq) poll_words(fl, fo, tlo, thi);
      LSTAMP();
#pragma unroll 1
      for (int s = isq ? APRE - 4 : -4; s < nb - 4; ++s) {
        if (s == (isq ? APRE - 4 : -1) || s >= 0) { WAITV(32); LBAR(); LSTAMP(); }
        issue_a();
      }
      fl = ws + WS_OF + rt2 * 512;
      fo = lane * 8;
      u32x2 f;
      asm volatile("s_waitcnt vmcnt(32)\n\ts_barrier\n\tglobal_load_dwordx2 %0, %1, %2 sc1\n\ts_waitcnt vmcnt(17)\n\ts_barrier\n\t"
                   "s_waitcnt vmcnt(1)\n\ts_barrier\n\ts_waitcnt vmcnt(0)"
                   : "=&v"(f) : "v"(fo), "s"(fl) : "memory");
      LSTAMP();
      ok = __all(f[0] == tlo && f[1] == thi);
      if (!ok) { LBAR(); poll_words(fl, fo, tlo, thi); }
      LSTAMP();
      const char* asrc = abase + 54 * 1024;
#pragma unroll 1
      for (int s = 0; s < 5; ++s) {
        if (s == 2 && ok) LBAR();
        if (s >= 3) { WAITV(32); LBAR(); LSTAMP(); }
        asm volatile(
            "s_mov_b32 m0, %5\n\ts_nop 0\n\tglobal_load_lds_dwordx4 %0, %4 sc1\n\tglobal_load_lds_dwordx4 %0, %4 offset:1024 sc1\n\t"
            "global_load_lds_dwordx4 %0, %4 offset:2048 sc1\n\tglobal_load_lds_dwordx4 %0, %4 offset:3072 sc1\n\t"
            "s_add_u32 m0, m0, 4096\n\ts_nop 0\n\tglobal_load_lds_dwordx4 %1, %4 sc1\n\tglobal_load_lds_dwordx4 %1, %4 offset:1024 sc1\n\t"
            "global_load_lds_dwordx4 %1, %4 offset:2048 sc1\n\tglobal_load_lds_dwordx4 %1, %4 offset:3072 sc1\n\t"
            "s_add_u32 m0, m0, 4096\n\ts_nop 0\n\tglobal_load_lds_dwordx4 %2, %4 sc1\n\tglobal_load_lds_dwordx4 %2, %4 offset:1024 sc1\n\t"
            "global_load_lds_dwordx4 %2, %4 offset:2048 sc1\n\tglobal_load_lds_dwordx4 %2, %4 offset:3072 sc1\n\t"
            "s_add_u32 m0, m0, 4096\n\ts_nop 0\n\tglobal_load_lds_dwordx4 %3, %4 sc1\n\tglobal_load_lds_dwordx4 %3, %4 offset:1024 sc1\n\t"
            "global_load_lds_dwordx4 %3, %4 offset:2048 sc1\n\tglobal_load_lds_dwordx4 %3, %4 offset:3072 sc1"
            ::"v"(ao[0]), "v"(ao[2]), "v"(ao[4]), "v"(ao[6]), "s"(asrc), "s"(al) : "memory", "scc");
        asrc += 4096;
        al = al == L_AR + 49152 ? L_AR : al + 16384;
      }
      WAITV(32); LBAR(); LSTAMP();
      WAITV(16); LBAR(); LSTAMP();
      WAITV(0); LBAR(); LSTAMP();
    }
    return;
  }

  asm volatile("s_waitcnt lgkmcnt(0)" ::"s"(ka), "s"(kws));
  const int mw = w & 3, nw = w >> 2;
  const char *wphi = a_wphi, *gate = a_gate;
  char *out = a_out, *ws = a_ws;
  u64 epoch;
  {
    const char *px = a_x, *pq = a_wqkv, *ph = a_w1;
    asm volatile("s_load_dwordx2 %0, %1, 0x0" : "=&s"(epoch) : "s"(ws), "s"(px), "s"(pq), "s"(ph), "s"(wphi), "s"(gate), "s"(out) : "memory");
  }
  unsigned warm = 0;
  if (w == 4) {
    const unsigned o = ((idx * 8 + (lane & 7)) * 128);
    asm volatile("global_load_dword %0, %1, %2" : "=v"(warm) : "v"(o < CODE_BYTES ? o : 0), "s"(pc & ~127ull) : "memory");
  }
  {
    if (w == 3) {
      __builtin_amdgcn_s_setprio(3);
      const char* src = isq ? wphi + jg * 3200 : gate;
      const unsigned lim = isq ? 3184 : 1264;
#pragma unroll
      for (int n = 0; n < 4; ++n) {
        const unsigned o = n * 1024 + boff;
        glds(o < lim ? o : lim, src, L_WPHI + n * 1024);
      }
      __builtin_amdgcn_s_setprio(0);
    }
  }
  if (tid < 128) sts128(L_ZERO + tid * 16, (u32x4){0u, 0u, 0u, 0u});
  if (tid < 448) {
    const int hd = tid >= 224 ? 1 : 0, j = tid - 224 * hd, r = j - 160;
    const int ft = r >= 40 ? 2 : (r >= 20 ? 1 : 0), r2 = r - 20 * ft, gq = (r2 * 13) >> 6;
    const int d = j < 160 ? (j & 15) : 16 + r2 - 5 * gq, quad = j < 160 ? (j >> 4) : 4 * ft + gq;
    *(LAS unsigned*)(size_t)(L_TAB + tid * 4) = j < 210 ? L_KV + hd * 1680 + d * 80 + quad * 8 : L_ONE + 16;
  }
  if (tid == 128) sts128(L_ONE, (u32x4){0x3f803f80u, 0x3f803f80u, 0x3f803f80u, 0x3f803f80u});
  TAG();

  const unsigned xa = L_S0 + (32 * mw + c16) * 128 + ((g ^ ((c16 >> 1) & 7)) << 4);
  const unsigned wrd = L_S0 + 16384 + (4 * g + q4) * 480 + (isq ? 40 * (p4 & 1) + 240 * (p4 >> 1) : 8 * p4);
  const bool gatew = !isq && jg == 7 && nw == 1;
  const bool wide = nw == 0 && jg < 6;
  f32x4 acc[8][2];
  unsigned pbase = (p4 == g) ? L_WPHI + (2 * nw + (g & 1)) * 800 + q4 * 40 : L_ZERO;
  unsigned phiw = L_PHIK + (32 * mw + 16 * (g >> 1) + c16) * 352 + 80 * (2 * nw + (g & 1));
  unsigned vw = L_V + (32 * mw + c16) * 160 + 40 * g + 24 * nw;
  unsigned ab = L_PHIK + (4 * g + q4) * 352 + 80 * mw + 8 * p4;
  const bool vreal = nw == 0 || p4 == 0;
  unsigned vb = vreal ? L_V + (4 * g + q4) * 160 + 40 * mw + 32 * nw + 8 * p4 : L_ONE;
  unsigned po = (mw * 224 + (nw == 0 ? lane : 160 + 5 * g + c16)) * 16;
  unsigned ab2 = ab + 64 - 8 * (p4 & 2), vs = vreal ? 2560 : 0;
  PIN(pbase); PIN(phiw); PIN(vw); PIN(ab); PIN(vb); PIN(po); PIN(ab2); PIN(vs);
  STAMP(1);
  WAITV(0);
  if (isq) {
    if (nw == 0) gemm1_loop<8, true, false>(acc, xa, wrd, 0, 0);
    else gemm1_loop<7, true, false>(acc, xa, wrd, 0, 0);
  } else {
    const unsigned wh = wrd + (nw == 0 ? 0 : (jg < 6 ? 256 : 192));
    if (wide) gemm1_loop<8, false, false>(acc, xa, wh, 0, 0);
    else if (gatew) gemm1_loop<7, false, true>(acc, xa, wh, L_GATE + g * 16 + rot * 128, 2 * rot);
    else gemm1_loop<6, false, false>(acc, xa, wh, 0, 0);
  }
  STAMP(2);
  PIN(warm);

  unsigned kq[10];
  if (!isq) {
    const int k0 = (hstart >> 6) + (nw == 0 ? 0 : (jg < 6 ? 4 : 3));
    char* abase = ws + WS_A + ((rt * 8 + 2 * mw) * 74 + k0) * 1024;
#pragma unroll
    for (int pr = 0; pr < 4; ++pr) {
      if (pr < 3 || wide) {
        const u32x4 f0 = silu8(acc[2 * pr][0], acc[2 * pr + 1][0]), f1 = silu8(acc[2 * pr][1], acc[2 * pr + 1][1]);
        gst128_sc1(boff, abase + pr * 1024, f0);
        gst128_sc1(boff, abase + (74 + pr) * 1024, f1);
      }
    }
    if (gatew && g == 0) {
#pragma unroll
      for (int mt = 0; mt < 2; ++mt) {
        const float gv = __builtin_amdgcn_rcpf(1.f + __builtin_amdgcn_exp2f(acc[6][mt][0] * -1.4426950408889634f));
        gst32_sc1((rt * 128 + 32 * mw + 16 * mt + c16) * 4, ws + WS_G, __builtin_bit_cast(unsigned, gv));
      }
    }
    STAMP(3);
    WAITV(0);
    BAR();
    STAMP(4);
#pragma unroll
    for (int i = 0; i < 10; ++i) kq[i] = 0;
  } else {
    WPhi wf;
    wphi_read(wf, pbase);
    SB();
    unsigned kk[10];
#pragma unroll
    for (int i = 0; i < 10; ++i) {
      const f32x4 a = acc[i >> 1][0], c = acc[i >> 1][1];
      kq[i] = pk2(a[2 * (i & 1)], a[2 * (i & 1) + 1]); kk[i] = pk2(c[2 * (i & 1)], c[2 * (i & 1) + 1]);
    }
    SB();
#pragma unroll
    for (int i = 0; i < 10; ++i) {
      const auto r = __builtin_amdgcn_permlane32_swap(kq[i], kk[i], false, false);
      kq[i] = r[0]; kk[i] = r[1];
    }
    SB();
    f32x4 pp[5];
    phi_proj(wf, kk, pp);
    SB();
#pragma unroll
    for (int i = 5; i < 8; ++i) {
      if (i < 7 || nw == 0) {
#pragma unroll
        for (int mt = 0; mt < 2; ++mt) {
          const f32x4 a = acc[i][mt];
          sts64(vw + mt * 2560 + 8 * (i - 5), (u32x2){pk2(a[0], a[1]), pk2(a[2], a[3])});
        }
      }
    }
    SB();
    unsigned phi[20];
    dual_softmax(pp, phi);
#pragma unroll
    for (int i = 0; i < 5; ++i) sts128(phiw + 16 * i, (u32x4){phi[4 * i], phi[4 * i + 1], phi[4 * i + 2], phi[4 * i + 3]});
    unsigned va[8];
    va[0] = vb;
#pragma unroll
    for (int i = 1; i < 8; ++i) { va[i] = va[i - 1] + vs; PIN(va[i]); }
    BAR();
    STAMP(3);
    u32x4 vf[4], pf[4][3];
#pragma unroll
    for (int ks = 0; ks < 4; ++ks) {
      const unsigned o = ks * 32 * 352;
      vf[ks] = cat(ldstr(va[2 * ks]), ldstr(va[2 * ks + 1]));
      pf[ks][0] = cat(ldstr(ab + o), ldstr(ab + o + 16 * 352));
      pf[ks][1] = cat(ldstr(ab + o + 32), ldstr(ab + o + 32 + 16 * 352));
      pf[ks][2] = cat(ldstr(ab2 + o), ldstr(ab2 + o + 16 * 352));
      SB();
    }
    f32x4 kv[3];
#pragma unroll
    for (int i = 0; i < 3; ++i) kv[i] = (f32x4){0.f, 0.f, 0.f, 0.f};
#pragma unroll
    for (int ks = 0; ks < 4; ++ks)
#pragma unroll
      for (int i = 0; i < 3; ++i) kv[i] = mfma(pf[ks][i], vf[ks], kv[i]);
    char* pb = ws + WS_PART + (jg * 16 + rt) * GR_BYTES;
    const unsigned fs = nw == 0 ? 1024 : 320;
    if (nw == 0 || c16 < 5) {
      gst128_sc1(po, pb, (u32x4){pk2(kv[0][0], kv[0][1]), tlo, pk2(kv[0][2], kv[0][3]), thi});
      gst128_sc1(po, pb + fs, (u32x4){pk2(kv[1][0], kv[1][1]), tlo, pk2(kv[1][2], kv[1][3]), thi});
      if (g < 2) gst128_sc1(po, pb + 2 * fs, (u32x4){pk2(kv[2][0], kv[2][1]), tlo, pk2(kv[2][2], kv[2][3]), thi});
    }
  }

  {
    P2 p;
    p.aa = L_AR + mw * 4096 + boff;
    p.ba = L_BR + nw * 3072 + boff;
    p.st = 0;
    f32x4 ob[2][3], oa[3];
#pragma unroll
    for (int i = 0; i < 6; ++i) ob[i / 3][i % 3] = (f32x4){0.f, 0.f, 0.f, 0.f};
    if (isq) {
      BAR();
      STAMP(5);
      unsigned phq[20];
      {
        WPhi wf;
        wphi_read(wf, pbase);
        SB();
        f32x4 pp[5];
        phi_proj(wf, kq, pp);
        dual_softmax(pp, phq);
      }
#pragma unroll
      for (int i = 0; i < 10; ++i) {
        const int a = i < 8 ? i : 8 + i, c = i < 8 ? i + 8 : 10 + i;
        const auto r = __builtin_amdgcn_permlane32_swap(phq[a], phq[c], false, false);
        phq[a] = r[0]; phq[c] = r[1];
      }
#pragma unroll
      for (int i = 0; i < 10; ++i) {
        const int a = i < 8 ? (i & 3) + 8 * (i >> 2) : 2 * i, c = i < 8 ? a + 4 : a + 1;
        const auto r = __builtin_amdgcn_permlane16_swap(phq[a], phq[c], false, false);
        phq[a] = r[0]; phq[c] = r[1];
      }
      unsigned k0a = L_KV + 2 * nw * 1680 + c16 * 80, k1a = L_KV + 2 * nw * 1680 + (p4 == 0 ? 16 + q4 : 20) * 80;
      unsigned k0b = k0a + 64 + 4 * g, k1b = k1a + 64 + 4 * g;
      k0a += 16 * g; k1a += 16 * g;
      unsigned ow = (g & 1) * 74 * 1024 + ((2 * (jg & 1) + nw) * 16 + c16) * 16 + (g >> 1) * 8;
      PIN(k0a); PIN(k1a); PIN(k0b); PIN(k1b); PIN(ow);
#pragma unroll
      for (int i = 0; i < 20; ++i) PIN(phq[i]);
      STAMP(6);
      BAR();
      STAMP(7);
      {
        u32x4 a32[2][2];
        unsigned a16[2][2];
#pragma unroll
        for (int h = 0; h < 2; ++h) {
          a32[h][1] = lds128(k1a + h * 1680); a16[h][1] = *(LAS unsigned*)(size_t)(k1b + h * 1680);
          a32[h][0] = lds128(k0a + h * 1680); a16[h][0] = *(LAS unsigned*)(size_t)(k0b + h * 1680);
        }
        SB();
        f32x4 o[4][2];
#pragma unroll
        for (int t = 1; t >= 0; --t) {
#pragma unroll
          for (int u = 0; u < 4; ++u)
            o[u][t] = mfma(a32[u & 1][t], (u32x4){phq[4 * u], phq[4 * u + 1], phq[4 * u + 2], phq[4 * u + 3]}, (f32x4){0.f, 0.f, 0.f, 0.f});
#pragma unroll
          for (int u = 0; u < 4; ++u)
            o[u][t] = __builtin_amdgcn_mfma_f32_16x16x16bf16_1k(__builtin_bit_cast(s16x4, (u32x2){a16[u & 1][t], 0u}),
                                                                 __builtin_bit_cast(s16x4, (u32x2){phq[16 + u], 0u}), o[u][t], 0, 0, 0);
        }
        char* abase = ws + WS_A + ((rt * 8 + 2 * mw) * 74 + 54 + (jg >> 1) * 5) * 1024;
        float sc[4];
#pragma unroll
        for (int u = 0; u < 4; ++u) sc[u] = __builtin_amdgcn_rcpf(o[u][1][1] + 1e-6f);
        unsigned l0 = pk2(o[0][1][0] * sc[0], o[1][1][0] * sc[1]), l1 = pk2(o[2][1][0] * sc[2], o[3][1][0] * sc[3]);
#pragma unroll
        for (int mt = 0; mt < 2; ++mt) {
          const f32x4 a = o[2 * mt][0] * sc[2 * mt], c = o[2 * mt + 1][0] * sc[2 * mt + 1];
          gst128_sc1(boff, abase + (mt * 74 + 2 * (jg & 1) + nw) * 1024, (u32x4){pk2(a[0], a[1]), pk2(a[2], a[3]), pk2(c[0], c[1]), pk2(c[2], c[3])});
        }
        const auto r = __builtin_amdgcn_permlane16_swap(l0, l1, false, false);
        gst64_sc1(ow, abase + 4 * 1024, (u32x2){r[0], r[1]});
      }
      STAMP(4);
    }
#pragma unroll
    for (int i = 0; i < 6; ++i) PIN(ob[i / 3][i % 3]);
    PIN(p.aa); PIN(p.ba);
    __builtin_amdgcn_s_setprio(2);
    BAR();
    if (!isq) STAMP(5);
    p2_first(p);
#pragma unroll 1
    for (int part = 0; part < 2; ++part) {
      p2_run(p, ob, part == 0 ? OFL : (isq ? NQ2 : NH2) - OFL, nw);
      if (part == 0 && isq) {
        WAITV(0);
        if (lane == 0) gst64_sc1(rt * 512 + (jg * 8 + w) * 8, ws + WS_OF, tagv);
        STAMP(8);
      }
    }
    char* pbuf = ws + WS_P + ((rt2 * 8 + ct) * 16 + w) * 3072;
#pragma unroll
    for (int n = 0; n < 3; ++n) {
      oa[n] = (f32x4){0.f, 0.f, 0.f, 0.f};
      if (n < 2 || nw == 0)
        gst128_sc1(boff, pbuf + (isq ? 0 : 8 * 3072) + n * 1024, (u32x4){pk2(ob[1][n][0], ob[1][n][1]), tlo, pk2(ob[1][n][2], ob[1][n][3]), thi});
    }
    STAMP(9);
    p.sa = p.st;
    p2a_run(p, oa, p.aa, 3, nw);
    const unsigned row = rt2 * 128 + 32 * mw + (isq ? 0 : 16) + c16;
    const char* prd = pbuf + (isq ? 8 * 3072 : 0);
    const unsigned p2o = boff + (nw == 0 ? 2048 : 0);
    u32x4 pt[3];
    unsigned gb = gld32_sc1(row * 4, ws + WS_G);
    pt[0] = gld128_sc1(boff, prd); pt[1] = gld128_sc1(boff, prd + 1024); pt[2] = gld128_sc1(p2o, prd);
    p2a_run(p, oa, p.aa, 2, nw);
    STAMP(10);
    asm volatile("s_waitcnt vmcnt(0)" : "+v"(gb), "+v"(pt[0]), "+v"(pt[1]), "+v"(pt[2])::"memory");
#pragma unroll 1
    for (unsigned spins = 0; spins < SPIN_CAP; ++spins) {
      const unsigned bad = (pt[0][1] ^ tlo) | (pt[0][3] ^ thi) | (pt[1][1] ^ tlo) | (pt[1][3] ^ thi) | (pt[2][1] ^ tlo) | (pt[2][3] ^ thi);
      if (__all(bad == 0)) break;
      __builtin_amdgcn_s_sleep(8);
      asm volatile("global_load_dwordx4 %0, %3, %5 sc1\n\tglobal_load_dwordx4 %1, %3, %5 offset:1024 sc1\n\tglobal_load_dwordx4 %2, %4, %5 sc1\n\ts_waitcnt vmcnt(0)"
                   : "=&v"(pt[0]), "=&v"(pt[1]), "=&v"(pt[2]) : "v"(boff), "v"(p2o), "s"(prd) : "memory");
    }
    const unsigned oo = row * 1280 + ct * 160 + nw * 96 + g * 8;
    const float gv = bitsf(gb), omg = 1.f - gv;
#pragma unroll
    for (int n = 0; n < 3; ++n) {
      if (n < 2 || nw == 0) {
        const f32x4 q = {bitsf(pt[n][0] << 16), bitsf(pt[n][0] & 0xffff0000u), bitsf(pt[n][2] << 16), bitsf(pt[n][2] & 0xffff0000u)};
        const f32x4 r = (ob[0][n] + q) * omg + oa[n] * gv;
        gst64(oo + n * 32, out, (u32x2){pk2(r[0], r[1]), pk2(r[2], r[3])});
      }
    }
  }
}

extern "C" void kernel_launch(void* const* d_in, const int* in_sizes, int n_in, void* d_out, int out_size, void* d_ws,
                              size_t ws_size, hipStream_t stream) {
  (void)in_sizes; (void)n_in; (void)out_size; (void)ws_size;
  (void)hipFuncSetAttribute((const void*)fused_kernel, hipFuncAttributeMaxDynamicSharedMemorySize, LDS_BYTES);
  fused_kernel<<<256, 768, LDS_BYTES, stream>>>((const char*)d_in[0], (const char*)d_in[1], (const char*)d_in[2],
                                                (const char*)d_in[3], (const char*)d_in[5], (const char*)d_in[6],
                                                (const char*)d_in[7], (char*)d_out, (char*)d_ws);
}
```

```cpp
#include <hip/hip_runtime.h>
#include <stdint.h>

typedef __attribute__((ext_vector_type(8))) __bf16 bf16x8;
typedef __attribute__((ext_vector_type(2))) __bf16 bf16x2;
typedef __attribute__((ext_vector_type(4))) short s16x4;
typedef __attribute__((ext_vector_type(4))) float f32x4;
typedef __attribute__((ext_vector_type(2))) float f32x2;
typedef __attribute__((ext_vector_type(4))) unsigned u32x4;
typedef __attribute__((ext_vector_type(2))) unsigned u32x2;
typedef unsigned long long u64;
#define LAS __attribute__((address_space(3)))
#define DI __device__ __forceinline__

constexpr unsigned WS_EPOCH = 0;
constexpr unsigned WS_RT = 1024;
constexpr unsigned WS_HF = 4096;
constexpr unsigned WS_OF = 12288;
constexpr unsigned WS_G = 20480;
constexpr unsigned WS_RED = 49152;
constexpr unsigned GR_BYTES = 896 * 16;
constexpr unsigned WS_PART = WS_RED + 8 * GR_BYTES;
constexpr unsigned WS_B = 2u << 20;
constexpr unsigned WS_A = 5u << 20;
constexpr unsigned WS_P = 15u << 20;
constexpr unsigned WS_PROF = 24u << 20;
static_assert(WS_PART + 128 * GR_BYTES <= WS_B, "ws");

constexpr unsigned SLOT = 47104;
constexpr unsigned L_S1 = 0, L_S2 = SLOT, L_S0 = 2 * SLOT, L_MISC = 3 * SLOT;
constexpr unsigned L_PHIK = L_S2 + 40960;
constexpr unsigned L_V = L_PHIK + 128 * 352;
constexpr unsigned L_BR = 0;
constexpr unsigned L_AR = 81920;
constexpr unsigned L_KV = 147456;
constexpr unsigned L_WPHI = 154240;
constexpr unsigned L_GATE = L_WPHI;
constexpr unsigned L_ZERO = L_WPHI + 4096;
constexpr unsigned L_ONE = L_ZERO + 2048;
constexpr unsigned L_TAB = L_ONE + 32;
constexpr unsigned LDS_BYTES = 163840;
static_assert(L_ONE + 32 + 3360 <= LDS_BYTES, "lds");

#define WPOL ""
#define HOP1_SLEEP 8
#define HOP2_SLEEP 10
#define RT_SLEEP 0
#define OFL 1
#define TLBM 0
#define SPF 0
#define P2ROT 2
#define RETRY_SLEEP 2
#define POLL_SLEEP 2
#define APRE 2
#define NQ2 7
#define SG 2
constexpr int NH2 = 27 - NQ2;
constexpr unsigned CODE_BYTES = 22528;
constexpr unsigned SPIN_CAP = 1u << 16;

DI unsigned pk2(float a, float b) {
  bf16x2 v = __builtin_convertvector((f32x2){a, b}, bf16x2);
  return __builtin_bit_cast(unsigned, v);
}
DI unsigned fbits(float f) { return __builtin_bit_cast(unsigned, f); }
DI float bitsf(unsigned u) { return __builtin_bit_cast(float, u); }
DI f32x4 mfma(u32x4 a, u32x4 b, f32x4 c) {
  return __builtin_amdgcn_mfma_f32_16x16x32_bf16(__builtin_bit_cast(bf16x8, a), __builtin_bit_cast(bf16x8, b), c, 0, 0, 0);
}
DI u32x4 lds128(unsigned a) { return *(LAS u32x4*)(size_t)a; }
DI u32x2 lds64(unsigned a) { return *(LAS u32x2*)(size_t)a; }
DI void sts128(unsigned a, u32x4 v) { *(LAS u32x4*)(size_t)a = v; }
DI void sts64(unsigned a, u32x2 v) { *(LAS u32x2*)(size_t)a = v; }
DI u32x2 ldstr(unsigned a) {
  return __builtin_bit_cast(u32x2, __builtin_amdgcn_ds_read_tr16_b64_v4i16((LAS s16x4*)(size_t)a));
}
DI u32x4 cat(u32x2 a, u32x2 b) { return (u32x4){a[0], a[1], b[0], b[1]}; }
DI void glds(unsigned voff, const void* sbase, unsigned lds) {
  asm volatile("s_mov_b32 m0, %2\n\ts_nop 0\n\tglobal_load_lds_dwordx4 %0, %1" ::"v"(voff), "s"(sbase), "s"(lds) : "memory");
}
DI void glds_nt(unsigned voff, const void* sbase, unsigned lds) {
  asm volatile("s_mov_b32 m0, %2\n\ts_nop 0\n\tglobal_load_lds_dwordx4 %0, %1 nt" ::"v"(voff), "s"(sbase), "s"(lds) : "memory");
}
DI void glds5_sc1(unsigned voff, unsigned voff1k, const void* sbase, unsigned lds) {
  asm volatile(
      "s_mov_b32 m0, %2\n\ts_nop 0\n\t"
      "global_load_lds_dwordx4 %0, %1 sc1\n\t"
      "global_load_lds_dwordx4 %0, %1 offset:1024 sc1\n\t"
      "global_load_lds_dwordx4 %0, %1 offset:2048 sc1\n\t"
      "global_load_lds_dwordx4 %0, %1 offset:3072 sc1\n\t"
      "s_add_u32 m0, m0, 1024\n\ts_nop 0\n\t"
      "global_load_lds_dwordx4 %3, %1 offset:3072 sc1" ::"v"(voff), "s"(sbase), "s"(lds), "v"(voff1k)
      : "memory", "scc");
}
DI u32x4 gld128_sc1(unsigned voff, const void* sbase) {
  u32x4 r;
  asm volatile("global_load_dwordx4 %0, %1, %2 sc1" : "=v"(r) : "v"(voff), "s"(sbase) : "memory");
  return r;
}
DI u32x2 gld64_sc1(unsigned voff, const void* sbase) {
  u32x2 r;
  asm volatile("global_load_dwordx2 %0, %1, %2 sc1" : "=v"(r) : "v"(voff), "s"(sbase) : "memory");
  return r;
}
DI unsigned gld32_sc1(unsigned voff, const void* sbase) {
  unsigned r;
  asm volatile("global_load_dword %0, %1, %2 sc1" : "=v"(r) : "v"(voff), "s"(sbase) : "memory");
  return r;
}
DI unsigned gld32(unsigned voff, const void* sbase) {
  unsigned r;
  asm volatile("global_load_dword %0, %1, %2" : "=v"(r) : "v"(voff), "s"(sbase) : "memory");
  return r;
}
DI void gst128_sc1(unsigned voff, void* sbase, u32x4 v) {
  asm volatile("global_store_dwordx4 %0, %1, %2 sc1\n\ts_nop 1" ::"v"(voff), "v"(v), "s"(sbase) : "memory");
}
DI void gst64_sc1(unsigned voff, void* sbase, u32x2 v) {
  asm volatile("global_store_dwordx2 %0, %1, %2 sc1" ::"v"(voff), "v"(v), "s"(sbase) : "memory");
}
DI void gst32_sc1(unsigned voff, void* sbase, unsigned v) {
  asm volatile("global_store_dword %0, %1, %2 sc1" ::"v"(voff), "v"(v), "s"(sbase) : "memory");
}
DI void gst64(unsigned voff, void* sbase, u32x2 v) {
  asm volatile("global_store_dwordx2 %0, %1, %2" ::"v"(voff), "v"(v), "s"(sbase) : "memory");
}
#define WAITV(n) asm volatile("s_waitcnt vmcnt(" #n ")" ::: "memory")
#define BAR() asm volatile("s_waitcnt lgkmcnt(0)\n\ts_barrier" ::: "memory")
#define PIN(x) asm volatile("" : "+v"(x))
#define SB() __builtin_amdgcn_sched_barrier(0)

DI void poll_words(const char* base, unsigned voff, unsigned tlo, unsigned thi) {
#pragma unroll 1
  for (unsigned spins = 0; spins < SPIN_CAP; ++spins) {
    u32x2 v = gld64_sc1(voff, base);
    asm volatile("s_waitcnt vmcnt(0)" : "+v"(v)::"memory");
    if (__all(v[0] == tlo && v[1] == thi)) break;
    __builtin_amdgcn_s_sleep(POLL_SLEEP);
  }
}

template <bool STORE>
DI void dual_softmax(const f32x4 (&p)[5], unsigned (&o)[20], unsigned dst) {
  float mx = __builtin_fmaxf(p[0][0], p[0][1]), mn = __builtin_fminf(p[0][0], p[0][1]);
  float mx2 = __builtin_fmaxf(p[0][2], p[0][3]), mn2 = __builtin_fminf(p[0][2], p[0][3]);
#pragma unroll
  for (int t = 1; t < 5; ++t) {
    mx = __builtin_fmaxf(__builtin_fmaxf(mx, p[t][0]), p[t][1]); mn = __builtin_fminf(__builtin_fminf(mn, p[t][0]), p[t][1]);
    mx2 = __builtin_fmaxf(__builtin_fmaxf(mx2, p[t][2]), p[t][3]); mn2 = __builtin_fminf(__builtin_fminf(mn2, p[t][2]), p[t][3]);
  }
  const float L = 1.4426950408889634f;
  const float mxl = __builtin_fmaxf(mx, mx2) * -L, mnl = __builtin_fminf(mn, mn2) * L;
  f32x2 ep[10], en[10];
#pragma unroll
  for (int i = 0; i < 10; ++i) {
    const f32x2 v = {p[i >> 1][2 * (i & 1)], p[i >> 1][2 * (i & 1) + 1]};
    ep[i] = __builtin_elementwise_fma(v, (f32x2){L, L}, (f32x2){mxl, mxl});
    en[i] = __builtin_elementwise_fma(v, (f32x2){-L, -L}, (f32x2){mnl, mnl});
  }
  SB();
  f32x2 sp, sn;
#pragma unroll
  for (int i = 0; i < 10; ++i) {
    ep[i][0] = __builtin_amdgcn_exp2f(ep[i][0]); ep[i][1] = __builtin_amdgcn_exp2f(ep[i][1]);
    en[i][0] = __builtin_amdgcn_exp2f(en[i][0]); en[i][1] = __builtin_amdgcn_exp2f(en[i][1]);
    if (i == 1) { sp = ep[0]; sn = en[0]; }
    if (i > 1) { sp += ep[i - 1]; sn += en[i - 1]; }
    SB();
  }
  sp += ep[9]; sn += en[9];
  const float rp = __builtin_amdgcn_rcpf(sp[0] + sp[1]), rn = __builtin_amdgcn_rcpf(sn[0] + sn[1]);
  f32x2 rp2 = {rp, rp}, rn2 = {rn, rn};
  PIN(rp2); PIN(rn2);
#pragma unroll
  for (int c = 0; c < 5; ++c) {
    f32x2 t[4];
#pragma unroll
    for (int i = 0; i < 4; ++i) t[i] = 4 * c + i < 10 ? ep[4 * c + i] * rp2 : en[4 * c + i - 10] * rn2;
    SB();
#pragma unroll
    for (int i = 0; i < 4; ++i) o[4 * c + i] = pk2(t[i][0], t[i][1]);
    if (STORE) sts128(dst + 16 * c, (u32x4){o[4 * c], o[4 * c + 1], o[4 * c + 2], o[4 * c + 3]});
    SB();
  }
}
struct WPhi { u32x4 a0[5], a1[5]; u32x2 a2[5]; };
DI void wphi_read(WPhi& f, unsigned pbase) {
#pragma unroll
  for (int t = 0; t < 5; ++t) f.a0[t] = cat(ldstr(pbase + 8 * t), ldstr(pbase + 8 * t + 160));
#pragma unroll
  for (int t = 0; t < 5; ++t) f.a1[t] = cat(ldstr(pbase + 8 * t + 320), ldstr(pbase + 8 * t + 480));
#pragma unroll
  for (int t = 0; t < 5; ++t) f.a2[t] = ldstr(pbase + 8 * t + 640);
}
DI void phi_proj(const WPhi& f, const unsigned (&kq)[10], f32x4 (&pp)[5]) {
  const u32x4 f0 = {kq[0], kq[1], kq[2], kq[3]}, f1 = {kq[4], kq[5], kq[6], kq[7]};
  const u32x2 f2 = {kq[8], kq[9]};
  asm volatile("s_nop 1\n\tv_mfma_f32_16x16x32_bf16 %0, %5, %10, 0\n\tv_mfma_f32_16x16x32_bf16 %1, %6, %10, 0\n\tv_mfma_f32_16x16x32_bf16 %2, %7, %10, 0\n\t"
               "v_mfma_f32_16x16x32_bf16 %3, %8, %10, 0\n\tv_mfma_f32_16x16x32_bf16 %4, %9, %10, 0"
               : "=&v"(pp[0]), "=&v"(pp[1]), "=&v"(pp[2]), "=&v"(pp[3]), "=&v"(pp[4])
               : "v"(f.a0[0]), "v"(f.a0[1]), "v"(f.a0[2]), "v"(f.a0[3]), "v"(f.a0[4]), "v"(f0), "v"(f1), "v"(f2));
  asm volatile("v_mfma_f32_16x16x32_bf16 %0, %5, %10, %0\n\tv_mfma_f32_16x16x32_bf16 %1, %6, %10, %1\n\tv_mfma_f32_16x16x32_bf16 %2, %7, %10, %2\n\t"
               "v_mfma_f32_16x16x32_bf16 %3, %8, %10, %3\n\tv_mfma_f32_16x16x32_bf16 %4, %9, %10, %4"
               : "+v"(pp[0]), "+v"(pp[1]), "+v"(pp[2]), "+v"(pp[3]), "+v"(pp[4])
               : "v"(f.a1[0]), "v"(f.a1[1]), "v"(f.a1[2]), "v"(f.a1[3]), "v"(f.a1[4]), "v"(f1));
  asm volatile("s_nop 1\n\tv_mfma_f32_16x16x16_bf16 %0, %5, %10, %0\n\tv_mfma_f32_16x16x16_bf16 %1, %6, %10, %1\n\tv_mfma_f32_16x16x16_bf16 %2, %7, %10, %2\n\t"
               "v_mfma_f32_16x16x16_bf16 %3, %8, %10, %3\n\tv_mfma_f32_16x16x16_bf16 %4, %9, %10, %4"
               : "+v"(pp[0]), "+v"(pp[1]), "+v"(pp[2]), "+v"(pp[3]), "+v"(pp[4])
               : "v"(f.a2[0]), "v"(f.a2[1]), "v"(f.a2[2]), "v"(f.a2[3]), "v"(f.a2[4]), "v"(f2));
}
#define PROJ_DONE(pp) asm volatile("s_nop 7" : "+v"(pp[0]), "+v"(pp[1]), "+v"(pp[2]), "+v"(pp[3]), "+v"(pp[4]))
DI u32x4 silu8(f32x4 a, f32x4 c) {
  const float NL = -1.4426950408889634f;
  f32x2 v[4] = {{a[0], a[1]}, {a[2], a[3]}, {c[0], c[1]}, {c[2], c[3]}}, e[4];
#pragma unroll
  for (int i = 0; i < 4; ++i) e[i] = v[i] * (f32x2){NL, NL};
#pragma unroll
  for (int i = 0; i < 4; ++i) { e[i][0] = __builtin_amdgcn_exp2f(e[i][0]); e[i][1] = __builtin_amdgcn_exp2f(e[i][1]); }
#pragma unroll
  for (int i = 0; i < 4; ++i) e[i] += (f32x2){1.f, 1.f};
#pragma unroll
  for (int i = 0; i < 4; ++i) { e[i][0] = __builtin_amdgcn_rcpf(e[i][0]); e[i][1] = __builtin_amdgcn_rcpf(e[i][1]); }
#pragma unroll
  for (int i = 0; i < 4; ++i) v[i] *= e[i];
  return (u32x4){pk2(v[0][0], v[0][1]), pk2(v[1][0], v[1][1]), pk2(v[2][0], v[2][1]), pk2(v[3][0], v[3][1])};
}

DI void amfma(f32x4& c, const u32x4& a, const u32x4& b) { asm volatile("v_mfma_f32_16x16x32_bf16 %0, %1, %2, %0" : "+v"(c) : "v"(a), "v"(b)); }
template <int OFF> DI void trlo(u32x4& f, unsigned a) { u32x2 t; asm volatile("ds_read_b64_tr_b16 %0, %1 offset:%2" : "=v"(t) : "v"(a), "i"(OFF)); f[0] = t[0]; f[1] = t[1]; }
template <int OFF> DI void trhi(u32x4& f, unsigned a) { u32x2 t; asm volatile("ds_read_b64_tr_b16 %0, %1 offset:%2" : "=v"(t) : "v"(a), "i"(OFF)); f[2] = t[0]; f[3] = t[1]; }
template <int OFF> DI void rd128(u32x4& f, unsigned a) { asm volatile("ds_read_b128 %0, %1 offset:%2" : "=v"(f) : "v"(a), "i"(OFF)); }
DI void vadd(unsigned& v, unsigned s) { asm volatile("v_add_u32 %0, %1, %0" : "+v"(v) : "s"(s)); }
#define WL(n) asm volatile("s_waitcnt lgkmcnt(" #n ")")
#define MFMA_DONE() asm volatile("s_waitcnt lgkmcnt(0)\n\ts_nop 7\n\ts_nop 3" ::: "memory")

template <int NT, bool Q, bool GATE>
DI void gemm1_loop(f32x4 (&acc)[8][2], unsigned xa, unsigned wa, unsigned wb, unsigned gk) {
  u32x4 W[8], X0[2], X1[2];
  unsigned xb = xa ^ 64;
#pragma unroll
  for (int t = 0; t < NT; ++t) { acc[t][0] = (f32x4){0.f, 0.f, 0.f, 0.f}; acc[t][1] = (f32x4){0.f, 0.f, 0.f, 0.f}; PIN(acc[t][0]); PIN(acc[t][1]); }
  BAR();
#define TO(i) (Q ? ((i) < 5 ? (NT == 8 ? 0 : 80) + 8 * (i) : 160 + 8 * ((i) - (NT == 8 ? 5 : 2))) : 32 * (i))
#define AD(i) wa
#define RL(i, off) if constexpr (GATE && (i) == 6) { rd128<0>(W[i], wb); } else { trlo<TO(i) + (off)>(W[i], AD(i)); }
#define RH(i, off) if constexpr (GATE && (i) == 6) { gk = gk == 19 ? 0 : gk + 1; vadd(wb, gk == 0 ? -1216 : 64); } else { trhi<TO(i) + (off) + 7680>(W[i], AD(i)); }
#define M0(i, X) amfma(acc[i][0], W[i], X[0]);
#define M1(i, X) amfma(acc[i][1], W[i], X[1]);
#define G1(i, j) M0(i, X0) RL(j, 15360) M1(i, X0) RH(j, 15360)
#define G1X(i, j, h) M0(i, X0) RL(j, 15360) rd128<2048 * (h)>(X1[h], xb); M1(i, X0) RH(j, 15360)
  rd128<0>(X0[0], xa); rd128<2048>(X0[1], xa);
  RL(0, 0) RH(0, 0) RL(1, 0) RH(1, 0) RL(2, 0) RH(2, 0) RL(3, 0) RH(3, 0) RL(4, 0) RH(4, 0)
  if constexpr (NT > 6) { RL(5, 0) RH(5, 0) }
  if constexpr (NT > 7) { RL(6, 0) RH(6, 0) }
  unsigned sb = L_S0;
#pragma unroll 1
  for (int t = 0; t < 10; ++t) {
    if constexpr (NT == 8) {
      WL(10); M0(0, X0) RL(7, 0) M1(0, X0) RH(7, 0) G1(1, 0)
      WL(10); G1(2, 1) G1(3, 2)
      WL(10); G1X(4, 3, 0) G1X(5, 4, 1)
      WL(12); G1(6, 5) G1(7, 6)
      WL(5);
    } else if constexpr (NT == 7) {
      WL(8); M0(0, X0) RL(6, 0) M1(0, X0) RH(6, 0) G1(1, 0)
      WL(7); G1(2, 1) G1X(3, 2, 0)
      WL(8); G1X(4, 3, 1) G1(5, 4)
      WL(12); G1(6, 5)
      WL(5);
    } else {
      WL(6); M0(0, X0) RL(5, 0) M1(0, X0) RH(5, 0) G1(1, 0)
      WL(6); G1X(2, 1, 0) G1X(3, 2, 1)
      WL(8); G1(4, 3) G1(5, 4)
      WL(4);
    }
    M0(0, X1) RL(NT - 1, 15360) M1(0, X1) RH(NT - 1, 15360)
    const unsigned sn = sb == L_S0 ? L_S1 : sb + SLOT, dl = sn - sb;
    sb = sn;
    M0(1, X1) vadd(xa, dl); M1(1, X1) vadd(wa, dl);
    M0(2, X1) vadd(xb, dl); M1(2, X1)
    BAR();
    M0(3, X1) rd128<0>(X0[0], xa); rd128<2048>(X0[1], xa); M1(3, X1) RL(0, 0) RH(0, 0)
    M0(4, X1) RL(1, 0) RH(1, 0) M1(4, X1) RL(2, 0) RH(2, 0)
    if constexpr (NT == 8) {
      M0(5, X1) RL(3, 0) RH(3, 0) M1(5, X1) RL(4, 0)
      M0(6, X1) RH(4, 0) RL(5, 0) M1(6, X1) RH(5, 0)
      M0(7, X1) RL(6, 0) M1(7, X1) RH(6, 0)
    } else if constexpr (NT == 7) {
      M0(5, X1) RL(3, 0) RH(3, 0) M1(5, X1) RL(4, 0) RH(4, 0)
      M0(6, X1) RL(5, 0) M1(6, X1) RH(5, 0)
    } else {
      M0(5, X1) RL(3, 0) RH(3, 0) M1(5, X1) RL(4, 0) RH(4, 0)
    }
  }
  MFMA_DONE();
#pragma unroll
  for (int t = 0; t < NT; ++t) { PIN(acc[t][0]); PIN(acc[t][1]); PIN(W[t]); }
  PIN(X0[0]); PIN(X0[1]);
#undef TO
#undef AD
#undef RL
#undef RH
#undef M0
#undef M1
#undef G1
#undef G1X
}

struct P2 {
  u32x4 A[2], B[3];
  unsigned aa, ba;
  unsigned st, sa;
};
DI void p2_first(P2& p) {
  asm volatile("ds_read_b128 %0, %5\n\tds_read_b128 %2, %6\n\tds_read_b128 %3, %6 offset:1024\n\tds_read_b128 %4, %6 offset:2048\n\t"
               "ds_read_b128 %1, %5 offset:2048\n\ts_waitcnt lgkmcnt(0)"
               : "=&v"(p.A[0]), "=&v"(p.A[1]), "=&v"(p.B[0]), "=&v"(p.B[1]), "=&v"(p.B[2]) : "v"(p.aa), "v"(p.ba) : "memory");
}
#define MF(d, a, b) "v_mfma_f32_16x16x32_bf16 %" #d ", %" #a ", %" #b ", %" #d "\n\t"
#define RD(d, a, o) "ds_read_b128 %" #d ", %" #a " offset:" #o "\n\t"
DI void p2_run(P2& p, f32x4 (&o)[2][3], int n, int nw) {
  u32x4 A0, A1, B0, B1, B2;
  unsigned da, db;
  asm volatile(
      "s_cmp_lg_u32 %22, 0\n\ts_cbranch_scc1 2f\n"
      "1:\n\t"
      "s_add_u32 %18, %18, 1\n\ts_and_b32 %20, %18, 3\n\ts_cselect_b32 %20, 16384, %23\n\t"
      "s_waitcnt lgkmcnt(2)\n\t" MF(0, 8, 6) RD(11, 16, 1024)
      "s_and_b32 %21, %18, 7\n\t" MF(1, 9, 6) RD(13, 17, 5120)
      "s_waitcnt lgkmcnt(2)\n\t" MF(2, 10, 6) RD(14, 17, 6144)
      "s_cselect_b32 %21, 10240, %24\n\t" MF(3, 8, 7) RD(15, 17, 7168)
      MF(4, 9, 7) RD(12, 16, 3072)
      MF(5, 10, 7) "v_add_u32 %16, %20, %16\n\t"
      "s_waitcnt lgkmcnt(2)\n\t" MF(0, 13, 11) "v_add_u32 %17, %21, %17\n\t"
      MF(1, 14, 11)
      "s_waitcnt lgkmcnt(0)\n\ts_barrier\n\t"
      MF(2, 15, 11) RD(6, 16, 0) RD(8, 17, 0)
      MF(3, 13, 12) RD(9, 17, 1024) RD(10, 17, 2048)
      MF(4, 14, 12) RD(7, 16, 2048) "s_sub_u32 %19, %19, 1\n\t"
      MF(5, 15, 12)
      "s_cmp_lg_u32 %19, 0\n\ts_cbranch_scc1 1b\n\t"
      "s_branch 3f\n"
      "2:\n\t"
      "s_add_u32 %18, %18, 1\n\ts_and_b32 %20, %18, 3\n\ts_cselect_b32 %20, 16384, %23\n\t"
      "s_waitcnt lgkmcnt(1)\n\t" MF(0, 8, 6) RD(11, 16, 1024) RD(13, 17, 5120)
      "s_and_b32 %21, %18, 7\n\t" MF(1, 9, 6) RD(14, 17, 6144)
      "s_cselect_b32 %21, 10240, %24\n\ts_waitcnt lgkmcnt(3)\n\t" MF(3, 8, 7) RD(12, 16, 3072)
      MF(4, 9, 7) "v_add_u32 %16, %20, %16\n\tv_add_u32 %17, %21, %17\n\t"
      "s_waitcnt lgkmcnt(1)\n\t" MF(0, 13, 11)
      MF(1, 14, 11)
      "s_waitcnt lgkmcnt(0)\n\ts_barrier\n\t"
      RD(6, 16, 0) RD(8, 17, 0)
      MF(3, 13, 12) RD(9, 17, 1024) RD(7, 16, 2048) "s_sub_u32 %19, %19, 1\n\t"
      MF(4, 14, 12)
      "s_cmp_lg_u32 %19, 0\n\ts_cbranch_scc1 2b\n"
      "3:\n\t"
      "s_waitcnt lgkmcnt(0)\n\ts_nop 7"
      : "+v"(o[0][0]), "+v"(o[0][1]), "+v"(o[0][2]), "+v"(o[1][0]), "+v"(o[1][1]), "+v"(o[1][2]),
        "+v"(p.A[0]), "+v"(p.A[1]), "+v"(p.B[0]), "+v"(p.B[1]), "+v"(p.B[2]),
        "=&v"(A0), "=&v"(A1), "=&v"(B0), "=&v"(B1), "=&v"(B2),
        "+v"(p.aa), "+v"(p.ba), "+s"(p.st), "+s"(n), "=&s"(da), "=&s"(db)
      : "s"(nw), "s"(-49152), "s"(-71680)
      : "memory", "scc");
}
DI void p2a_run(P2& p, f32x4 (&o)[3], unsigned& aa, int nw, u32x4 (&pt)[3], unsigned po, unsigned po2, const char* prd) {
  int n = 5;
  u32x4 A0, A1, A2, A3, B0, B1, B2;
  unsigned da, db;
#define ADVB "s_add_u32 %15, %15, 1\n\ts_and_b32 %18, %15, 7\n\ts_cselect_b32 %18, 10240, %25\n\tv_add_u32 %14, %18, %14\n\t"
#define ADVA "s_add_u32 %19, %19, 1\n\ts_and_b32 %17, %19, 3\n\ts_cselect_b32 %17, 16384, %24\n\tv_add_u32 %13, %17, %13\n\t"
#define PLD(l) "s_cmp_lg_u32 %16, 2\n\ts_cbranch_scc1 " #l "f\n\tglobal_load_dwordx4 %20, %26, %28 sc1\n\tglobal_load_dwordx4 %21, %26, %28 offset:1024 sc1\n\t" \
               "global_load_dwordx4 %22, %27, %28 sc1\n" #l ":\n\t"
  asm volatile(
      "s_cmp_lg_u32 %23, 0\n\ts_cbranch_scc1 2f\n"
      "1:\n\t"
      "s_barrier\n\t" RD(6, 13, 0) RD(10, 14, 5120) RD(11, 14, 6144) RD(12, 14, 7168) RD(7, 13, 1024) RD(8, 13, 2048) RD(9, 13, 3072)
      PLD(4) ADVB
      "s_waitcnt lgkmcnt(6)\n\t" MF(0, 3, 6) MF(1, 4, 6) MF(2, 5, 6)
      RD(3, 14, 0) RD(4, 14, 1024) RD(5, 14, 2048) ADVA
      "s_waitcnt lgkmcnt(5)\n\t" MF(0, 10, 7) MF(1, 11, 7) MF(2, 12, 7)
      RD(10, 14, 5120) RD(11, 14, 6144) RD(12, 14, 7168) ADVB
      "s_waitcnt lgkmcnt(3)\n\t" MF(0, 3, 8) MF(1, 4, 8) MF(2, 5, 8)
      RD(3, 14, 0) RD(4, 14, 1024) RD(5, 14, 2048) "s_sub_u32 %16, %16, 1\n\t"
      "s_waitcnt lgkmcnt(3)\n\t" MF(0, 10, 9) MF(1, 11, 9) MF(2, 12, 9)
      "s_cmp_lg_u32 %16, 0\n\ts_cbranch_scc1 1b\n\t"
      "s_branch 3f\n"
      "2:\n\t"
      "s_barrier\n\t" RD(6, 13, 0) RD(10, 14, 5120) RD(11, 14, 6144) RD(7, 13, 1024) RD(8, 13, 2048) RD(9, 13, 3072)
      PLD(5) ADVB
      "s_waitcnt lgkmcnt(5)\n\t" MF(0, 3, 6) MF(1, 4, 6)
      RD(3, 14, 0) RD(4, 14, 1024) ADVA
      "s_waitcnt lgkmcnt(4)\n\t" MF(0, 10, 7) MF(1, 11, 7)
      RD(10, 14, 5120) RD(11, 14, 6144) ADVB
      "s_waitcnt lgkmcnt(2)\n\t" MF(0, 3, 8) MF(1, 4, 8)
      RD(3, 14, 0) RD(4, 14, 1024) "s_sub_u32 %16, %16, 1\n\t"
      "s_waitcnt lgkmcnt(2)\n\t" MF(0, 10, 9) MF(1, 11, 9)
      "s_cmp_lg_u32 %16, 0\n\ts_cbranch_scc1 2b\n"
      "3:\n\t"
      "s_waitcnt vmcnt(0) lgkmcnt(0)\n\ts_nop 7"
      : "+v"(o[0]), "+v"(o[1]), "+v"(o[2]), "+v"(p.B[0]), "+v"(p.B[1]), "+v"(p.B[2]),
        "=&v"(A0), "=&v"(A1), "=&v"(A2), "=&v"(A3), "=&v"(B0), "=&v"(B1), "=&v"(B2),
        "+v"(aa), "+v"(p.ba), "+s"(p.st), "+s"(n), "=&s"(da), "=&s"(db), "+s"(p.sa),
        "=&v"(pt[0]), "=&v"(pt[1]), "=&v"(pt[2])
      : "s"(nw), "s"(-49152), "s"(-71680), "v"(po), "v"(po2), "s"(prd)
      : "memory", "scc");
#undef ADVA
#undef PLD
#undef ADVB
}
#undef MF
#undef RD

#define TRW 0
#define TRL 0
#define STAMP(i)

#define SL2(r, o) "s_load_dword s95, %" #r ", " #o "\n\ts_load_dword s95, %" #r ", " #o "+64\n\t"
DI void spf(int kind, int st, const char* x, const char* wqkv, const char* w1, int xcd, int idx) {
  const int xr = xcd & 1, cq = xcd >> 1, krow = st * 64 + xr * 32 + idx;
  const char* px = x + (xr * 1024 + cq * 256 + idx * 8) * 1280 + st * 128;
  const char* pq = wqkv + krow * 3840 + ((cq * 320) & ~127);
  const char* ph = w1 + krow * 3456 + cq * 896 - (cq == 3 ? 128 : 0);
  asm volatile("s_cmp_lt_u32 %3, 2\n\ts_cbranch_scc0 2f\n\ts_cmp_eq_u32 %3, 0\n\ts_cbranch_scc0 1f\n\t"
               SL2(0, 0) SL2(0, 1280) SL2(0, 2560) SL2(0, 3840) SL2(0, 5120) SL2(0, 6400) "s_branch 4f\n"
               "1:\n\t" SL2(0, 7680) SL2(0, 8960) SL2(2, 0) SL2(2, 128) SL2(2, 256) SL2(2, 384) "s_branch 4f\n"
               "2:\n\ts_cmp_eq_u32 %3, 2\n\ts_cbranch_scc0 3f\n\t"
               SL2(1, 0) SL2(1, 128) SL2(1, 256) SL2(1, 1280) SL2(1, 1408) SL2(1, 1536) "s_branch 4f\n"
               "3:\n\t" SL2(1, 2560) SL2(1, 2688) SL2(1, 2816) SL2(2, 512) SL2(2, 640) SL2(2, 768)
               "\n4:" ::"s"(px), "s"(pq), "s"(ph), "s"(kind) : "s95", "scc");
}
typedef __attribute__((ext_vector_type(16))) unsigned u32x16;
DI const char* mkptr(unsigned lo, unsigned hi) { return (const char*)(((u64)hi << 32) | lo); }

__global__ __launch_bounds__(768, 3) void fused_kernel(const char* a_x, const char* a_wqkv, const char* a_wphi, const char* a_wo,
                                                       const char* a_w1, const char* a_w2, const char* a_gate, char* a_out, char* a_ws) {
  extern __shared__ __attribute__((aligned(16))) char smem[];
  u32x16 ka;
  u64 kws, pc;
  asm volatile("s_load_dwordx16 %0, %3, 0x0\n\ts_load_dwordx2 %1, %3, 0x40\n\ts_getpc_b64 %2"
               : "=&s"(ka), "=&s"(kws), "=&s"(pc) : "s"(__builtin_amdgcn_kernarg_segment_ptr()));
  __builtin_amdgcn_sched_barrier(0);
#define ARGS()                                                                                                   \
  asm volatile("s_waitcnt lgkmcnt(0)" : "+s"(ka), "+s"(kws));                                                    \
  const char *x = mkptr(ka[0], ka[1]), *wqkv = mkptr(ka[2], ka[3]), *wphi = mkptr(ka[4], ka[5]), *wo = mkptr(ka[6], ka[7]), \
             *w1 = mkptr(ka[8], ka[9]), *w2 = mkptr(ka[10], ka[11]), *gate = mkptr(ka[12], ka[13]);              \
  char *out = (char*)mkptr(ka[14], ka[15]), *ws = (char*)kws;                                                    \
  (void)x; (void)wqkv; (void)wphi; (void)wo; (void)w1; (void)w2; (void)gate; (void)out;                          \
  u64 epoch;                                                                                                     \
  asm volatile("s_load_dwordx2 %0, %1, 0x0" : "=s"(epoch) : "s"(ws) : "memory")
#define TAG()                                                                                                    \
  asm volatile("s_waitcnt lgkmcnt(0)" : "+s"(epoch));                                                            \
  const u64 tag = epoch * 6364136223846793005ull + 1442695040888963407ull;                                       \
  const unsigned tlo = (unsigned)tag, thi = (unsigned)(tag >> 32);                                               \
  const u32x2 tagv = {tlo, thi}
  const int tid = threadIdx.x, lane = tid & 63;
  const int w = __builtin_amdgcn_readfirstlane(tid >> 6);
  const int b = blockIdx.x, xcd = b & 7, idx = b >> 3;
  const int rt = (xcd & 1) * 8 + (idx & 7);
  const int cb = idx >> 3, cq = xcd >> 1;
  const bool isq = cb < 2;
  const int jg = 2 * cq + (cb & 1);
  const int rt2 = ((xcd & 1) ^ 1) * 8 + ((cq & 1) ^ (isq ? 0 : 1)) * 4 + ((idx >> 2) & 3), ct = (cq >> 1) * 4 + (idx & 3);
  const int g = lane >> 4, c16 = lane & 15, q4 = c16 >> 2, p4 = lane & 3;
  const int hstart = jg * 448 - (jg == 7 ? 64 : 0);
  const unsigned boff = lane * 16;
#define ROTM 7
  const int rl = idx & 7, xr = xcd & 1;
  const int rot = ROTM == 0 ? 0
                : ROTM == 1 ? ((rl ^ cb) & 1) * 5
                : ROTM == 3 ? ((rl ^ cb) & 1) * 5 + xr * 2 + (cq & 1)
                : ROTM == 4 ? ((((rl + cb) % 3) * 10) / 3 + xr * 2) % 10
                : ROTM == 5 ? ((((rl + cb) & 3) * 5) >> 1) + xr
                : ROTM == 6 ? ((rl ^ cb) & 1) * 5 + xr * 3
                : ROTM == 8 ? (xr * 5 + cq) % 10
                : ((rl + cb) % 5) * 2 + xr;

  if (__builtin_expect(w >= 8, 1)) {
    const int L = w - 8;
    const int ldw = isq ? 3840 : 3456;
    unsigned xo[4], wv[8];
    {
      const unsigned x0 = (rt * 128 + 8 * L + (lane >> 3)) * 1280 + (((lane & 7) ^ ((4 * L + (lane >> 4)) & 7)) << 4);
#pragma unroll
      for (int i = 0; i < 4; ++i) xo[i] = x0 + i * 32 * 1280;
      const int rs = lane >= 30 ? 1 : 0, pos = lane - 30 * rs;
      const int sec = pos < 10 ? 0 : (pos < 15 ? 2560 - 160 : (pos < 25 ? 1280 - 240 : 2560 - 320));
      const int col = isq ? sec + jg * 160 + pos * 16 : hstart + pos * 16;
      const unsigned w0 = (8 * (L >> 1) + 2 * (L & 1) + rs) * ldw + col;
#pragma unroll
      for (int i = 0; i < 8; ++i) wv[i] = w0 + (32 * (i >> 2) + 16 * (i & 1) + 4 * ((i >> 1) & 1)) * ldw;
    }
    const u64 half = isq ? 0x3fffffffull : (jg < 6 ? 0x0fffffffull : 0x00ffffffull);
    const u64 wmask = half | (half << 30);
#pragma unroll
    for (int i = 0; i < 4; ++i) PIN(xo[i]);
#pragma unroll
    for (int i = 0; i < 8; ++i) PIN(wv[i]);
    ARGS();
    const char* wbase = isq ? wqkv : w1;
    const char* xs = x + rot * 128;
    const char* wsrc = wbase + rot * 64 * ldw;
    int kst = rot;
    const unsigned mx0 = L * 1024, mw0 = 16384 + L * 960;
    auto issue_lo = [&](unsigned sb) {
      asm volatile(
          "s_mov_b32 m0, %10\n\ts_nop 0\n\tglobal_load_lds_dwordx4 %0, %8\n\t"
          "s_add_u32 m0, m0, 4096\n\ts_nop 0\n\tglobal_load_lds_dwordx4 %1, %8\n\t"
          "s_add_u32 m0, m0, 4096\n\ts_nop 0\n\tglobal_load_lds_dwordx4 %2, %8\n\t"
          "s_add_u32 m0, m0, 4096\n\ts_nop 0\n\tglobal_load_lds_dwordx4 %3, %8\n\t"
          "s_mov_b32 m0, %11\n\ts_mov_b64 exec, %12\n\tglobal_load_lds_dwordx4 %4, %9 " WPOL "\n\t"
          "s_add_u32 m0, m0, 3840\n\ts_nop 0\n\tglobal_load_lds_dwordx4 %5, %9 " WPOL "\n\t"
          "s_add_u32 m0, m0, 3840\n\ts_nop 0\n\tglobal_load_lds_dwordx4 %6, %9 " WPOL "\n\t"
          "s_add_u32 m0, m0, 3840\n\ts_nop 0\n\tglobal_load_lds_dwordx4 %7, %9 " WPOL "\n\t"
          "s_mov_b64 exec, -1"
          ::"v"(xo[0]), "v"(xo[1]), "v"(xo[2]), "v"(xo[3]), "v"(wv[0]), "v"(wv[1]), "v"(wv[2]), "v"(wv[3]),
            "s"(xs), "s"(wsrc), "s"(sb + mx0), "s"(sb + mw0), "s"(wmask)
          : "memory", "scc");
    };
    auto issue_hi = [&](unsigned sb) {
      asm volatile(
          "s_mov_b32 m0, %5\n\ts_mov_b64 exec, %6\n\tglobal_load_lds_dwordx4 %0, %4 " WPOL "\n\t"
          "s_add_u32 m0, m0, 3840\n\ts_nop 0\n\tglobal_load_lds_dwordx4 %1, %4 " WPOL "\n\t"
          "s_add_u32 m0, m0, 3840\n\ts_nop 0\n\tglobal_load_lds_dwordx4 %2, %4 " WPOL "\n\t"
          "s_add_u32 m0, m0, 3840\n\ts_nop 0\n\tglobal_load_lds_dwordx4 %3, %4 " WPOL "\n\t"
          "s_mov_b64 exec, -1"
          ::"v"(wv[4]), "v"(wv[5]), "v"(wv[6]), "v"(wv[7]), "s"(wsrc), "s"(sb + mw0 + 15360), "s"(wmask)
          : "memory", "scc");
      const bool wrap = ++kst == 10;
      xs = wrap ? x : xs + 128;
      wsrc = wrap ? wbase : wsrc + 64 * ldw;
    };
    auto issue_stage = [&](unsigned sb) { issue_lo(sb); issue_hi(sb); };
#define LSTAMP()
#define LBAR() asm volatile("s_barrier" ::: "memory")
#define STEP(N, SL) WAITV(N); LSTAMP(); LBAR(); LSTAMP(); issue_stage(SL); LSTAMP();
    LSTAMP();
    __builtin_amdgcn_s_setprio(3);
    issue_stage(L_S0);
    LSTAMP();
    __builtin_amdgcn_s_setprio(1);
    int lane2 = lane, unit = b * 3 + L;
    asm volatile("" : "+v"(lane2), "+s"(unit));
    const bool retile = (L < 3) && (unit < 740);
    const int rk = retile ? unit / 10 : 0, rcu = retile ? unit - rk * 10 : 0;
    const char* rsrc = rk < 54 ? w2 : wo;
    unsigned rv[4], ra[4], rd[4];
    {
      const int kp = rk - 54, P = kp / 5, s5 = kp - P * 5;
      const bool lft = rk >= 54 && s5 == 4;
      const int gs = lft ? 40 : 4;
      const int r0 = rk < 54 ? 32 * rk : (lft ? 160 * P + 16 : 160 * P + 40 * s5);
      const int rh = rk < 54 ? 16 : (lft ? 2 : 20);
      const int c = lane2 & 7, e = (lane2 >> 3) & 3, g2 = lane2 >> 4, q2 = (lane2 >> 2) & 3;
      const unsigned v0 = (r0 + (lane2 >> 5) * gs + (lft ? 20 * (e & 1) + (e >> 1) : e)) * 1280 + rcu * 128 + ((((c >> 1) ^ g2) << 5) | ((c & 1) << 4));
      const unsigned base = L_MISC + L * 4096 + (4 * g2 + q2) * 128 + 8 * (lane2 & 3);
      const int sw = (2 * g2 + (q2 >> 1)) & 3;
#pragma unroll
      for (int n = 0; n < 4; ++n) {
        const int N = 4 * rcu + n, c5 = N / 5;
        rv[n] = v0 + ((n & 1) * 2 * gs + (n >> 1) * rh) * 1280;
        ra[n] = base + ((n ^ sw) << 5);
        rd[n] = WS_B + ((c5 * 74 + rk) * 5 + (N - c5 * 5)) * 1024 + lane2 * 16;
        PIN(rv[n]); PIN(ra[n]); PIN(rd[n]);
      }
    }
    TAG();
    int pst = SPF > 1 ? 5 : 3;
#define PF()
    PF();
    LSTAMP();
    issue_lo(L_S1);
    LSTAMP();
    WAITV(8); LSTAMP(); LBAR(); LSTAMP();
    __builtin_amdgcn_s_setprio(3);
    issue_hi(L_S1);
    PF();
    __builtin_amdgcn_s_setprio(1);
    asm volatile("s_mov_b32 m0, %5\n\ts_mov_b64 exec, %6\n\tglobal_load_lds_dwordx4 %0, %4 nt\n\t"
                 "s_add_u32 m0, m0, %7\n\ts_nop 0\n\tglobal_load_lds_dwordx4 %1, %4 nt\n\t"
                 "s_add_u32 m0, m0, %7\n\ts_nop 0\n\tglobal_load_lds_dwordx4 %2, %4 nt\n\t"
                 "s_add_u32 m0, m0, %7\n\ts_nop 0\n\tglobal_load_lds_dwordx4 %3, %4 nt\n\ts_mov_b64 exec, -1"
                 ::"v"(rv[0]), "v"(rv[1]), "v"(rv[2]), "v"(rv[3]), "s"(rsrc), "s"(retile ? L_MISC + L * 4096 : L_ONE + 16),
                   "s"(retile ? ~0ull : 1ull), "s"(retile ? 1024 : 0) : "memory", "scc");
    issue_lo(L_S2);
#define HALF(PREV, NEXT) LSTAMP(); LBAR(); LSTAMP(); __builtin_amdgcn_s_setprio(3); issue_hi(PREV); PF(); __builtin_amdgcn_s_setprio(1); issue_lo(NEXT); LSTAMP();
    WAITV(12);
    HALF(L_S2, L_S0)
    WAITV(8);
    {
      u32x4 f[4];
#pragma unroll
      for (int n = 0; n < 4; ++n) f[n] = cat(ldstr(ra[n]), ldstr(ra[n] + 2048));
      LSTAMP(); LBAR(); LSTAMP();
      __builtin_amdgcn_s_setprio(3);
      issue_hi(L_S0);
      if (retile) {
#pragma unroll
        for (int n = 0; n < 4; ++n) gst128_sc1(rd[n], ws, f[n]);
      }
      PF();
      __builtin_amdgcn_s_setprio(1);
      issue_lo(L_S1);
      LSTAMP();
      if (retile) { WAITV(12); } else { WAITV(8); }
      unsigned sp = L_S1, sb = L_S2;
#pragma unroll 1
      for (int t = 2; t < 7; ++t) {
        HALF(sp, sb)
        sp = sb;
        sb = sb == L_S0 ? L_S1 : sb + SLOT;
        if (t == 3 && L == 3 && lane == 0) gst64_sc1(b * 8, ws + WS_RT, tagv);
        WAITV(8);
      }
      LSTAMP(); LBAR(); LSTAMP();
      issue_hi(sp);
    }
    const int k0 = isq ? 54 - 2 * NQ2 : 0, nb = isq ? NQ2 : NH2;
    const int rot2 = P2ROT == 1 ? (((idx + (idx >> 2)) & 3) * nb) >> 2 : P2ROT == 2 ? (((idx + (idx >> 2)) & 1) * nb) >> 1 : 0;
    const char* bbase = ws + WS_B + ct * 74 * 5120;
    const unsigned boff1k = boff + 1024;
    unsigned bl = L_BR;
    int bs = 0, bk = k0 + 2 * rot2;
    auto issue_b = [&](int n) {
#pragma unroll 1
      for (int j = 0; j < n; ++j) {
        glds5_sc1(boff, boff1k, bbase + bk * 5120, bl);
        glds5_sc1(boff, boff1k, bbase + bk * 5120 + 5120, bl + 5120);
        bl = bl == L_BR + 71680 ? L_BR : bl + 10240;
        ++bs;
        bk += 2;
        if (bs < nb && bk == k0 + 2 * nb) bk = k0;
        if (bs == nb) bk = 54;
      }
    };
    __builtin_amdgcn_s_setprio(3);
    if (L == 1) {
      u32x4 r0, r1;
      asm volatile("global_load_dwordx4 %0, %2, %3 sc1\n\tglobal_load_dwordx4 %1, %2, %3 offset:16 sc1\n\ts_waitcnt vmcnt(2)\n\ts_barrier\n\ts_waitcnt vmcnt(0)"
                   : "=&v"(r0), "=&v"(r1) : "v"(lane * 32), "s"(ws + WS_RT) : "memory");
      const unsigned bad = (r0[0] ^ tlo) | (r0[1] ^ thi) | (r0[2] ^ tlo) | (r0[3] ^ thi) | (r1[0] ^ tlo) | (r1[1] ^ thi) | (r1[2] ^ tlo) | (r1[3] ^ thi);
      if (__all(bad == 0)) {
        issue_b(3);
        LBAR();
        issue_b(5);
      } else {
        LBAR();
#pragma unroll 1
        for (int i = 0; i < 4; ++i) poll_words(ws + WS_RT + i * 512, lane * 8, tlo, thi);
        issue_b(8);
      }
      if (b == 0 && lane == 0) gst64_sc1(0, ws + WS_EPOCH, tagv);
    } else {
      WAITV(0);
      LBAR();
      if (!isq && L >= 2) return;
      LBAR();
    }
    LSTAMP();
    const char* abase = ws + WS_A + rt2 * 8 * 74 * 1024;
    unsigned al = L_AR;
    unsigned ao[8];
#pragma unroll
    for (int i = 0; i < 8; ++i) ao[i] = boff + (i ^ (isq ? 0 : 1)) * 74 * 1024;
    int ak = k0 + 2 * rot2;
    auto issue_a = [&]() {
        asm volatile(
            "s_mov_b32 m0, %9\n\ts_nop 0\n\tglobal_load_lds_dwordx4 %0, %8 sc1\n\tglobal_load_lds_dwordx4 %0, %8 offset:1024 sc1\n\t"
            "s_add_u32 m0, m0, 2048\n\ts_nop 0\n\tglobal_load_lds_dwordx4 %1, %8 sc1\n\tglobal_load_lds_dwordx4 %1, %8 offset:1024 sc1\n\t"
            "s_add_u32 m0, m0, 2048\n\ts_nop 0\n\tglobal_load_lds_dwordx4 %2, %8 sc1\n\tglobal_load_lds_dwordx4 %2, %8 offset:1024 sc1\n\t"
            "s_add_u32 m0, m0, 2048\n\ts_nop 0\n\tglobal_load_lds_dwordx4 %3, %8 sc1\n\tglobal_load_lds_dwordx4 %3, %8 offset:1024 sc1\n\t"
            "s_add_u32 m0, m0, 2048\n\ts_nop 0\n\tglobal_load_lds_dwordx4 %4, %8 sc1\n\tglobal_load_lds_dwordx4 %4, %8 offset:1024 sc1\n\t"
            "s_add_u32 m0, m0, 2048\n\ts_nop 0\n\tglobal_load_lds_dwordx4 %5, %8 sc1\n\tglobal_load_lds_dwordx4 %5, %8 offset:1024 sc1\n\t"
            "s_add_u32 m0, m0, 2048\n\ts_nop 0\n\tglobal_load_lds_dwordx4 %6, %8 sc1\n\tglobal_load_lds_dwordx4 %6, %8 offset:1024 sc1\n\t"
            "s_add_u32 m0, m0, 2048\n\ts_nop 0\n\tglobal_load_lds_dwordx4 %7, %8 sc1\n\tglobal_load_lds_dwordx4 %7, %8 offset:1024 sc1"
            ::"v"(ao[0]), "v"(ao[1]), "v"(ao[2]), "v"(ao[3]), "v"(ao[4]), "v"(ao[5]), "v"(ao[6]), "v"(ao[7]), "s"(abase + ak * 1024), "s"(al)
            : "memory", "scc");
        ak += 2;
        if (ak == k0 + 2 * nb) ak = k0;
        al = al == L_AR + 49152 ? L_AR : al + 16384;
    };
    const char* fl = ws + WS_HF + rt2 * 128;
    unsigned fo = (lane & 7) * 8;
    bool ok = false;
    if (isq) {
      unsigned ia[4];
#pragma unroll
      for (int i = 0; i < 4; ++i) {
        const int sl = L + 4 * i < 14 ? L + 4 * i : L;
        ia[i] = *(LAS unsigned*)(size_t)(L_TAB + (sl < 7 ? sl : sl - 7) * 256 + lane * 4) + (sl < 7 ? 0 : 3360);
        PIN(ia[i]);
      }
      unsigned my1 = *(LAS unsigned*)(size_t)(L_TAB + (rt < 7 ? rt : rt - 7) * 256 + (16 * L + c16) * 4) == L_ONE + 16 ? 0u : ~0u;
      unsigned h1o = g * 4 * GR_BYTES + c16 * 16;
      unsigned h2o = c16 * 16 + (g & 1) * 8, htag = (g & 1) ? thi : tlo;
      PIN(my1); PIN(h1o); PIN(h2o); PIN(htag);
      LBAR();
      WAITV(0);
      LBAR();
      LSTAMP();
      u32x2 f = gld64_sc1(fo, fl);
      u32x4 pv[4];
      if (rt < 14) {
        const char* pb = ws + WS_PART + (jg ^ 2) * 16 * GR_BYTES + rt * 1024 + L * 256;
        __builtin_amdgcn_s_sleep(HOP1_SLEEP);
#pragma unroll 1
        for (unsigned spins = 0; spins < SPIN_CAP; ++spins) {
#pragma unroll
          for (int i = 0; i < 4; ++i) pv[i] = gld128_sc1(h1o, pb + i * GR_BYTES);
          asm volatile("s_waitcnt vmcnt(0)" : "+v"(pv[0]), "+v"(pv[1]), "+v"(pv[2]), "+v"(pv[3])::"memory");
          LSTAMP();
          unsigned bad = 0;
#pragma unroll
          for (int i = 0; i < 4; ++i) bad |= (pv[i][1] ^ tlo) | (pv[i][3] ^ thi);
          if (__all((bad & my1) == 0)) break;
          __builtin_amdgcn_s_sleep(RETRY_SLEEP);
        }
        float s[4];
#pragma unroll
        for (int e = 0; e < 4; ++e) {
          float t[4];
#pragma unroll
          for (int i = 0; i < 4; ++i) t[i] = __builtin_bit_cast(float, (e & 1) ? pv[i][e & 2] & 0xffff0000u : pv[i][e & 2] << 16);
          s[e] = (t[0] + t[1]) + (t[2] + t[3]);
        }
        const auto ra = __builtin_amdgcn_permlane32_swap(fbits(s[0]), fbits(s[1]), false, false);
        const auto rc = __builtin_amdgcn_permlane32_swap(fbits(s[2]), fbits(s[3]), false, false);
        const auto rd = __builtin_amdgcn_permlane16_swap(fbits(bitsf(ra[0]) + bitsf(ra[1])), fbits(bitsf(rc[0]) + bitsf(rc[1])), false, false);
        unsigned u0 = fbits(bitsf(rd[0]) + bitsf(rd[1])), u1 = u0;
        PIN(u1);
        const auto re = __builtin_amdgcn_permlane32_swap(u0, u1, false, false);
        if (lane < 32) gst64_sc1(h2o, ws + WS_RED + (jg ^ 2) * GR_BYTES + rt * 1024 + L * 256, (u32x2){pk2(bitsf(re[0]), bitsf(re[1])), htag});
      }
      LSTAMP();
      if (rt >= 14) WAITV(0);
      PIN(f);
      if (L == 0) {
        if (!__all(f[0] == tlo && f[1] == thi)) poll_words(fl, fo, tlo, thi);
#pragma unroll 1
        for (int i = 0; i < APRE; ++i) issue_a();
      } else {
        __builtin_amdgcn_s_sleep(HOP2_SLEEP);
      }
      const char* rb = ws + WS_RED + jg * GR_BYTES + L * 1024;
#pragma unroll 1
      for (unsigned spins = 0; spins < SPIN_CAP; ++spins) {
#pragma unroll
        for (int i = 0; i < 4; ++i) pv[i] = gld128_sc1(boff, rb + ((i < 3 || L < 2) ? i * 4096 : 0));
        asm volatile("s_waitcnt vmcnt(0)" : "+v"(pv[0]), "+v"(pv[1]), "+v"(pv[2]), "+v"(pv[3])::"memory");
        LSTAMP();
        unsigned bad = 0;
#pragma unroll
        for (int i = 0; i < 4; ++i) {
          asm volatile("ds_write2_b32 %0, %1, %2 offset0:0 offset1:1" ::"v"(ia[i]), "v"(pv[i][0]), "v"(pv[i][2]) : "memory");
          bad |= (pv[i][1] ^ tlo) | (pv[i][3] ^ thi);
        }
        if (__all(bad == 0)) break;
        __builtin_amdgcn_s_sleep(RETRY_SLEEP);
      }
      BAR();
      LSTAMP();
    } else {
      LBAR();
      if (L == 0 && lane == 0) gst64_sc1(rt * 128 + jg * 8, ws + WS_HF, tagv);
    }
    __builtin_amdgcn_s_setprio(0);
    if (L == 1) {
      WAITV(0);
      LBAR();
#pragma unroll 1
      for (int s = 0; s < nb; ++s) {
        WAITV(40);
        LBAR();
        issue_b(1);
      }
      WAITV(40); LBAR();
      WAITV(30); LBAR();
      issue_b(1);
      WAITV(20); LBAR();
      issue_b(1);
      WAITV(10); LBAR();
      WAITV(0); LBAR();
    } else if (L == 0) {
      LSTAMP();
      if (!isq) poll_words(fl, fo, tlo, thi);
      LSTAMP();
#pragma unroll 1
      for (int s = isq ? APRE - 4 : -4; s < nb - 4; ++s) {
        if (s == (isq ? APRE - 4 : -1) || s >= 0) { WAITV(32); LBAR(); LSTAMP(); }
        issue_a();
      }
      fl = ws + WS_OF + rt2 * 512;
      fo = lane * 8;
      u32x2 f;
      asm volatile("s_waitcnt vmcnt(32)\n\ts_barrier\n\tglobal_load_dwordx2 %0, %1, %2 sc1\n\ts_waitcnt vmcnt(17)\n\ts_barrier\n\t"
                   "s_waitcnt vmcnt(1)\n\ts_barrier\n\ts_waitcnt vmcnt(0)"
                   : "=&v"(f) : "v"(fo), "s"(fl) : "memory");
      LSTAMP();
      ok = __all(f[0] == tlo && f[1] == thi);
      if (!ok) { LBAR(); poll_words(fl, fo, tlo, thi); }
      LSTAMP();
      const char* asrc = abase + 54 * 1024;
#pragma unroll 1
      for (int s = 0; s < 5; ++s) {
        if (s == 2 && ok) LBAR();
        if (s >= 3) { WAITV(32); LBAR(); LSTAMP(); }
        asm volatile(
            "s_mov_b32 m0, %5\n\ts_nop 0\n\tglobal_load_lds_dwordx4 %0, %4 sc1\n\tglobal_load_lds_dwordx4 %0, %4 offset:1024 sc1\n\t"
            "global_load_lds_dwordx4 %0, %4 offset:2048 sc1\n\tglobal_load_lds_dwordx4 %0, %4 offset:3072 sc1\n\t"
            "s_add_u32 m0, m0, 4096\n\ts_nop 0\n\tglobal_load_lds_dwordx4 %1, %4 sc1\n\tglobal_load_lds_dwordx4 %1, %4 offset:1024 sc1\n\t"
            "global_load_lds_dwordx4 %1, %4 offset:2048 sc1\n\tglobal_load_lds_dwordx4 %1, %4 offset:3072 sc1\n\t"
            "s_add_u32 m0, m0, 4096\n\ts_nop 0\n\tglobal_load_lds_dwordx4 %2, %4 sc1\n\tglobal_load_lds_dwordx4 %2, %4 offset:1024 sc1\n\t"
            "global_load_lds_dwordx4 %2, %4 offset:2048 sc1\n\tglobal_load_lds_dwordx4 %2, %4 offset:3072 sc1\n\t"
            "s_add_u32 m0, m0, 4096\n\ts_nop 0\n\tglobal_load_lds_dwordx4 %3, %4 sc1\n\tglobal_load_lds_dwordx4 %3, %4 offset:1024 sc1\n\t"
            "global_load_lds_dwordx4 %3, %4 offset:2048 sc1\n\tglobal_load_lds_dwordx4 %3, %4 offset:3072 sc1"
            ::"v"(ao[0]), "v"(ao[2]), "v"(ao[4]), "v"(ao[6]), "s"(asrc), "s"(al) : "memory", "scc");
        asrc += 4096;
        al = al == L_AR + 49152 ? L_AR : al + 16384;
      }
      WAITV(32); LBAR(); LSTAMP();
      WAITV(16); LBAR(); LSTAMP();
      WAITV(0); LBAR(); LSTAMP();
    }
    return;
  }

  asm volatile("s_waitcnt lgkmcnt(0)" ::"s"(ka), "s"(kws));
  const int mw = w & 3, nw = w >> 2;
  const char *wphi = a_wphi, *gate = a_gate;
  char *out = a_out, *ws = a_ws;
  u64 epoch;
  {
    const char *px = a_x, *pq = a_wqkv, *ph = a_w1;
    asm volatile("s_load_dwordx2 %0, %1, 0x0" : "=&s"(epoch) : "s"(ws), "s"(px), "s"(pq), "s"(ph), "s"(wphi), "s"(gate), "s"(out) : "memory");
  }
  unsigned warm = 0;
  if (w == 4) {
    const unsigned o = ((idx * 8 + (lane & 7)) * 128);
    asm volatile("global_load_dword %0, %1, %2" : "=v"(warm) : "v"(o < CODE_BYTES ? o : 0), "s"(pc & ~127ull) : "memory");
  }
  {
    if (w == 3) {
      __builtin_amdgcn_s_setprio(3);
      const char* src = isq ? wphi + jg * 3200 : gate;
      const unsigned lim = isq ? 3184 : 1264;
#pragma unroll
      for (int n = 0; n < 4; ++n) {
        const unsigned o = n * 1024 + boff;
        glds(o < lim ? o : lim, src, L_WPHI + n * 1024);
      }
      __builtin_amdgcn_s_setprio(0);
    }
  }
  if (tid < 128) sts128(L_ZERO + tid * 16, (u32x4){0u, 0u, 0u, 0u});
  if (tid < 448) {
    const int hd = tid >= 224 ? 1 : 0, j = tid - 224 * hd, r = j - 160;
    const int ft = r >= 40 ? 2 : (r >= 20 ? 1 : 0), r2 = r - 20 * ft, gq = (r2 * 13) >> 6;
    const int d = j < 160 ? (j & 15) : 16 + r2 - 5 * gq, quad = j < 160 ? (j >> 4) : 4 * ft + gq;
    *(LAS unsigned*)(size_t)(L_TAB + tid * 4) = j < 210 ? L_KV + hd * 1680 + d * 80 + quad * 8 : L_ONE + 16;
  }
  if (tid == 128) sts128(L_ONE, (u32x4){0x3f803f80u, 0x3f803f80u, 0x3f803f80u, 0x3f803f80u});
  TAG();

  const unsigned xa = L_S0 + (32 * mw + c16) * 128 + ((g ^ ((c16 >> 1) & 7)) << 4);
  const unsigned wrd = L_S0 + 16384 + (4 * g + q4) * 480 + (isq ? 40 * (p4 & 1) + 240 * (p4 >> 1) : 8 * p4);
  const bool gatew = !isq && jg == 7 && nw == 1;
  const bool wide = nw == 0 && jg < 6;
  f32x4 acc[8][2];
  unsigned pbase = (p4 == g) ? L_WPHI + (2 * nw + (g & 1)) * 800 + q4 * 40 : L_ZERO;
  unsigned phiw = L_PHIK + (32 * mw + 16 * (g >> 1) + c16) * 352 + 80 * (2 * nw + (g & 1));
  unsigned vw = L_V + (32 * mw + c16) * 160 + 40 * g + 24 * nw;
  unsigned ab = L_PHIK + (4 * g + q4) * 352 + 80 * mw + 8 * p4;
  const bool vreal = nw == 0 || p4 == 0;
  unsigned vb = vreal ? L_V + (4 * g + q4) * 160 + 40 * mw + 32 * nw + 8 * p4 : L_ONE;
  unsigned po = (mw * 224 + (nw == 0 ? lane : 160 + 5 * g + c16)) * 16;
  unsigned ab2 = ab + 64 - 8 * (p4 & 2), vs = vreal ? 2560 : 0;
  PIN(pbase); PIN(phiw); PIN(vw); PIN(ab); PIN(vb); PIN(po); PIN(ab2); PIN(vs);
  STAMP(1);
  WAITV(0);
  if (isq) {
    if (nw == 0) gemm1_loop<8, true, false>(acc, xa, wrd, 0, 0);
    else gemm1_loop<7, true, false>(acc, xa, wrd, 0, 0);
  } else {
    const unsigned wh = wrd + (nw == 0 ? 0 : (jg < 6 ? 256 : 192));
    if (wide) gemm1_loop<8, false, false>(acc, xa, wh, 0, 0);
    else if (gatew) gemm1_loop<7, false, true>(acc, xa, wh, L_GATE + g * 16 + rot * 128, 2 * rot);
    else gemm1_loop<6, false, false>(acc, xa, wh, 0, 0);
  }
  STAMP(2);
  PIN(warm);

  unsigned kq[10];
  if (!isq) {
    const int k0 = (hstart >> 6) + (nw == 0 ? 0 : (jg < 6 ? 4 : 3));
    char* abase = ws + WS_A + ((rt * 8 + 2 * mw) * 74 + k0) * 1024;
#pragma unroll
    for (int pr = 0; pr < 4; ++pr) {
      if (pr < 3 || wide) {
        const u32x4 f0 = silu8(acc[2 * pr][0], acc[2 * pr + 1][0]), f1 = silu8(acc[2 * pr][1], acc[2 * pr + 1][1]);
        gst128_sc1(boff, abase + pr * 1024, f0);
        gst128_sc1(boff, abase + (74 + pr) * 1024, f1);
      }
    }
    if (gatew && g == 0) {
#pragma unroll
      for (int mt = 0; mt < 2; ++mt) {
        const float gv = __builtin_amdgcn_rcpf(1.f + __builtin_amdgcn_exp2f(acc[6][mt][0] * -1.4426950408889634f));
        gst32_sc1((rt * 128 + 32 * mw + 16 * mt + c16) * 4, ws + WS_G, __builtin_bit_cast(unsigned, gv));
      }
    }
    STAMP(3);
    WAITV(0);
    BAR();
    STAMP(4);
#pragma unroll
    for (int i = 0; i < 10; ++i) kq[i] = 0;
  } else {
    WPhi wf;
    wphi_read(wf, pbase);
    SB();
    unsigned kk[10];
#pragma unroll
    for (int i = 0; i < 10; ++i) {
      const f32x4 a = acc[i >> 1][0], c = acc[i >> 1][1];
      kq[i] = pk2(a[2 * (i & 1)], a[2 * (i & 1) + 1]); kk[i] = pk2(c[2 * (i & 1)], c[2 * (i & 1) + 1]);
    }
    SB();
#pragma unroll
    for (int i = 0; i < 10; ++i) {
      const auto r = __builtin_amdgcn_permlane32_swap(kq[i], kk[i], false, false);
      kq[i] = r[0]; kk[i] = r[1];
    }
    SB();
    f32x4 pp[5];
    phi_proj(wf, kk, pp);
    SB();
#pragma unroll
    for (int i = 5; i < 8; ++i) {
      if (i < 7 || nw == 0) {
#pragma unroll
        for (int mt = 0; mt < 2; ++mt) {
          const f32x4 a = acc[i][mt];
          sts64(vw + mt * 2560 + 8 * (i - 5), (u32x2){pk2(a[0], a[1]), pk2(a[2], a[3])});
        }
      }
    }
    SB();
    unsigned phi[20];
    PROJ_DONE(pp);
    dual_softmax<true>(pp, phi, phiw);
    unsigned va[8];
    va[0] = vb;
#pragma unroll
    for (int i = 1; i < 8; ++i) { va[i] = va[i - 1] + vs; PIN(va[i]); }
    BAR();
    STAMP(3);
    u32x4 vf[4], pf[4][3];
#pragma unroll
    for (int ks = 0; ks < 4; ++ks) {
      const unsigned o = ks * 32 * 352;
      vf[ks] = cat(ldstr(va[2 * ks]), ldstr(va[2 * ks + 1]));
      pf[ks][0] = cat(ldstr(ab + o), ldstr(ab + o + 16 * 352));
      pf[ks][1] = cat(ldstr(ab + o + 32), ldstr(ab + o + 32 + 16 * 352));
      pf[ks][2] = cat(ldstr(ab2 + o), ldstr(ab2 + o + 16 * 352));
      SB();
    }
    f32x4 kv[3];
#pragma unroll
    for (int i = 0; i < 3; ++i) kv[i] = (f32x4){0.f, 0.f, 0.f, 0.f};
#pragma unroll
    for (int ks = 0; ks < 4; ++ks)
#pragma unroll
      for (int i = 0; i < 3; ++i) kv[i] = mfma(pf[ks][i], vf[ks], kv[i]);
    char* pb = ws + WS_PART + (jg * 16 + rt) * GR_BYTES;
    const unsigned fs = nw == 0 ? 1024 : 320;
    if (nw == 0 || c16 < 5) {
      gst128_sc1(po, pb, (u32x4){pk2(kv[0][0], kv[0][1]), tlo, pk2(kv[0][2], kv[0][3]), thi});
      gst128_sc1(po, pb + fs, (u32x4){pk2(kv[1][0], kv[1][1]), tlo, pk2(kv[1][2], kv[1][3]), thi});
      if (g < 2) gst128_sc1(po, pb + 2 * fs, (u32x4){pk2(kv[2][0], kv[2][1]), tlo, pk2(kv[2][2], kv[2][3]), thi});
    }
  }

  {
    P2 p;
    p.aa = L_AR + mw * 4096 + boff;
    p.ba = L_BR + nw * 3072 + boff;
    p.st = 0;
    f32x4 ob[2][3], oa[3];
#pragma unroll
    for (int i = 0; i < 6; ++i) ob[i / 3][i % 3] = (f32x4){0.f, 0.f, 0.f, 0.f};
    if (isq) {
      BAR();
      STAMP(5);
      unsigned phq[20];
      {
        WPhi wf;
        wphi_read(wf, pbase);
        SB();
        f32x4 pp[5];
        phi_proj(wf, kq, pp);
        PROJ_DONE(pp);
        dual_softmax<false>(pp, phq, 0);
      }
#pragma unroll
      for (int i = 0; i < 10; ++i) {
        const int a = i < 8 ? i : 8 + i, c = i < 8 ? i + 8 : 10 + i;
        const auto r = __builtin_amdgcn_permlane32_swap(phq[a], phq[c], false, false);
        phq[a] = r[0]; phq[c] = r[1];
      }
#pragma unroll
      for (int i = 0; i < 10; ++i) {
        const int a = i < 8 ? (i & 3) + 8 * (i >> 2) : 2 * i, c = i < 8 ? a + 4 : a + 1;
        const auto r = __builtin_amdgcn_permlane16_swap(phq[a], phq[c], false, false);
        phq[a] = r[0]; phq[c] = r[1];
      }
      unsigned k0a = L_KV + 2 * nw * 1680 + c16 * 80, k1a = L_KV + 2 * nw * 1680 + (p4 == 0 ? 16 + q4 : 20) * 80;
      unsigned k0b = k0a + 64 + 4 * g, k1b = k1a + 64 + 4 * g;
      k0a += 16 * g; k1a += 16 * g;
      unsigned ow = (g & 1) * 74 * 1024 + ((2 * (jg & 1) + nw) * 16 + c16) * 16 + (g >> 1) * 8;
      PIN(k0a); PIN(k1a); PIN(k0b); PIN(k1b); PIN(ow);
      u32x4 qf[4];
      u32x2 qt[4], a16[2][2];
#pragma unroll
      for (int u = 0; u < 4; ++u) {
        qf[u] = (u32x4){phq[4 * u], phq[4 * u + 1], phq[4 * u + 2], phq[4 * u + 3]};
        qt[u] = (u32x2){phq[16 + u], 0u};
        a16[u >> 1][u & 1] = (u32x2){0u, 0u};
        PIN(qf[u]); PIN(qt[u]); PIN(a16[u >> 1][u & 1]);
      }
      STAMP(6);
      BAR();
      STAMP(7);
      {
        u32x4 a32[2][2];
#pragma unroll
        for (int h = 0; h < 2; ++h) { a32[h][1] = lds128(k1a + h * 1680); a16[h][1][0] = *(LAS unsigned*)(size_t)(k1b + h * 1680); }
#pragma unroll
        for (int h = 0; h < 2; ++h) { a32[h][0] = lds128(k0a + h * 1680); a16[h][0][0] = *(LAS unsigned*)(size_t)(k0b + h * 1680); }
        SB();
        f32x4 o[4][2];
#pragma unroll
        for (int t = 1; t >= 0; --t)
          asm volatile("s_nop 1\n\tv_mfma_f32_16x16x32_bf16 %0, %4, %8, 0\n\tv_mfma_f32_16x16x32_bf16 %1, %5, %9, 0\n\t"
                       "v_mfma_f32_16x16x32_bf16 %2, %4, %10, 0\n\tv_mfma_f32_16x16x32_bf16 %3, %5, %11, 0\n\ts_nop 2\n\t"
                       "v_mfma_f32_16x16x16_bf16 %0, %6, %12, %0\n\tv_mfma_f32_16x16x16_bf16 %1, %7, %13, %1\n\t"
                       "v_mfma_f32_16x16x16_bf16 %2, %6, %14, %2\n\tv_mfma_f32_16x16x16_bf16 %3, %7, %15, %3"
                       : "=&v"(o[0][t]), "=&v"(o[1][t]), "=&v"(o[2][t]), "=&v"(o[3][t])
                       : "v"(a32[0][t]), "v"(a32[1][t]), "v"(a16[0][t]), "v"(a16[1][t]), "v"(qf[0]), "v"(qf[1]), "v"(qf[2]), "v"(qf[3]),
                         "v"(qt[0]), "v"(qt[1]), "v"(qt[2]), "v"(qt[3]));
        asm volatile("" : "+v"(o[0][1]), "+v"(o[1][1]), "+v"(o[2][1]), "+v"(o[3][1]));
        char* abase = ws + WS_A + ((rt * 8 + 2 * mw) * 74 + 54 + (jg >> 1) * 5) * 1024;
        float sc[4];
#pragma unroll
        for (int u = 0; u < 4; ++u) sc[u] = __builtin_amdgcn_rcpf(o[u][1][1] + 1e-6f);
        unsigned l0 = pk2(o[0][1][0] * sc[0], o[1][1][0] * sc[1]), l1 = pk2(o[2][1][0] * sc[2], o[3][1][0] * sc[3]);
        asm volatile("s_nop 7" : "+v"(o[0][0]), "+v"(o[1][0]), "+v"(o[2][0]), "+v"(o[3][0]), "+v"(l0), "+v"(l1));
#pragma unroll
        for (int mt = 0; mt < 2; ++mt) {
          const f32x4 a = o[2 * mt][0] * sc[2 * mt], c = o[2 * mt + 1][0] * sc[2 * mt + 1];
          gst128_sc1(boff, abase + (mt * 74 + 2 * (jg & 1) + nw) * 1024, (u32x4){pk2(a[0], a[1]), pk2(a[2], a[3]), pk2(c[0], c[1]), pk2(c[2], c[3])});
        }
        const auto r = __builtin_amdgcn_permlane16_swap(l0, l1, false, false);
        gst64_sc1(ow, abase + 4 * 1024, (u32x2){r[0], r[1]});
      }
      STAMP(4);
    }
#pragma unroll
    for (int i = 0; i < 6; ++i) PIN(ob[i / 3][i % 3]);
    char* pbuf = ws + WS_P + ((rt2 * 8 + ct) * 16 + w) * 3072;
    const unsigned row = rt2 * 128 + 32 * mw + (isq ? 0 : 16) + c16;
    const char* prd = pbuf + (isq ? 8 * 3072 : 0);
    unsigned p2o = boff + (nw == 0 ? 2048 : 0), go = row * 4;
    unsigned oo = row * 1280 + ct * 160 + nw * 96 + (g & 1) * 32 + (g >> 1) * 16, oo2 = row * 1280 + ct * 160 + 64 + g * 8;
    u32x4 pt[3];
    PIN(p.aa); PIN(p.ba); PIN(p2o); PIN(go); PIN(oo); PIN(oo2);
    __builtin_amdgcn_s_setprio(2);
    BAR();
    if (!isq) STAMP(5);
    p2_first(p);
#pragma unroll 1
    for (int part = 0; part < 2; ++part) {
      p2_run(p, ob, part == 0 ? OFL : (isq ? NQ2 : NH2) - OFL, nw);
      if (part == 0 && isq) {
        WAITV(0);
        if (lane == 0) gst64_sc1(rt * 512 + (jg * 8 + w) * 8, ws + WS_OF, tagv);
        STAMP(8);
      }
    }
    unsigned gb = gld32_sc1(go, ws + WS_G);
#pragma unroll
    for (int n = 0; n < 3; ++n) {
      oa[n] = (f32x4){0.f, 0.f, 0.f, 0.f};
      if (n < 2 || nw == 0)
        gst128_sc1(boff, pbuf + (isq ? 0 : 8 * 3072) + n * 1024, (u32x4){pk2(ob[1][n][0], ob[1][n][1]), tlo, pk2(ob[1][n][2], ob[1][n][3]), thi});
    }
    STAMP(9);
    p.sa = p.st;
    p2a_run(p, oa, p.aa, nw, pt, boff, p2o, prd);
    PIN(gb);
    STAMP(10);
#pragma unroll 1
    for (unsigned spins = 0; spins < SPIN_CAP; ++spins) {
      const unsigned bad = (pt[0][1] ^ tlo) | (pt[0][3] ^ thi) | (pt[1][1] ^ tlo) | (pt[1][3] ^ thi) | (pt[2][1] ^ tlo) | (pt[2][3] ^ thi);
      if (__all(bad == 0)) break;
      __builtin_amdgcn_s_sleep(8);
      asm volatile("global_load_dwordx4 %0, %3, %5 sc1\n\tglobal_load_dwordx4 %1, %3, %5 offset:1024 sc1\n\tglobal_load_dwordx4 %2, %4, %5 sc1\n\ts_waitcnt vmcnt(0)"
                   : "=&v"(pt[0]), "=&v"(pt[1]), "=&v"(pt[2]) : "v"(boff), "v"(p2o), "s"(prd) : "memory");
    }
    const float gv = bitsf(gb), omg = 1.f - gv;
    unsigned r[3][2];
#pragma unroll
    for (int n = 0; n < 3; ++n) {
      const f32x4 q = {bitsf(pt[n][0] << 16), bitsf(pt[n][0] & 0xffff0000u), bitsf(pt[n][2] << 16), bitsf(pt[n][2] & 0xffff0000u)};
      const f32x4 t = (ob[0][n] + q) * omg + oa[n] * gv;
      r[n][0] = pk2(t[0], t[1]); r[n][1] = pk2(t[2], t[3]);
    }
    const auto s0 = __builtin_amdgcn_permlane16_swap(r[0][0], r[1][0], false, false);
    const auto s1 = __builtin_amdgcn_permlane16_swap(r[0][1], r[1][1], false, false);
    asm volatile("global_store_dwordx4 %0, %1, %2\n\ts_nop 1" ::"v"(oo), "v"((u32x4){s0[0], s1[0], s0[1], s1[1]}), "s"(out) : "memory");
    if (nw == 0) gst64(oo2, out, (u32x2){r[2][0], r[2][1]});
  }
}

extern "C" void kernel_launch(void* const* d_in, const int* in_sizes, int n_in, void* d_out, int out_size, void* d_ws,
                              size_t ws_size, hipStream_t stream) {
  (void)in_sizes; (void)n_in; (void)out_size; (void)ws_size;
  (void)hipFuncSetAttribute((const void*)fused_kernel, hipFuncAttributeMaxDynamicSharedMemorySize, LDS_BYTES);
  fused_kernel<<<256, 768, LDS_BYTES, stream>>>((const char*)d_in[0], (const char*)d_in[1], (const char*)d_in[2],
                                                (const char*)d_in[3], (const char*)d_in[5], (const char*)d_in[6],
                                                (const char*)d_in[7], (char*)d_out, (char*)d_ws);
}
```

```cpp
#include <hip/hip_runtime.h>
#include <stdint.h>

typedef __attribute__((ext_vector_type(8))) __bf16 bf16x8;
typedef __attribute__((ext_vector_type(2))) __bf16 bf16x2;
typedef __attribute__((ext_vector_type(4))) short s16x4;
typedef __attribute__((ext_vector_type(4))) float f32x4;
typedef __attribute__((ext_vector_type(2))) float f32x2;
typedef __attribute__((ext_vector_type(4))) unsigned u32x4;
typedef __attribute__((ext_vector_type(2))) unsigned u32x2;
typedef unsigned long long u64;
#define LAS __attribute__((address_space(3)))
#define DI __device__ __forceinline__

constexpr unsigned WS_EPOCH = 0;
constexpr unsigned WS_RT = 1024;
constexpr unsigned WS_HF = 4096;
constexpr unsigned WS_OF = 12288;
constexpr unsigned WS_G = 20480;
constexpr unsigned WS_RED = 49152;
constexpr unsigned GR_BYTES = 896 * 16;
constexpr unsigned WS_PART = WS_RED + 8 * GR_BYTES;
constexpr unsigned WS_B = 2u << 20;
constexpr unsigned WS_A = 5u << 20;
constexpr unsigned WS_P = 15u << 20;
constexpr unsigned WS_PROF = 24u << 20;
static_assert(WS_PART + 128 * GR_BYTES <= WS_B, "ws");

constexpr unsigned SLOT = 47104;
constexpr unsigned L_S1 = 0, L_S2 = SLOT, L_S0 = 2 * SLOT, L_MISC = 3 * SLOT;
constexpr unsigned L_PHIK = L_S2 + 40960;
constexpr unsigned L_V = L_PHIK + 128 * 352;
constexpr unsigned L_BR = 0;
constexpr unsigned L_AR = 81920;
constexpr unsigned L_KV = 147456;
constexpr unsigned L_WPHI = 154240;
constexpr unsigned L_GATE = L_WPHI;
constexpr unsigned L_ZERO = L_WPHI + 4096;
constexpr unsigned L_ONE = L_ZERO + 2048;
constexpr unsigned L_TAB = L_ONE + 32;
constexpr unsigned LDS_BYTES = 163840;
static_assert(L_ONE + 32 + 3360 <= LDS_BYTES, "lds");

#define WPOL ""
#define HOP1_SLEEP 12
#define HOP2_SLEEP 14
#define RT_SLEEP 0
#define OFL 1
#define TLBM 0
#define SPF 0
#define P2ROT 2
#define RETRY_SLEEP 1
#define POLL_SLEEP 2
#define APRE 2
#define PREW 0
#define BQ2 2
#define BQ3 1
#define AIMG 2
#define NQ2 7
#define SG 2
constexpr int NH2 = 27 - NQ2;
#define CODE_SZ 23808
constexpr unsigned CODE_BYTES = CODE_SZ;
constexpr unsigned SPIN_CAP = 1u << 16;

DI unsigned pk2(float a, float b) {
  bf16x2 v = __builtin_convertvector((f32x2){a, b}, bf16x2);
  return __builtin_bit_cast(unsigned, v);
}
DI unsigned fbits(float f) { return __builtin_bit_cast(unsigned, f); }
DI float bitsf(unsigned u) { return __builtin_bit_cast(float, u); }
DI f32x4 mfma(u32x4 a, u32x4 b, f32x4 c) {
  return __builtin_amdgcn_mfma_f32_16x16x32_bf16(__builtin_bit_cast(bf16x8, a), __builtin_bit_cast(bf16x8, b), c, 0, 0, 0);
}
DI u32x4 lds128(unsigned a) { return *(LAS u32x4*)(size_t)a; }
DI u32x2 lds64(unsigned a) { return *(LAS u32x2*)(size_t)a; }
DI void sts128(unsigned a, u32x4 v) { *(LAS u32x4*)(size_t)a = v; }
DI void sts64(unsigned a, u32x2 v) { *(LAS u32x2*)(size_t)a = v; }
DI u32x2 ldstr(unsigned a) {
  return __builtin_bit_cast(u32x2, __builtin_amdgcn_ds_read_tr16_b64_v4i16((LAS s16x4*)(size_t)a));
}
DI u32x4 cat(u32x2 a, u32x2 b) { return (u32x4){a[0], a[1], b[0], b[1]}; }
DI void glds(unsigned voff, const void* sbase, unsigned lds) {
  asm volatile("s_mov_b32 m0, %2\n\ts_nop 0\n\tglobal_load_lds_dwordx4 %0, %1" ::"v"(voff), "s"(sbase), "s"(lds) : "memory");
}
DI void glds_nt(unsigned voff, const void* sbase, unsigned lds) {
  asm volatile("s_mov_b32 m0, %2\n\ts_nop 0\n\tglobal_load_lds_dwordx4 %0, %1 nt" ::"v"(voff), "s"(sbase), "s"(lds) : "memory");
}
DI void glds5_sc1(unsigned voff, unsigned voff1k, const void* sbase, unsigned lds) {
  asm volatile(
      "s_mov_b32 m0, %2\n\ts_nop 0\n\t"
      "global_load_lds_dwordx4 %0, %1 sc1\n\t"
      "global_load_lds_dwordx4 %0, %1 offset:1024 sc1\n\t"
      "global_load_lds_dwordx4 %0, %1 offset:2048 sc1\n\t"
      "global_load_lds_dwordx4 %0, %1 offset:3072 sc1\n\t"
      "s_add_u32 m0, m0, 1024\n\ts_nop 0\n\t"
      "global_load_lds_dwordx4 %3, %1 offset:3072 sc1" ::"v"(voff), "s"(sbase), "s"(lds), "v"(voff1k)
      : "memory", "scc");
}
DI u32x4 gld128_sc1(unsigned voff, const void* sbase) {
  u32x4 r;
  asm volatile("global_load_dwordx4 %0, %1, %2 sc1" : "=v"(r) : "v"(voff), "s"(sbase) : "memory");
  return r;
}
DI u32x2 gld64_sc1(unsigned voff, const void* sbase) {
  u32x2 r;
  asm volatile("global_load_dwordx2 %0, %1, %2 sc1" : "=v"(r) : "v"(voff), "s"(sbase) : "memory");
  return r;
}
DI unsigned gld32_sc1(unsigned voff, const void* sbase) {
  unsigned r;
  asm volatile("global_load_dword %0, %1, %2 sc1" : "=v"(r) : "v"(voff), "s"(sbase) : "memory");
  return r;
}
DI unsigned gld32(unsigned voff, const void* sbase) {
  unsigned r;
  asm volatile("global_load_dword %0, %1, %2" : "=v"(r) : "v"(voff), "s"(sbase) : "memory");
  return r;
}
DI void gst128_sc1(unsigned voff, void* sbase, u32x4 v) {
  asm volatile("global_store_dwordx4 %0, %1, %2 sc1\n\ts_nop 1" ::"v"(voff), "v"(v), "s"(sbase) : "memory");
}
DI void gst64_sc1(unsigned voff, void* sbase, u32x2 v) {
  asm volatile("global_store_dwordx2 %0, %1, %2 sc1" ::"v"(voff), "v"(v), "s"(sbase) : "memory");
}
DI void gst32_sc1(unsigned voff, void* sbase, unsigned v) {
  asm volatile("global_store_dword %0, %1, %2 sc1" ::"v"(voff), "v"(v), "s"(sbase) : "memory");
}
DI void gst64(unsigned voff, void* sbase, u32x2 v) {
  asm volatile("global_store_dwordx2 %0, %1, %2" ::"v"(voff), "v"(v), "s"(sbase) : "memory");
}
#define WAITV(n) asm volatile("s_waitcnt vmcnt(" #n ")" ::: "memory")
#define BAR() asm volatile("s_waitcnt lgkmcnt(0)\n\ts_barrier" ::: "memory")
#define PIN(x) asm volatile("" : "+v"(x))
#define SB() __builtin_amdgcn_sched_barrier(0)

DI void poll_words(const char* base, unsigned voff, unsigned tlo, unsigned thi) {
#pragma unroll 1
  for (unsigned spins = 0; spins < SPIN_CAP; ++spins) {
    u32x2 v = gld64_sc1(voff, base);
    asm volatile("s_waitcnt vmcnt(0)" : "+v"(v)::"memory");
    if (__all(v[0] == tlo && v[1] == thi)) break;
    __builtin_amdgcn_s_sleep(POLL_SLEEP);
  }
}

template <bool STORE>
DI void dual_softmax(const f32x4 (&p)[5], unsigned (&o)[20], unsigned dst) {
  float mx = __builtin_fmaxf(p[0][0], p[0][1]), mn = __builtin_fminf(p[0][0], p[0][1]);
  float mx2 = __builtin_fmaxf(p[0][2], p[0][3]), mn2 = __builtin_fminf(p[0][2], p[0][3]);
#pragma unroll
  for (int t = 1; t < 5; ++t) {
    mx = __builtin_fmaxf(__builtin_fmaxf(mx, p[t][0]), p[t][1]); mn = __builtin_fminf(__builtin_fminf(mn, p[t][0]), p[t][1]);
    mx2 = __builtin_fmaxf(__builtin_fmaxf(mx2, p[t][2]), p[t][3]); mn2 = __builtin_fminf(__builtin_fminf(mn2, p[t][2]), p[t][3]);
  }
  const float L = 1.4426950408889634f;
  const float mxl = __builtin_fmaxf(mx, mx2) * -L, mnl = __builtin_fminf(mn, mn2) * L;
  f32x2 ep[10], en[10];
#pragma unroll
  for (int i = 0; i < 10; ++i) {
    const f32x2 v = {p[i >> 1][2 * (i & 1)], p[i >> 1][2 * (i & 1) + 1]};
    ep[i] = __builtin_elementwise_fma(v, (f32x2){L, L}, (f32x2){mxl, mxl});
    en[i] = __builtin_elementwise_fma(v, (f32x2){-L, -L}, (f32x2){mnl, mnl});
  }
  SB();
  f32x2 sp, sn;
#pragma unroll
  for (int i = 0; i < 10; ++i) {
    ep[i][0] = __builtin_amdgcn_exp2f(ep[i][0]); ep[i][1] = __builtin_amdgcn_exp2f(ep[i][1]);
    en[i][0] = __builtin_amdgcn_exp2f(en[i][0]); en[i][1] = __builtin_amdgcn_exp2f(en[i][1]);
    if (i == 1) { sp = ep[0]; sn = en[0]; }
    if (i > 1) { sp += ep[i - 1]; sn += en[i - 1]; }
    SB();
  }
  sp += ep[9]; sn += en[9];
  const float rp = __builtin_amdgcn_rcpf(sp[0] + sp[1]), rn = __builtin_amdgcn_rcpf(sn[0] + sn[1]);
  f32x2 rp2 = {rp, rp}, rn2 = {rn, rn};
  PIN(rp2); PIN(rn2);
#pragma unroll
  for (int c = 0; c < 5; ++c) {
    f32x2 t[4];
#pragma unroll
    for (int i = 0; i < 4; ++i) t[i] = 4 * c + i < 10 ? ep[4 * c + i] * rp2 : en[4 * c + i - 10] * rn2;
    SB();
#pragma unroll
    for (int i = 0; i < 4; ++i) o[4 * c + i] = pk2(t[i][0], t[i][1]);
    if (STORE) sts128(dst + 16 * c, (u32x4){o[4 * c], o[4 * c + 1], o[4 * c + 2], o[4 * c + 3]});
    SB();
  }
}
struct WPhi { u32x4 a0[5], a1[5]; u32x2 a2[5]; };
DI void wphi_read(WPhi& f, unsigned pbase) {
#pragma unroll
  for (int t = 0; t < 5; ++t) f.a0[t] = cat(ldstr(pbase + 8 * t), ldstr(pbase + 8 * t + 160));
#pragma unroll
  for (int t = 0; t < 5; ++t) f.a1[t] = cat(ldstr(pbase + 8 * t + 320), ldstr(pbase + 8 * t + 480));
#pragma unroll
  for (int t = 0; t < 5; ++t) f.a2[t] = ldstr(pbase + 8 * t + 640);
}
DI void phi_proj(const WPhi& f, const unsigned (&kq)[10], f32x4 (&pp)[5]) {
  const u32x4 f0 = {kq[0], kq[1], kq[2], kq[3]}, f1 = {kq[4], kq[5], kq[6], kq[7]};
  const u32x2 f2 = {kq[8], kq[9]};
  asm volatile("s_nop 1\n\tv_mfma_f32_16x16x32_bf16 %0, %5, %10, 0\n\tv_mfma_f32_16x16x32_bf16 %1, %6, %10, 0\n\tv_mfma_f32_16x16x32_bf16 %2, %7, %10, 0\n\t"
               "v_mfma_f32_16x16x32_bf16 %3, %8, %10, 0\n\tv_mfma_f32_16x16x32_bf16 %4, %9, %10, 0"
               : "=&v"(pp[0]), "=&v"(pp[1]), "=&v"(pp[2]), "=&v"(pp[3]), "=&v"(pp[4])
               : "v"(f.a0[0]), "v"(f.a0[1]), "v"(f.a0[2]), "v"(f.a0[3]), "v"(f.a0[4]), "v"(f0), "v"(f1), "v"(f2));
  asm volatile("v_mfma_f32_16x16x32_bf16 %0, %5, %10, %0\n\tv_mfma_f32_16x16x32_bf16 %1, %6, %10, %1\n\tv_mfma_f32_16x16x32_bf16 %2, %7, %10, %2\n\t"
               "v_mfma_f32_16x16x32_bf16 %3, %8, %10, %3\n\tv_mfma_f32_16x16x32_bf16 %4, %9, %10, %4"
               : "+v"(pp[0]), "+v"(pp[1]), "+v"(pp[2]), "+v"(pp[3]), "+v"(pp[4])
               : "v"(f.a1[0]), "v"(f.a1[1]), "v"(f.a1[2]), "v"(f.a1[3]), "v"(f.a1[4]), "v"(f1));
  asm volatile("s_nop 1\n\tv_mfma_f32_16x16x16_bf16 %0, %5, %10, %0\n\tv_mfma_f32_16x16x16_bf16 %1, %6, %10, %1\n\tv_mfma_f32_16x16x16_bf16 %2, %7, %10, %2\n\t"
               "v_mfma_f32_16x16x16_bf16 %3, %8, %10, %3\n\tv_mfma_f32_16x16x16_bf16 %4, %9, %10, %4"
               : "+v"(pp[0]), "+v"(pp[1]), "+v"(pp[2]), "+v"(pp[3]), "+v"(pp[4])
               : "v"(f.a2[0]), "v"(f.a2[1]), "v"(f.a2[2]), "v"(f.a2[3]), "v"(f.a2[4]), "v"(f2));
}
#define PROJ_DONE(pp) asm volatile("s_nop 7" : "+v"(pp[0]), "+v"(pp[1]), "+v"(pp[2]), "+v"(pp[3]), "+v"(pp[4]))
DI u32x4 silu8(f32x4 a, f32x4 c) {
  const float NL = -1.4426950408889634f;
  f32x2 v[4] = {{a[0], a[1]}, {a[2], a[3]}, {c[0], c[1]}, {c[2], c[3]}}, e[4];
#pragma unroll
  for (int i = 0; i < 4; ++i) e[i] = v[i] * (f32x2){NL, NL};
#pragma unroll
  for (int i = 0; i < 4; ++i) { e[i][0] = __builtin_amdgcn_exp2f(e[i][0]); e[i][1] = __builtin_amdgcn_exp2f(e[i][1]); }
#pragma unroll
  for (int i = 0; i < 4; ++i) e[i] += (f32x2){1.f, 1.f};
#pragma unroll
  for (int i = 0; i < 4; ++i) { e[i][0] = __builtin_amdgcn_rcpf(e[i][0]); e[i][1] = __builtin_amdgcn_rcpf(e[i][1]); }
#pragma unroll
  for (int i = 0; i < 4; ++i) v[i] *= e[i];
  return (u32x4){pk2(v[0][0], v[0][1]), pk2(v[1][0], v[1][1]), pk2(v[2][0], v[2][1]), pk2(v[3][0], v[3][1])};
}

DI void amfma(f32x4& c, const u32x4& a, const u32x4& b) { asm volatile("v_mfma_f32_16x16x32_bf16 %0, %1, %2, %0" : "+v"(c) : "v"(a), "v"(b)); }
template <int OFF> DI void trlo(u32x4& f, unsigned a) { u32x2 t; asm volatile("ds_read_b64_tr_b16 %0, %1 offset:%2" : "=v"(t) : "v"(a), "i"(OFF)); f[0] = t[0]; f[1] = t[1]; }
template <int OFF> DI void trhi(u32x4& f, unsigned a) { u32x2 t; asm volatile("ds_read_b64_tr_b16 %0, %1 offset:%2" : "=v"(t) : "v"(a), "i"(OFF)); f[2] = t[0]; f[3] = t[1]; }
template <int OFF> DI void rd128(u32x4& f, unsigned a) { asm volatile("ds_read_b128 %0, %1 offset:%2" : "=v"(f) : "v"(a), "i"(OFF)); }
DI void vadd(unsigned& v, unsigned s) { asm volatile("v_add_u32 %0, %1, %0" : "+v"(v) : "s"(s)); }
#define WL(n) asm volatile("s_waitcnt lgkmcnt(" #n ")")
#define MFMA_DONE() asm volatile("s_waitcnt lgkmcnt(0)\n\ts_nop 7\n\ts_nop 3" ::: "memory")

template <int NT, bool Q, bool GATE>
DI void gemm1_loop(f32x4 (&acc)[8][2], unsigned xa, unsigned wa, unsigned wb, unsigned gk) {
  u32x4 W[8], X0[2], X1[2];
  unsigned xb = xa ^ 64;
#pragma unroll
  for (int t = 0; t < NT; ++t) { acc[t][0] = (f32x4){0.f, 0.f, 0.f, 0.f}; acc[t][1] = (f32x4){0.f, 0.f, 0.f, 0.f}; PIN(acc[t][0]); PIN(acc[t][1]); }
  BAR();
#define TO(i) (Q ? ((i) < 5 ? (NT == 8 ? 0 : 80) + 8 * (i) : 160 + 8 * ((i) - (NT == 8 ? 5 : 2))) : 32 * (i))
#define AD(i) wa
#define RL(i, off) if constexpr (GATE && (i) == 6) { rd128<0>(W[i], wb); } else { trlo<TO(i) + (off)>(W[i], AD(i)); }
#define RH(i, off) if constexpr (GATE && (i) == 6) { gk = gk == 19 ? 0 : gk + 1; vadd(wb, gk == 0 ? -1216 : 64); } else { trhi<TO(i) + (off) + 7680>(W[i], AD(i)); }
#define M0(i, X) amfma(acc[i][0], W[i], X[0]);
#define M1(i, X) amfma(acc[i][1], W[i], X[1]);
#define G1(i, j) M0(i, X0) RL(j, 15360) M1(i, X0) RH(j, 15360)
#define G1X(i, j, h) M0(i, X0) RL(j, 15360) rd128<2048 * (h)>(X1[h], xb); M1(i, X0) RH(j, 15360)
  rd128<0>(X0[0], xa); rd128<2048>(X0[1], xa);
  RL(0, 0) RH(0, 0) RL(1, 0) RH(1, 0) RL(2, 0) RH(2, 0) RL(3, 0) RH(3, 0) RL(4, 0) RH(4, 0)
  if constexpr (NT > 6) { RL(5, 0) RH(5, 0) }
  if constexpr (NT > 7) { RL(6, 0) RH(6, 0) }
  unsigned sb = L_S0;
#pragma unroll 1
  for (int t = 0; t < 10; ++t) {
    if constexpr (NT == 8) {
      WL(10); M0(0, X0) RL(7, 0) M1(0, X0) RH(7, 0) G1(1, 0)
      WL(10); G1(2, 1) G1(3, 2)
      WL(10); G1X(4, 3, 0) G1X(5, 4, 1)
      WL(12); G1(6, 5) G1(7, 6)
      WL(5);
    } else if constexpr (NT == 7) {
      WL(8); M0(0, X0) RL(6, 0) M1(0, X0) RH(6, 0) G1(1, 0)
      WL(7); G1(2, 1) G1X(3, 2, 0)
      WL(8); G1X(4, 3, 1) G1(5, 4)
      WL(12); G1(6, 5)
      WL(5);
    } else {
      WL(6); M0(0, X0) RL(5, 0) M1(0, X0) RH(5, 0) G1(1, 0)
      WL(6); G1X(2, 1, 0) G1X(3, 2, 1)
      WL(8); G1(4, 3) G1(5, 4)
      WL(4);
    }
    M0(0, X1) RL(NT - 1, 15360) M1(0, X1) RH(NT - 1, 15360)
    const unsigned sn = sb == L_S0 ? L_S1 : sb + SLOT, dl = sn - sb;
    sb = sn;
    M0(1, X1) vadd(xa, dl); M1(1, X1) vadd(wa, dl);
    M0(2, X1) vadd(xb, dl); M1(2, X1)
    BAR();
    M0(3, X1) rd128<0>(X0[0], xa); rd128<2048>(X0[1], xa); M1(3, X1) RL(0, 0) RH(0, 0)
    M0(4, X1) RL(1, 0) RH(1, 0) M1(4, X1) RL(2, 0) RH(2, 0)
    if constexpr (NT == 8) {
      M0(5, X1) RL(3, 0) RH(3, 0) M1(5, X1) RL(4, 0)
      M0(6, X1) RH(4, 0) RL(5, 0) M1(6, X1) RH(5, 0)
      M0(7, X1) RL(6, 0) M1(7, X1) RH(6, 0)
    } else if constexpr (NT == 7) {
      M0(5, X1) RL(3, 0) RH(3, 0) M1(5, X1) RL(4, 0) RH(4, 0)
      M0(6, X1) RL(5, 0) M1(6, X1) RH(5, 0)
    } else {
      M0(5, X1) RL(3, 0) RH(3, 0) M1(5, X1) RL(4, 0) RH(4, 0)
    }
  }
  MFMA_DONE();
#pragma unroll
  for (int t = 0; t < NT; ++t) { PIN(acc[t][0]); PIN(acc[t][1]); PIN(W[t]); }
  PIN(X0[0]); PIN(X0[1]);
#undef TO
#undef AD
#undef RL
#undef RH
#undef M0
#undef M1
#undef G1
#undef G1X
}

struct P2 {
  u32x4 A[2], B[3];
  unsigned aa, ba;
  unsigned st, sa;
};
DI void p2_first(P2& p) {
  asm volatile("ds_read_b128 %0, %5\n\tds_read_b128 %2, %6\n\tds_read_b128 %3, %6 offset:1024\n\tds_read_b128 %4, %6 offset:2048\n\t"
               "ds_read_b128 %1, %5 offset:2048\n\ts_waitcnt lgkmcnt(0)"
               : "=&v"(p.A[0]), "=&v"(p.A[1]), "=&v"(p.B[0]), "=&v"(p.B[1]), "=&v"(p.B[2]) : "v"(p.aa), "v"(p.ba) : "memory");
}
#define MF(d, a, b) "v_mfma_f32_16x16x32_bf16 %" #d ", %" #a ", %" #b ", %" #d "\n\t"
#define RD(d, a, o) "ds_read_b128 %" #d ", %" #a " offset:" #o "\n\t"
DI void p2_run(P2& p, f32x4 (&o)[2][3], int n, int nw) {
  u32x4 A0, A1, B0, B1, B2;
  unsigned da, db;
  asm volatile(
      "s_cmp_lg_u32 %22, 0\n\ts_cbranch_scc1 2f\n"
      "1:\n\t"
      "s_add_u32 %18, %18, 1\n\ts_and_b32 %20, %18, 3\n\ts_cselect_b32 %20, 16384, %23\n\t"
      "s_waitcnt lgkmcnt(2)\n\t" MF(0, 8, 6) RD(11, 16, 1024)
      "s_and_b32 %21, %18, 7\n\t" MF(1, 9, 6) RD(13, 17, 5120)
      "s_waitcnt lgkmcnt(2)\n\t" MF(2, 10, 6) RD(14, 17, 6144)
      "s_cselect_b32 %21, 10240, %24\n\t" MF(3, 8, 7) RD(15, 17, 7168)
      MF(4, 9, 7) RD(12, 16, 3072)
      MF(5, 10, 7) "v_add_u32 %16, %20, %16\n\t"
      "s_waitcnt lgkmcnt(2)\n\t" MF(0, 13, 11) "v_add_u32 %17, %21, %17\n\t"
      MF(1, 14, 11)
      "s_waitcnt lgkmcnt(0)\n\ts_barrier\n\t"
      MF(2, 15, 11) RD(6, 16, 0) RD(8, 17, 0)
      MF(3, 13, 12) RD(9, 17, 1024) RD(10, 17, 2048)
      MF(4, 14, 12) RD(7, 16, 2048) "s_sub_u32 %19, %19, 1\n\t"
      MF(5, 15, 12)
      "s_cmp_lg_u32 %19, 0\n\ts_cbranch_scc1 1b\n\t"
      "s_branch 3f\n"
      "2:\n\t"
      "s_add_u32 %18, %18, 1\n\ts_and_b32 %20, %18, 3\n\ts_cselect_b32 %20, 16384, %23\n\t"
      "s_waitcnt lgkmcnt(1)\n\t" MF(0, 8, 6) RD(11, 16, 1024) RD(13, 17, 5120)
      "s_and_b32 %21, %18, 7\n\t" MF(1, 9, 6) RD(14, 17, 6144)
      "s_cselect_b32 %21, 10240, %24\n\ts_waitcnt lgkmcnt(3)\n\t" MF(3, 8, 7) RD(12, 16, 3072)
      MF(4, 9, 7) "v_add_u32 %16, %20, %16\n\tv_add_u32 %17, %21, %17\n\t"
      "s_waitcnt lgkmcnt(1)\n\t" MF(0, 13, 11)
      MF(1, 14, 11)
      "s_waitcnt lgkmcnt(0)\n\ts_barrier\n\t"
      RD(6, 16, 0) RD(8, 17, 0)
      MF(3, 13, 12) RD(9, 17, 1024) RD(7, 16, 2048) "s_sub_u32 %19, %19, 1\n\t"
      MF(4, 14, 12)
      "s_cmp_lg_u32 %19, 0\n\ts_cbranch_scc1 2b\n"
      "3:\n\t"
      "s_waitcnt lgkmcnt(0)\n\ts_nop 7"
      : "+v"(o[0][0]), "+v"(o[0][1]), "+v"(o[0][2]), "+v"(o[1][0]), "+v"(o[1][1]), "+v"(o[1][2]),
        "+v"(p.A[0]), "+v"(p.A[1]), "+v"(p.B[0]), "+v"(p.B[1]), "+v"(p.B[2]),
        "=&v"(A0), "=&v"(A1), "=&v"(B0), "=&v"(B1), "=&v"(B2),
        "+v"(p.aa), "+v"(p.ba), "+s"(p.st), "+s"(n), "=&s"(da), "=&s"(db)
      : "s"(nw), "s"(-49152), "s"(-71680)
      : "memory", "scc");
}
DI void p2a_run(P2& p, f32x4 (&o)[3], unsigned& aa, int nw, u32x4 (&pt)[3], unsigned po, unsigned po2, const char* prd) {
  int n = 5;
  u32x4 A0, A1, A2, A3, B0, B1, B2;
  unsigned da, db;
#define ADVB "s_add_u32 %15, %15, 1\n\ts_and_b32 %18, %15, 7\n\ts_cselect_b32 %18, 10240, %25\n\tv_add_u32 %14, %18, %14\n\t"
#define ADVA "s_add_u32 %19, %19, 1\n\ts_and_b32 %17, %19, 3\n\ts_cselect_b32 %17, 16384, %24\n\tv_add_u32 %13, %17, %13\n\t"
#define PLD(l) "s_cmp_lg_u32 %16, 2\n\ts_cbranch_scc1 " #l "f\n\tglobal_load_dwordx4 %20, %26, %28 sc1\n\tglobal_load_dwordx4 %21, %26, %28 offset:1024 sc1\n\t" \
               "global_load_dwordx4 %22, %27, %28 sc1\n" #l ":\n\t"
  asm volatile(
      "s_cmp_lg_u32 %23, 0\n\ts_cbranch_scc1 2f\n"
      "1:\n\t"
      "s_barrier\n\t" RD(6, 13, 0) RD(10, 14, 5120) RD(11, 14, 6144) RD(12, 14, 7168) RD(7, 13, 1024) RD(8, 13, 2048) RD(9, 13, 3072)
      PLD(4) ADVB
      "s_waitcnt lgkmcnt(6)\n\t" MF(0, 3, 6) MF(1, 4, 6) MF(2, 5, 6)
      RD(3, 14, 0) RD(4, 14, 1024) RD(5, 14, 2048) ADVA
      "s_waitcnt lgkmcnt(5)\n\t" MF(0, 10, 7) MF(1, 11, 7) MF(2, 12, 7)
      RD(10, 14, 5120) RD(11, 14, 6144) RD(12, 14, 7168) ADVB
      "s_waitcnt lgkmcnt(3)\n\t" MF(0, 3, 8) MF(1, 4, 8) MF(2, 5, 8)
      RD(3, 14, 0) RD(4, 14, 1024) RD(5, 14, 2048) "s_sub_u32 %16, %16, 1\n\t"
      "s_waitcnt lgkmcnt(3)\n\t" MF(0, 10, 9) MF(1, 11, 9) MF(2, 12, 9)
      "s_cmp_lg_u32 %16, 0\n\ts_cbranch_scc1 1b\n\t"
      "s_branch 3f\n"
      "2:\n\t"
      "s_barrier\n\t" RD(6, 13, 0) RD(10, 14, 5120) RD(11, 14, 6144) RD(7, 13, 1024) RD(8, 13, 2048) RD(9, 13, 3072)
      PLD(5) ADVB
      "s_waitcnt lgkmcnt(5)\n\t" MF(0, 3, 6) MF(1, 4, 6)
      RD(3, 14, 0) RD(4, 14, 1024) ADVA
      "s_waitcnt lgkmcnt(4)\n\t" MF(0, 10, 7) MF(1, 11, 7)
      RD(10, 14, 5120) RD(11, 14, 6144) ADVB
      "s_waitcnt lgkmcnt(2)\n\t" MF(0, 3, 8) MF(1, 4, 8)
      RD(3, 14, 0) RD(4, 14, 1024) "s_sub_u32 %16, %16, 1\n\t"
      "s_waitcnt lgkmcnt(2)\n\t" MF(0, 10, 9) MF(1, 11, 9)
      "s_cmp_lg_u32 %16, 0\n\ts_cbranch_scc1 2b\n"
      "3:\n\t"
      "s_waitcnt vmcnt(0) lgkmcnt(0)\n\ts_nop 7"
      : "+v"(o[0]), "+v"(o[1]), "+v"(o[2]), "+v"(p.B[0]), "+v"(p.B[1]), "+v"(p.B[2]),
        "=&v"(A0), "=&v"(A1), "=&v"(A2), "=&v"(A3), "=&v"(B0), "=&v"(B1), "=&v"(B2),
        "+v"(aa), "+v"(p.ba), "+s"(p.st), "+s"(n), "=&s"(da), "=&s"(db), "+s"(p.sa),
        "=&v"(pt[0]), "=&v"(pt[1]), "=&v"(pt[2])
      : "s"(nw), "s"(-49152), "s"(-71680), "v"(po), "v"(po2), "s"(prd)
      : "memory", "scc");
#undef ADVA
#undef PLD
#undef ADVB
}
#undef MF
#undef RD

#define TRW 0
#define TRL 0
#define STAMP(i)

#define SL2(r, o) "s_load_dword s95, %" #r ", " #o "\n\ts_load_dword s95, %" #r ", " #o "+64\n\t"
DI void spf(int kind, int st, const char* x, const char* wqkv, const char* w1, int xcd, int idx) {
  const int xr = xcd & 1, cq = xcd >> 1, krow = st * 64 + xr * 32 + idx;
  const char* px = x + (xr * 1024 + cq * 256 + idx * 8) * 1280 + st * 128;
  const char* pq = wqkv + krow * 3840 + ((cq * 320) & ~127);
  const char* ph = w1 + krow * 3456 + cq * 896 - (cq == 3 ? 128 : 0);
  asm volatile("s_cmp_lt_u32 %3, 2\n\ts_cbranch_scc0 2f\n\ts_cmp_eq_u32 %3, 0\n\ts_cbranch_scc0 1f\n\t"
               SL2(0, 0) SL2(0, 1280) SL2(0, 2560) SL2(0, 3840) SL2(0, 5120) SL2(0, 6400) "s_branch 4f\n"
               "1:\n\t" SL2(0, 7680) SL2(0, 8960) SL2(2, 0) SL2(2, 128) SL2(2, 256) SL2(2, 384) "s_branch 4f\n"
               "2:\n\ts_cmp_eq_u32 %3, 2\n\ts_cbranch_scc0 3f\n\t"
               SL2(1, 0) SL2(1, 128) SL2(1, 256) SL2(1, 1280) SL2(1, 1408) SL2(1, 1536) "s_branch 4f\n"
               "3:\n\t" SL2(1, 2560) SL2(1, 2688) SL2(1, 2816) SL2(2, 512) SL2(2, 640) SL2(2, 768)
               "\n4:" ::"s"(px), "s"(pq), "s"(ph), "s"(kind) : "s95", "scc");
}
typedef __attribute__((ext_vector_type(16))) unsigned u32x16;
DI const char* mkptr(unsigned lo, unsigned hi) { return (const char*)(((u64)hi << 32) | lo); }

__global__ __launch_bounds__(768, 3) void fused_kernel(const char* a_x, const char* a_wqkv, const char* a_wphi, const char* a_wo,
                                                       const char* a_w1, const char* a_w2, const char* a_gate, char* a_out, char* a_ws) {
  extern __shared__ __attribute__((aligned(16))) char smem[];
  u32x16 ka;
  u64 kws, pc;
  asm volatile("s_load_dwordx16 %0, %3, 0x0\n\ts_load_dwordx2 %1, %3, 0x40\n\ts_getpc_b64 %2"
               : "=&s"(ka), "=&s"(kws), "=&s"(pc) : "s"(__builtin_amdgcn_kernarg_segment_ptr()));
  __builtin_amdgcn_sched_barrier(0);
#define ARGS()                                                                                                   \
  asm volatile("s_waitcnt lgkmcnt(0)" : "+s"(ka), "+s"(kws));                                                    \
  const char *x = mkptr(ka[0], ka[1]), *wqkv = mkptr(ka[2], ka[3]), *wphi = mkptr(ka[4], ka[5]), *wo = mkptr(ka[6], ka[7]), \
             *w1 = mkptr(ka[8], ka[9]), *w2 = mkptr(ka[10], ka[11]), *gate = mkptr(ka[12], ka[13]);              \
  char *out = (char*)mkptr(ka[14], ka[15]), *ws = (char*)kws;                                                    \
  (void)x; (void)wqkv; (void)wphi; (void)wo; (void)w1; (void)w2; (void)gate; (void)out;                          \
  u64 epoch;                                                                                                     \
  asm volatile("s_load_dwordx2 %0, %1, 0x0" : "=s"(epoch) : "s"(ws) : "memory")
#define TAG()                                                                                                    \
  asm volatile("s_waitcnt lgkmcnt(0)" : "+s"(epoch));                                                            \
  const u64 tag = epoch * 6364136223846793005ull + 1442695040888963407ull;                                       \
  const unsigned tlo = (unsigned)tag, thi = (unsigned)(tag >> 32);                                               \
  const u32x2 tagv = {tlo, thi}
  const int tid = threadIdx.x, lane = tid & 63;
  const int w = __builtin_amdgcn_readfirstlane(tid >> 6);
  const int b = blockIdx.x, xcd = b & 7, idx = b >> 3;
  const int rt = (xcd & 1) * 8 + (idx & 7);
  const int cb = idx >> 3, cq = xcd >> 1;
  const bool isq = cb < 2;
  const int jg = 2 * cq + (cb & 1);
  const int rt2 = ((xcd & 1) ^ 1) * 8 + ((cq & 1) ^ (isq ? 0 : 1)) * 4 + ((idx >> 2) & 3), ct = (cq >> 1) * 4 + (idx & 3);
  const int g = lane >> 4, c16 = lane & 15, q4 = c16 >> 2, p4 = lane & 3;
  const int hstart = jg * 448 - (jg == 7 ? 64 : 0);
  const unsigned boff = lane * 16;
#define ROTM 7
  const int rl = idx & 7, xr = xcd & 1;
  const int rot = ROTM == 0 ? 0
                : ROTM == 1 ? ((rl ^ cb) & 1) * 5
                : ROTM == 3 ? ((rl ^ cb) & 1) * 5 + xr * 2 + (cq & 1)
                : ROTM == 4 ? ((((rl + cb) % 3) * 10) / 3 + xr * 2) % 10
                : ROTM == 5 ? ((((rl + cb) & 3) * 5) >> 1) + xr
                : ROTM == 6 ? ((rl ^ cb) & 1) * 5 + xr * 3
                : ROTM == 8 ? (xr * 5 + cq) % 10
                : ((rl + cb) % 5) * 2 + xr;

  if (__builtin_expect(w >= 8, 1)) {
    const int L = w - 8;
    const int ldw = isq ? 3840 : 3456;
    unsigned xo[4], wv[8];
    {
      const unsigned x0 = (rt * 128 + 8 * L + (lane >> 3)) * 1280 + (((lane & 7) ^ ((4 * L + (lane >> 4)) & 7)) << 4);
#pragma unroll
      for (int i = 0; i < 4; ++i) xo[i] = x0 + i * 32 * 1280;
      const int rs = lane >= 30 ? 1 : 0, pos = lane - 30 * rs;
      const int sec = pos < 10 ? 0 : (pos < 15 ? 2560 - 160 : (pos < 25 ? 1280 - 240 : 2560 - 320));
      const int col = isq ? sec + jg * 160 + pos * 16 : hstart + pos * 16;
      const unsigned w0 = (8 * (L >> 1) + 2 * (L & 1) + rs) * ldw + col;
#pragma unroll
      for (int i = 0; i < 8; ++i) wv[i] = w0 + (32 * (i >> 2) + 16 * (i & 1) + 4 * ((i >> 1) & 1)) * ldw;
    }
    const u64 half = isq ? 0x3fffffffull : (jg < 6 ? 0x0fffffffull : 0x00ffffffull);
    const u64 wmask = half | (half << 30);
#pragma unroll
    for (int i = 0; i < 4; ++i) PIN(xo[i]);
#pragma unroll
    for (int i = 0; i < 8; ++i) PIN(wv[i]);
    ARGS();
    const char* wbase = isq ? wqkv : w1;
    const char* xs = x + rot * 128;
    const char* wsrc = wbase + rot * 64 * ldw;
    int kst = rot;
    const unsigned mx0 = L * 1024, mw0 = 16384 + L * 960;
    auto issue_lo = [&](unsigned sb) {
      asm volatile(
          "s_mov_b32 m0, %10\n\ts_nop 0\n\tglobal_load_lds_dwordx4 %0, %8\n\t"
          "s_add_u32 m0, m0, 4096\n\ts_nop 0\n\tglobal_load_lds_dwordx4 %1, %8\n\t"
          "s_add_u32 m0, m0, 4096\n\ts_nop 0\n\tglobal_load_lds_dwordx4 %2, %8\n\t"
          "s_add_u32 m0, m0, 4096\n\ts_nop 0\n\tglobal_load_lds_dwordx4 %3, %8\n\t"
          "s_mov_b32 m0, %11\n\ts_mov_b64 exec, %12\n\tglobal_load_lds_dwordx4 %4, %9 " WPOL "\n\t"
          "s_add_u32 m0, m0, 3840\n\ts_nop 0\n\tglobal_load_lds_dwordx4 %5, %9 " WPOL "\n\t"
          "s_add_u32 m0, m0, 3840\n\ts_nop 0\n\tglobal_load_lds_dwordx4 %6, %9 " WPOL "\n\t"
          "s_add_u32 m0, m0, 3840\n\ts_nop 0\n\tglobal_load_lds_dwordx4 %7, %9 " WPOL "\n\t"
          "s_mov_b64 exec, -1"
          ::"v"(xo[0]), "v"(xo[1]), "v"(xo[2]), "v"(xo[3]), "v"(wv[0]), "v"(wv[1]), "v"(wv[2]), "v"(wv[3]),
            "s"(xs), "s"(wsrc), "s"(sb + mx0), "s"(sb + mw0), "s"(wmask)
          : "memory", "scc");
    };
    auto issue_hi = [&](unsigned sb) {
      asm volatile(
          "s_mov_b32 m0, %5\n\ts_mov_b64 exec, %6\n\tglobal_load_lds_dwordx4 %0, %4 " WPOL "\n\t"
          "s_add_u32 m0, m0, 3840\n\ts_nop 0\n\tglobal_load_lds_dwordx4 %1, %4 " WPOL "\n\t"
          "s_add_u32 m0, m0, 3840\n\ts_nop 0\n\tglobal_load_lds_dwordx4 %2, %4 " WPOL "\n\t"
          "s_add_u32 m0, m0, 3840\n\ts_nop 0\n\tglobal_load_lds_dwordx4 %3, %4 " WPOL "\n\t"
          "s_mov_b64 exec, -1"
          ::"v"(wv[4]), "v"(wv[5]), "v"(wv[6]), "v"(wv[7]), "s"(wsrc), "s"(sb + mw0 + 15360), "s"(wmask)
          : "memory", "scc");
      const bool wrap = ++kst == 10;
      xs = wrap ? x : xs + 128;
      wsrc = wrap ? wbase : wsrc + 64 * ldw;
    };
    auto issue_stage = [&](unsigned sb) { issue_lo(sb); issue_hi(sb); };
#define LSTAMP()
#define LBAR() asm volatile("s_barrier" ::: "memory")
#define STEP(N, SL) WAITV(N); LSTAMP(); LBAR(); LSTAMP(); issue_stage(SL); LSTAMP();
    LSTAMP();
    __builtin_amdgcn_s_setprio(3);
    issue_stage(L_S0);
    LSTAMP();
    __builtin_amdgcn_s_setprio(1);
    int lane2 = lane, unit = b * 3 + L;
    asm volatile("" : "+v"(lane2), "+s"(unit));
    const bool retile = (L < 3) && (unit < 740);
    const int rk = retile ? unit / 10 : 0, rcu = retile ? unit - rk * 10 : 0;
    const char* rsrc = rk < 54 ? w2 : wo;
    unsigned rv[4], ra[4], rd[4];
    {
      const int kp = rk - 54, P = kp / 5, s5 = kp - P * 5;
      const bool lft = rk >= 54 && s5 == 4;
      const int gs = lft ? 40 : 4;
      const int r0 = rk < 54 ? 32 * rk : (lft ? 160 * P + 16 : 160 * P + 40 * s5);
      const int rh = rk < 54 ? 16 : (lft ? 2 : 20);
      const int c = lane2 & 7, e = (lane2 >> 3) & 3, g2 = lane2 >> 4, q2 = (lane2 >> 2) & 3;
      const unsigned v0 = (r0 + (lane2 >> 5) * gs + (lft ? 20 * (e & 1) + (e >> 1) : e)) * 1280 + rcu * 128 + ((((c >> 1) ^ g2) << 5) | ((c & 1) << 4));
      const unsigned base = L_MISC + L * 4096 + (4 * g2 + q2) * 128 + 8 * (lane2 & 3);
      const int sw = (2 * g2 + (q2 >> 1)) & 3;
#pragma unroll
      for (int n = 0; n < 4; ++n) {
        const int N = 4 * rcu + n, c5 = N / 5;
        rv[n] = v0 + ((n & 1) * 2 * gs + (n >> 1) * rh) * 1280;
        ra[n] = base + ((n ^ sw) << 5);
        rd[n] = WS_B + ((c5 * 74 + rk) * 5 + (N - c5 * 5)) * 1024 + lane2 * 16;
        PIN(rv[n]); PIN(ra[n]); PIN(rd[n]);
      }
    }
    TAG();
    int pst = SPF > 1 ? 5 : 3;
#define PF()
    PF();
    LSTAMP();
    issue_lo(L_S1);
    LSTAMP();
    WAITV(8); LSTAMP(); LBAR(); LSTAMP();
    __builtin_amdgcn_s_setprio(3);
    issue_hi(L_S1);
    PF();
    __builtin_amdgcn_s_setprio(1);
    asm volatile("s_mov_b32 m0, %5\n\ts_mov_b64 exec, %6\n\tglobal_load_lds_dwordx4 %0, %4 nt\n\t"
                 "s_add_u32 m0, m0, %7\n\ts_nop 0\n\tglobal_load_lds_dwordx4 %1, %4 nt\n\t"
                 "s_add_u32 m0, m0, %7\n\ts_nop 0\n\tglobal_load_lds_dwordx4 %2, %4 nt\n\t"
                 "s_add_u32 m0, m0, %7\n\ts_nop 0\n\tglobal_load_lds_dwordx4 %3, %4 nt\n\ts_mov_b64 exec, -1"
                 ::"v"(rv[0]), "v"(rv[1]), "v"(rv[2]), "v"(rv[3]), "s"(rsrc), "s"(retile ? L_MISC + L * 4096 : L_ONE + 16),
                   "s"(retile ? ~0ull : 1ull), "s"(retile ? 1024 : 0) : "memory", "scc");
    issue_lo(L_S2);
#define HALF(PREV, NEXT) LSTAMP(); LBAR(); LSTAMP(); __builtin_amdgcn_s_setprio(3); issue_hi(PREV); PF(); __builtin_amdgcn_s_setprio(1); issue_lo(NEXT); LSTAMP();
    WAITV(12);
    HALF(L_S2, L_S0)
    WAITV(8);
    {
      u32x4 f[4];
#pragma unroll
      for (int n = 0; n < 4; ++n) f[n] = cat(ldstr(ra[n]), ldstr(ra[n] + 2048));
      LSTAMP(); LBAR(); LSTAMP();
      __builtin_amdgcn_s_setprio(3);
      issue_hi(L_S0);
      if (retile) {
#pragma unroll
        for (int n = 0; n < 4; ++n) gst128_sc1(rd[n], ws, f[n]);
      }
      PF();
      __builtin_amdgcn_s_setprio(1);
      issue_lo(L_S1);
      LSTAMP();
      if (retile) { WAITV(12); } else { WAITV(8); }
      unsigned sp = L_S1, sb = L_S2;
#pragma unroll 1
      for (int t = 2; t < 7; ++t) {
        HALF(sp, sb)
        sp = sb;
        sb = sb == L_S0 ? L_S1 : sb + SLOT;
        if (t == 3 && L == 3 && lane == 0) gst64_sc1(b * 8, ws + WS_RT, tagv);
        WAITV(8);
      }
      LSTAMP(); LBAR(); LSTAMP();
      issue_hi(sp);
    }
    const int k0 = isq ? 54 - 2 * NQ2 : 0, nb = isq ? NQ2 : NH2;
    const int rot2 = P2ROT == 1 ? (((idx + (idx >> 2)) & 3) * nb) >> 2 : P2ROT == 2 ? (((idx + (idx >> 2)) & 1) * nb) >> 1 : 0;
    const char* bbase = ws + WS_B + ct * 74 * 5120;
    const unsigned boff1k = boff + 1024;
    unsigned bl = L_BR;
    int bs = 0, bk = k0 + 2 * rot2;
    auto issue_b = [&](int n) {
#pragma unroll 1
      for (int j = 0; j < n; ++j) {
        glds5_sc1(boff, boff1k, bbase + bk * 5120, bl);
        glds5_sc1(boff, boff1k, bbase + bk * 5120 + 5120, bl + 5120);
        bl = bl == L_BR + 71680 ? L_BR : bl + 10240;
        ++bs;
        bk += 2;
        if (bs < nb && bk == k0 + 2 * nb) bk = k0;
        if (bs == nb) bk = 54;
      }
    };
    __builtin_amdgcn_s_setprio(3);
    if (L == 1) {
      u32x4 r0, r1;
      const bool late = PREW && isq;
      asm volatile("global_load_dwordx4 %0, %2, %3 sc1\n\tglobal_load_dwordx4 %1, %2, %3 offset:16 sc1\n\ts_waitcnt vmcnt(2)\n\ts_barrier\n\t"
                   "s_cmp_ge_u32 %4, 2\n\ts_cbranch_scc1 1f\n\ts_barrier\n1:\n\ts_waitcnt vmcnt(0)"
                   : "=&v"(r0), "=&v"(r1) : "v"(lane * 32), "s"(ws + WS_RT), "s"(PREW ? cb : 2) : "memory", "scc");
      const unsigned bad = (r0[0] ^ tlo) | (r0[1] ^ thi) | (r0[2] ^ tlo) | (r0[3] ^ thi) | (r1[0] ^ tlo) | (r1[1] ^ thi) | (r1[2] ^ tlo) | (r1[3] ^ thi);
      if (__all(bad == 0)) {
        issue_b(3);
        if (!late) LBAR();
        issue_b(isq ? BQ2 : 5);
      } else {
        if (!late) LBAR();
#pragma unroll 1
        for (int i = 0; i < 4; ++i) poll_words(ws + WS_RT + i * 512, lane * 8, tlo, thi);
        issue_b(isq ? 3 : 8);
      }
      if (b == 0 && lane == 0) gst64_sc1(0, ws + WS_EPOCH, tagv);
    } else {
      WAITV(0);
      LBAR();
      if (!isq && L >= 2) return;
      LBAR();
    }
    LSTAMP();
    const char* abase = ws + WS_A + rt2 * 8 * 74 * 1024;
    unsigned al = L_AR;
    unsigned ao[8];
#pragma unroll
    for (int i = 0; i < 8; ++i) ao[i] = boff + (i ^ (isq ? 0 : 1)) * 74 * 1024;
    int ak = k0 + 2 * rot2;
    auto issue_a = [&]() {
        asm volatile(
            "s_mov_b32 m0, %9\n\ts_nop 0\n\tglobal_load_lds_dwordx4 %0, %8 sc1\n\tglobal_load_lds_dwordx4 %0, %8 offset:1024 sc1\n\t"
            "s_add_u32 m0, m0, 2048\n\ts_nop 0\n\tglobal_load_lds_dwordx4 %1, %8 sc1\n\tglobal_load_lds_dwordx4 %1, %8 offset:1024 sc1\n\t"
            "s_add_u32 m0, m0, 2048\n\ts_nop 0\n\tglobal_load_lds_dwordx4 %2, %8 sc1\n\tglobal_load_lds_dwordx4 %2, %8 offset:1024 sc1\n\t"
            "s_add_u32 m0, m0, 2048\n\ts_nop 0\n\tglobal_load_lds_dwordx4 %3, %8 sc1\n\tglobal_load_lds_dwordx4 %3, %8 offset:1024 sc1\n\t"
            "s_add_u32 m0, m0, 2048\n\ts_nop 0\n\tglobal_load_lds_dwordx4 %4, %8 sc1\n\tglobal_load_lds_dwordx4 %4, %8 offset:1024 sc1\n\t"
            "s_add_u32 m0, m0, 2048\n\ts_nop 0\n\tglobal_load_lds_dwordx4 %5, %8 sc1\n\tglobal_load_lds_dwordx4 %5, %8 offset:1024 sc1\n\t"
            "s_add_u32 m0, m0, 2048\n\ts_nop 0\n\tglobal_load_lds_dwordx4 %6, %8 sc1\n\tglobal_load_lds_dwordx4 %6, %8 offset:1024 sc1\n\t"
            "s_add_u32 m0, m0, 2048\n\ts_nop 0\n\tglobal_load_lds_dwordx4 %7, %8 sc1\n\tglobal_load_lds_dwordx4 %7, %8 offset:1024 sc1"
            ::"v"(ao[0]), "v"(ao[1]), "v"(ao[2]), "v"(ao[3]), "v"(ao[4]), "v"(ao[5]), "v"(ao[6]), "v"(ao[7]), "s"(abase + ak * 1024), "s"(al)
            : "memory", "scc");
        ak += 2;
        if (ak == k0 + 2 * nb) ak = k0;
        al = al == L_AR + 49152 ? L_AR : al + 16384;
    };
    const char* fl = ws + WS_HF + rt2 * 128;
    unsigned fo = (lane & 7) * 8;
    bool ok = false;
    if (isq) {
      unsigned ia[4];
#pragma unroll
      for (int i = 0; i < 4; ++i) {
        const int sl = L + 4 * i < 14 ? L + 4 * i : L;
        ia[i] = *(LAS unsigned*)(size_t)(L_TAB + (sl < 7 ? sl : sl - 7) * 256 + lane * 4) + (sl < 7 ? 0 : 3360);
        PIN(ia[i]);
      }
      unsigned my1 = *(LAS unsigned*)(size_t)(L_TAB + (rt < 7 ? rt : rt - 7) * 256 + (16 * L + c16) * 4) == L_ONE + 16 ? 0u : ~0u;
      unsigned h1o = g * 4 * GR_BYTES + c16 * 16;
      unsigned h2o = c16 * 16 + (g & 1) * 8, htag = (g & 1) ? thi : tlo;
      PIN(my1); PIN(h1o); PIN(h2o); PIN(htag);
      LBAR();
      LBAR();
      LSTAMP();
      u32x2 f = gld64_sc1(fo, fl);
      u32x4 pv[4];
      if (rt < 14) {
        const char* pb = ws + WS_PART + (jg ^ 2) * 16 * GR_BYTES + rt * 1024 + L * 256;
        __builtin_amdgcn_s_sleep(HOP1_SLEEP);
#pragma unroll 1
        for (unsigned spins = 0; spins < SPIN_CAP; ++spins) {
#pragma unroll
          for (int i = 0; i < 4; ++i) pv[i] = gld128_sc1(h1o, pb + i * GR_BYTES);
          asm volatile("s_waitcnt vmcnt(0)" : "+v"(pv[0]), "+v"(pv[1]), "+v"(pv[2]), "+v"(pv[3])::"memory");
          LSTAMP();
          unsigned bad = 0;
#pragma unroll
          for (int i = 0; i < 4; ++i) bad |= (pv[i][1] ^ tlo) | (pv[i][3] ^ thi);
          if (__all((bad & my1) == 0)) break;
          __builtin_amdgcn_s_sleep(RETRY_SLEEP);
        }
        float s[4];
#pragma unroll
        for (int e = 0; e < 4; ++e) {
          float t[4];
#pragma unroll
          for (int i = 0; i < 4; ++i) t[i] = __builtin_bit_cast(float, (e & 1) ? pv[i][e & 2] & 0xffff0000u : pv[i][e & 2] << 16);
          s[e] = (t[0] + t[1]) + (t[2] + t[3]);
        }
        const auto ra = __builtin_amdgcn_permlane32_swap(fbits(s[0]), fbits(s[1]), false, false);
        const auto rc = __builtin_amdgcn_permlane32_swap(fbits(s[2]), fbits(s[3]), false, false);
        const auto rd = __builtin_amdgcn_permlane16_swap(fbits(bitsf(ra[0]) + bitsf(ra[1])), fbits(bitsf(rc[0]) + bitsf(rc[1])), false, false);
        unsigned u0 = fbits(bitsf(rd[0]) + bitsf(rd[1])), u1 = u0;
        PIN(u1);
        const auto re = __builtin_amdgcn_permlane32_swap(u0, u1, false, false);
        if (lane < 32) gst64_sc1(h2o, ws + WS_RED + (jg ^ 2) * GR_BYTES + rt * 1024 + L * 256, (u32x2){pk2(bitsf(re[0]), bitsf(re[1])), htag});
      }
      LSTAMP();
      if (rt >= 14) WAITV(0);
      PIN(f);
      if (L == 0) {
        if (!__all(f[0] == tlo && f[1] == thi)) poll_words(fl, fo, tlo, thi);
#pragma unroll 1
        for (int i = 0; i < APRE; ++i) issue_a();
      } else {
        if (L == 1 && BQ3) issue_b(8 - bs);
        __builtin_amdgcn_s_sleep(HOP2_SLEEP);
      }
      const char* rb = ws + WS_RED + jg * GR_BYTES + L * 1024;
#pragma unroll 1
      for (unsigned spins = 0; spins < SPIN_CAP; ++spins) {
#pragma unroll
        for (int i = 0; i < 4; ++i) pv[i] = gld128_sc1(boff, rb + ((i < 3 || L < 2) ? i * 4096 : 0));
        asm volatile("s_waitcnt vmcnt(0)" : "+v"(pv[0]), "+v"(pv[1]), "+v"(pv[2]), "+v"(pv[3])::"memory");
        LSTAMP();
        unsigned bad = 0;
#pragma unroll
        for (int i = 0; i < 4; ++i) {
          asm volatile("ds_write2_b32 %0, %1, %2 offset0:0 offset1:1" ::"v"(ia[i]), "v"(pv[i][0]), "v"(pv[i][2]) : "memory");
          bad |= (pv[i][1] ^ tlo) | (pv[i][3] ^ thi);
        }
        if (__all(bad == 0)) break;
        __builtin_amdgcn_s_sleep(RETRY_SLEEP);
      }
      BAR();
      LSTAMP();
    } else {
      LBAR();
      if (L == 0 && lane == 0) gst64_sc1(rt * 128 + jg * 8, ws + WS_HF, tagv);
    }
    __builtin_amdgcn_s_setprio(0);
    if (L == 1) {
      WAITV(0);
      LBAR();
      issue_b(8 - bs);
#pragma unroll 1
      for (int s = 0; s < nb; ++s) {
        WAITV(40);
        LBAR();
        issue_b(1);
      }
      WAITV(40); LBAR();
      WAITV(30); LBAR();
      issue_b(1);
      WAITV(20); LBAR();
      issue_b(1);
      WAITV(10); LBAR();
      WAITV(0); LBAR();
    } else if (L == 0) {
      LSTAMP();
      if (!isq) poll_words(fl, fo, tlo, thi);
      LSTAMP();
#pragma unroll 1
      for (int s = isq ? APRE - 4 : -4; s < nb - 4; ++s) {
        if (s == (isq ? APRE + AIMG - 4 : -1)) { WAITV(32); LBAR(); LSTAMP(); }
        if (s >= 0) { WAITV(32); LBAR(); LSTAMP(); }
        issue_a();
      }
      fl = ws + WS_OF + rt2 * 512;
      fo = lane * 8;
      u32x2 f;
      asm volatile("s_waitcnt vmcnt(32)\n\ts_barrier\n\tglobal_load_dwordx2 %0, %1, %2 sc1\n\ts_waitcnt vmcnt(17)\n\ts_barrier\n\t"
                   "s_waitcnt vmcnt(1)\n\ts_barrier\n\ts_waitcnt vmcnt(0)"
                   : "=&v"(f) : "v"(fo), "s"(fl) : "memory");
      LSTAMP();
      ok = __all(f[0] == tlo && f[1] == thi);
      if (!ok) { LBAR(); poll_words(fl, fo, tlo, thi); }
      LSTAMP();
      const char* asrc = abase + 54 * 1024;
#pragma unroll 1
      for (int s = 0; s < 5; ++s) {
        if (s == 2 && ok) LBAR();
        if (s >= 3) { WAITV(32); LBAR(); LSTAMP(); }
        asm volatile(
            "s_mov_b32 m0, %5\n\ts_nop 0\n\tglobal_load_lds_dwordx4 %0, %4 sc1\n\tglobal_load_lds_dwordx4 %0, %4 offset:1024 sc1\n\t"
            "global_load_lds_dwordx4 %0, %4 offset:2048 sc1\n\tglobal_load_lds_dwordx4 %0, %4 offset:3072 sc1\n\t"
            "s_add_u32 m0, m0, 4096\n\ts_nop 0\n\tglobal_load_lds_dwordx4 %1, %4 sc1\n\tglobal_load_lds_dwordx4 %1, %4 offset:1024 sc1\n\t"
            "global_load_lds_dwordx4 %1, %4 offset:2048 sc1\n\tglobal_load_lds_dwordx4 %1, %4 offset:3072 sc1\n\t"
            "s_add_u32 m0, m0, 4096\n\ts_nop 0\n\tglobal_load_lds_dwordx4 %2, %4 sc1\n\tglobal_load_lds_dwordx4 %2, %4 offset:1024 sc1\n\t"
            "global_load_lds_dwordx4 %2, %4 offset:2048 sc1\n\tglobal_load_lds_dwordx4 %2, %4 offset:3072 sc1\n\t"
            "s_add_u32 m0, m0, 4096\n\ts_nop 0\n\tglobal_load_lds_dwordx4 %3, %4 sc1\n\tglobal_load_lds_dwordx4 %3, %4 offset:1024 sc1\n\t"
            "global_load_lds_dwordx4 %3, %4 offset:2048 sc1\n\tglobal_load_lds_dwordx4 %3, %4 offset:3072 sc1"
            ::"v"(ao[0]), "v"(ao[2]), "v"(ao[4]), "v"(ao[6]), "s"(asrc), "s"(al) : "memory", "scc");
        asrc += 4096;
        al = al == L_AR + 49152 ? L_AR : al + 16384;
      }
      WAITV(32); LBAR(); LSTAMP();
      WAITV(16); LBAR(); LSTAMP();
      WAITV(0); LBAR(); LSTAMP();
    }
    return;
  }

  asm volatile("s_waitcnt lgkmcnt(0)" ::"s"(ka), "s"(kws));
  const int mw = w & 3, nw = w >> 2;
  const char *wphi = a_wphi, *gate = a_gate;
  char *out = a_out, *ws = a_ws;
  u64 epoch;
  {
    const char *px = a_x, *pq = a_wqkv, *ph = a_w1;
    asm volatile("s_load_dwordx2 %0, %1, 0x0" : "=&s"(epoch) : "s"(ws), "s"(px), "s"(pq), "s"(ph), "s"(wphi), "s"(gate), "s"(out) : "memory");
  }
  unsigned warm = 0;
  if (w == 4) {
    const unsigned o = ((idx * 8 + (lane & 7)) * 128);
    asm volatile("global_load_dword %0, %1, %2" : "=v"(warm) : "v"(o < CODE_BYTES ? o : 0), "s"(pc & ~127ull) : "memory");
  }
  {
    if (w == 3) {
      __builtin_amdgcn_s_setprio(3);
      const char* src = isq ? wphi + jg * 3200 : gate;
      const unsigned lim = isq ? 3184 : 1264;
#pragma unroll
      for (int n = 0; n < 4; ++n) {
        const unsigned o = n * 1024 + boff;
        glds(o < lim ? o : lim, src, L_WPHI + n * 1024);
      }
      __builtin_amdgcn_s_setprio(0);
    }
  }
  if (tid < 128) sts128(L_ZERO + tid * 16, (u32x4){0u, 0u, 0u, 0u});
  if (tid < 448) {
    const int hd = tid >= 224 ? 1 : 0, j = tid - 224 * hd, r = j - 160;
    const int ft = r >= 40 ? 2 : (r >= 20 ? 1 : 0), r2 = r - 20 * ft, gq = (r2 * 13) >> 6;
    const int d = j < 160 ? (j & 15) : 16 + r2 - 5 * gq, quad = j < 160 ? (j >> 4) : 4 * ft + gq;
    *(LAS unsigned*)(size_t)(L_TAB + tid * 4) = j < 210 ? L_KV + hd * 1680 + d * 80 + quad * 8 : L_ONE + 16;
  }
  if (tid == 128) sts128(L_ONE, (u32x4){0x3f803f80u, 0x3f803f80u, 0x3f803f80u, 0x3f803f80u});
  TAG();

  const unsigned xa = L_S0 + (32 * mw + c16) * 128 + ((g ^ ((c16 >> 1) & 7)) << 4);
  const unsigned wrd = L_S0 + 16384 + (4 * g + q4) * 480 + (isq ? 40 * (p4 & 1) + 240 * (p4 >> 1) : 8 * p4);
  const bool gatew = !isq && jg == 7 && nw == 1;
  const bool wide = nw == 0 && jg < 6;
  f32x4 acc[8][2];
  unsigned pbase = (p4 == g) ? L_WPHI + (2 * nw + (g & 1)) * 800 + q4 * 40 : L_ZERO;
  unsigned phiw = L_PHIK + (32 * mw + 16 * (g >> 1) + c16) * 352 + 80 * (2 * nw + (g & 1));
  unsigned vw = L_V + (32 * mw + c16) * 160 + 40 * g + 24 * nw;
  unsigned ab = L_PHIK + (4 * g + q4) * 352 + 80 * mw + 8 * p4;
  const bool vreal = nw == 0 || p4 == 0;
  unsigned vb = vreal ? L_V + (4 * g + q4) * 160 + 40 * mw + 32 * nw + 8 * p4 : L_ONE;
  unsigned po = (mw * 224 + (nw == 0 ? lane : 160 + 5 * g + c16)) * 16;
  unsigned ab2 = ab + 64 - 8 * (p4 & 2), vs = vreal ? 2560 : 0;
  PIN(pbase); PIN(phiw); PIN(vw); PIN(ab); PIN(vb); PIN(po); PIN(ab2); PIN(vs);
  STAMP(1);
  WAITV(0);
  if (isq) {
    if (nw == 0) gemm1_loop<8, true, false>(acc, xa, wrd, 0, 0);
    else gemm1_loop<7, true, false>(acc, xa, wrd, 0, 0);
  } else {
    const unsigned wh = wrd + (nw == 0 ? 0 : (jg < 6 ? 256 : 192));
    if (wide) gemm1_loop<8, false, false>(acc, xa, wh, 0, 0);
    else if (gatew) gemm1_loop<7, false, true>(acc, xa, wh, L_GATE + g * 16 + rot * 128, 2 * rot);
    else gemm1_loop<6, false, false>(acc, xa, wh, 0, 0);
  }
  STAMP(2);
  PIN(warm);

  unsigned kq[10];
  if (!isq) {
    const int k0 = (hstart >> 6) + (nw == 0 ? 0 : (jg < 6 ? 4 : 3));
    char* abase = ws + WS_A + ((rt * 8 + 2 * mw) * 74 + k0) * 1024;
#pragma unroll
    for (int pr = 0; pr < 4; ++pr) {
      if (pr < 3 || wide) {
        const u32x4 f0 = silu8(acc[2 * pr][0], acc[2 * pr + 1][0]), f1 = silu8(acc[2 * pr][1], acc[2 * pr + 1][1]);
        gst128_sc1(boff, abase + pr * 1024, f0);
        gst128_sc1(boff, abase + (74 + pr) * 1024, f1);
      }
    }
    if (gatew && g == 0) {
#pragma unroll
      for (int mt = 0; mt < 2; ++mt) {
        const float gv = __builtin_amdgcn_rcpf(1.f + __builtin_amdgcn_exp2f(acc[6][mt][0] * -1.4426950408889634f));
        gst32_sc1((rt * 128 + 32 * mw + 16 * mt + c16) * 4, ws + WS_G, __builtin_bit_cast(unsigned, gv));
      }
    }
    STAMP(3);
    WAITV(0);
    BAR();
    STAMP(4);
#pragma unroll
    for (int i = 0; i < 10; ++i) kq[i] = 0;
  } else {
    WPhi wf;
    wphi_read(wf, pbase);
    SB();
    unsigned kk[10];
#pragma unroll
    for (int i = 0; i < 10; ++i) {
      const f32x4 a = acc[i >> 1][0], c = acc[i >> 1][1];
      kq[i] = pk2(a[2 * (i & 1)], a[2 * (i & 1) + 1]); kk[i] = pk2(c[2 * (i & 1)], c[2 * (i & 1) + 1]);
    }
    SB();
#pragma unroll
    for (int i = 0; i < 10; ++i) {
      const auto r = __builtin_amdgcn_permlane32_swap(kq[i], kk[i], false, false);
      kq[i] = r[0]; kk[i] = r[1];
    }
    SB();
    f32x4 pp[5];
    phi_proj(wf, kk, pp);
    SB();
#pragma unroll
    for (int i = 5; i < 8; ++i) {
      if (i < 7 || nw == 0) {
#pragma unroll
        for (int mt = 0; mt < 2; ++mt) {
          const f32x4 a = acc[i][mt];
          sts64(vw + mt * 2560 + 8 * (i - 5), (u32x2){pk2(a[0], a[1]), pk2(a[2], a[3])});
        }
      }
    }
    SB();
    unsigned phi[20];
    PROJ_DONE(pp);
    dual_softmax<true>(pp, phi, phiw);
    unsigned va[8];
    va[0] = vb;
#pragma unroll
    for (int i = 1; i < 8; ++i) { va[i] = va[i - 1] + vs; PIN(va[i]); }
    BAR();
    STAMP(3);
    u32x4 vf[4], pf[4][3];
#pragma unroll
    for (int ks = 0; ks < 4; ++ks) {
      const unsigned o = ks * 32 * 352;
      vf[ks] = cat(ldstr(va[2 * ks]), ldstr(va[2 * ks + 1]));
      pf[ks][0] = cat(ldstr(ab + o), ldstr(ab + o + 16 * 352));
      pf[ks][1] = cat(ldstr(ab + o + 32), ldstr(ab + o + 32 + 16 * 352));
      pf[ks][2] = cat(ldstr(ab2 + o), ldstr(ab2 + o + 16 * 352));
      SB();
    }
    f32x4 kv[3];
#pragma unroll
    for (int i = 0; i < 3; ++i) kv[i] = (f32x4){0.f, 0.f, 0.f, 0.f};
#pragma unroll
    for (int ks = 0; ks < 4; ++ks)
#pragma unroll
      for (int i = 0; i < 3; ++i) kv[i] = mfma(pf[ks][i], vf[ks], kv[i]);
    char* pb = ws + WS_PART + (jg * 16 + rt) * GR_BYTES;
    const unsigned fs = nw == 0 ? 1024 : 320;
    if (nw == 0 || c16 < 5) {
      gst128_sc1(po, pb, (u32x4){pk2(kv[0][0], kv[0][1]), tlo, pk2(kv[0][2], kv[0][3]), thi});
      gst128_sc1(po, pb + fs, (u32x4){pk2(kv[1][0], kv[1][1]), tlo, pk2(kv[1][2], kv[1][3]), thi});
      if (g < 2) gst128_sc1(po, pb + 2 * fs, (u32x4){pk2(kv[2][0], kv[2][1]), tlo, pk2(kv[2][2], kv[2][3]), thi});
    }
  }

  {
    P2 p;
    p.aa = L_AR + mw * 4096 + boff;
    p.ba = L_BR + nw * 3072 + boff;
    p.st = 0;
    f32x4 ob[2][3], oa[3];
#pragma unroll
    for (int i = 0; i < 6; ++i) ob[i / 3][i % 3] = (f32x4){0.f, 0.f, 0.f, 0.f};
    if (isq) {
      BAR();
      STAMP(5);
      unsigned phq[20];
      {
        WPhi wf;
        wphi_read(wf, pbase);
        SB();
        f32x4 pp[5];
        phi_proj(wf, kq, pp);
        PROJ_DONE(pp);
        dual_softmax<false>(pp, phq, 0);
      }
#pragma unroll
      for (int i = 0; i < 10; ++i) {
        const int a = i < 8 ? i : 8 + i, c = i < 8 ? i + 8 : 10 + i;
        const auto r = __builtin_amdgcn_permlane32_swap(phq[a], phq[c], false, false);
        phq[a] = r[0]; phq[c] = r[1];
      }
#pragma unroll
      for (int i = 0; i < 10; ++i) {
        const int a = i < 8 ? (i & 3) + 8 * (i >> 2) : 2 * i, c = i < 8 ? a + 4 : a + 1;
        const auto r = __builtin_amdgcn_permlane16_swap(phq[a], phq[c], false, false);
        phq[a] = r[0]; phq[c] = r[1];
      }
      unsigned k0a = L_KV + 2 * nw * 1680 + c16 * 80, k1a = L_KV + 2 * nw * 1680 + (p4 == 0 ? 16 + q4 : 20) * 80;
      unsigned k0b = k0a + 64 + 4 * g, k1b = k1a + 64 + 4 * g;
      k0a += 16 * g; k1a += 16 * g;
      unsigned ow = (g & 1) * 74 * 1024 + ((2 * (jg & 1) + nw) * 16 + c16) * 16 + (g >> 1) * 8;
      PIN(k0a); PIN(k1a); PIN(k0b); PIN(k1b); PIN(ow);
      u32x4 qf[4];
      u32x2 qt[4], a16[2][2];
#pragma unroll
      for (int u = 0; u < 4; ++u) {
        qf[u] = (u32x4){phq[4 * u], phq[4 * u + 1], phq[4 * u + 2], phq[4 * u + 3]};
        qt[u] = (u32x2){phq[16 + u], 0u};
        a16[u >> 1][u & 1] = (u32x2){0u, 0u};
        PIN(qf[u]); PIN(qt[u]); PIN(a16[u >> 1][u & 1]);
      }
      STAMP(6);
      BAR();
      STAMP(7);
      {
        u32x4 a32[2][2];
#pragma unroll
        for (int h = 0; h < 2; ++h) { a32[h][1] = lds128(k1a + h * 1680); a16[h][1][0] = *(LAS unsigned*)(size_t)(k1b + h * 1680); }
#pragma unroll
        for (int h = 0; h < 2; ++h) { a32[h][0] = lds128(k0a + h * 1680); a16[h][0][0] = *(LAS unsigned*)(size_t)(k0b + h * 1680); }
        SB();
        f32x4 o[4][2];
#pragma unroll
        for (int t = 1; t >= 0; --t)
          asm volatile("s_nop 1\n\tv_mfma_f32_16x16x32_bf16 %0, %4, %8, 0\n\tv_mfma_f32_16x16x32_bf16 %1, %5, %9, 0\n\t"
                       "v_mfma_f32_16x16x32_bf16 %2, %4, %10, 0\n\tv_mfma_f32_16x16x32_bf16 %3, %5, %11, 0\n\ts_nop 2\n\t"
                       "v_mfma_f32_16x16x16_bf16 %0, %6, %12, %0\n\tv_mfma_f32_16x16x16_bf16 %1, %7, %13, %1\n\t"
                       "v_mfma_f32_16x16x16_bf16 %2, %6, %14, %2\n\tv_mfma_f32_16x16x16_bf16 %3, %7, %15, %3"
                       : "=&v"(o[0][t]), "=&v"(o[1][t]), "=&v"(o[2][t]), "=&v"(o[3][t])
                       : "v"(a32[0][t]), "v"(a32[1][t]), "v"(a16[0][t]), "v"(a16[1][t]), "v"(qf[0]), "v"(qf[1]), "v"(qf[2]), "v"(qf[3]),
                         "v"(qt[0]), "v"(qt[1]), "v"(qt[2]), "v"(qt[3]));
        asm volatile("" : "+v"(o[0][1]), "+v"(o[1][1]), "+v"(o[2][1]), "+v"(o[3][1]));
        char* abase = ws + WS_A + ((rt * 8 + 2 * mw) * 74 + 54 + (jg >> 1) * 5) * 1024;
        float sc[4];
#pragma unroll
        for (int u = 0; u < 4; ++u) sc[u] = __builtin_amdgcn_rcpf(o[u][1][1] + 1e-6f);
        unsigned l0 = pk2(o[0][1][0] * sc[0], o[1][1][0] * sc[1]), l1 = pk2(o[2][1][0] * sc[2], o[3][1][0] * sc[3]);
        asm volatile("s_nop 7" : "+v"(o[0][0]), "+v"(o[1][0]), "+v"(o[2][0]), "+v"(o[3][0]), "+v"(l0), "+v"(l1));
#pragma unroll
        for (int mt = 0; mt < 2; ++mt) {
          const f32x4 a = o[2 * mt][0] * sc[2 * mt], c = o[2 * mt + 1][0] * sc[2 * mt + 1];
          gst128_sc1(boff, abase + (mt * 74 + 2 * (jg & 1) + nw) * 1024, (u32x4){pk2(a[0], a[1]), pk2(a[2], a[3]), pk2(c[0], c[1]), pk2(c[2], c[3])});
        }
        const auto r = __builtin_amdgcn_permlane16_swap(l0, l1, false, false);
        gst64_sc1(ow, abase + 4 * 1024, (u32x2){r[0], r[1]});
      }
      STAMP(4);
    }
#pragma unroll
    for (int i = 0; i < 6; ++i) PIN(ob[i / 3][i % 3]);
    char* pbuf = ws + WS_P + ((rt2 * 8 + ct) * 16 + w) * 3072;
    const unsigned row = rt2 * 128 + 32 * mw + (isq ? 0 : 16) + c16;
    const char* prd = pbuf + (isq ? 8 * 3072 : 0);
    unsigned p2o = boff + (nw == 0 ? 2048 : 0), go = row * 4;
    unsigned oo = row * 1280 + ct * 160 + nw * 96 + (g & 1) * 32 + (g >> 1) * 16, oo2 = row * 1280 + ct * 160 + 64 + g * 8;
    u32x4 pt[3];
    PIN(p.aa); PIN(p.ba); PIN(p2o); PIN(go); PIN(oo); PIN(oo2);
    __builtin_amdgcn_s_setprio(2);
    BAR();
    if (!isq) STAMP(5);
    p2_first(p);
#pragma unroll 1
    for (int part = 0; part < 2; ++part) {
      p2_run(p, ob, part == 0 ? OFL : (isq ? NQ2 : NH2) - OFL, nw);
      if (part == 0 && isq) {
        WAITV(0);
        if (lane == 0) gst64_sc1(rt * 512 + (jg * 8 + w) * 8, ws + WS_OF, tagv);
        STAMP(8);
      }
    }
    unsigned gb = gld32_sc1(go, ws + WS_G);
#pragma unroll
    for (int n = 0; n < 3; ++n) {
      oa[n] = (f32x4){0.f, 0.f, 0.f, 0.f};
      if (n < 2 || nw == 0)
        gst128_sc1(boff, pbuf + (isq ? 0 : 8 * 3072) + n * 1024, (u32x4){pk2(ob[1][n][0], ob[1][n][1]), tlo, pk2(ob[1][n][2], ob[1][n][3]), thi});
    }
    STAMP(9);
    p.sa = p.st;
    p2a_run(p, oa, p.aa, nw, pt, boff, p2o, prd);
    PIN(gb);
    STAMP(10);
#pragma unroll 1
    for (unsigned spins = 0; spins < SPIN_CAP; ++spins) {
      const unsigned bad = (pt[0][1] ^ tlo) | (pt[0][3] ^ thi) | (pt[1][1] ^ tlo) | (pt[1][3] ^ thi) | (pt[2][1] ^ tlo) | (pt[2][3] ^ thi);
      if (__all(bad == 0)) break;
      __builtin_amdgcn_s_sleep(8);
      asm volatile("global_load_dwordx4 %0, %3, %5 sc1\n\tglobal_load_dwordx4 %1, %3, %5 offset:1024 sc1\n\tglobal_load_dwordx4 %2, %4, %5 sc1\n\ts_waitcnt vmcnt(0)"
                   : "=&v"(pt[0]), "=&v"(pt[1]), "=&v"(pt[2]) : "v"(boff), "v"(p2o), "s"(prd) : "memory");
    }
    const float gv = bitsf(gb), omg = 1.f - gv;
    unsigned r[3][2];
#pragma unroll
    for (int n = 0; n < 3; ++n) {
      const f32x4 q = {bitsf(pt[n][0] << 16), bitsf(pt[n][0] & 0xffff0000u), bitsf(pt[n][2] << 16), bitsf(pt[n][2] & 0xffff0000u)};
      const f32x4 t = (ob[0][n] + q) * omg + oa[n] * gv;
      r[n][0] = pk2(t[0], t[1]); r[n][1] = pk2(t[2], t[3]);
    }
    const auto s0 = __builtin_amdgcn_permlane16_swap(r[0][0], r[1][0], false, false);
    const auto s1 = __builtin_amdgcn_permlane16_swap(r[0][1], r[1][1], false, false);
    asm volatile("global_store_dwordx4 %0, %1, %2\n\ts_nop 1" ::"v"(oo), "v"((u32x4){s0[0], s1[0], s0[1], s1[1]}), "s"(out) : "memory");
    if (nw == 0) gst64(oo2, out, (u32x2){r[2][0], r[2][1]});
  }
}

extern "C" void kernel_launch(void* const* d_in, const int* in_sizes, int n_in, void* d_out, int out_size, void* d_ws,
                              size_t ws_size, hipStream_t stream) {
  (void)in_sizes; (void)n_in; (void)out_size; (void)ws_size;
  (void)hipFuncSetAttribute((const void*)fused_kernel, hipFuncAttributeMaxDynamicSharedMemorySize, LDS_BYTES);
  fused_kernel<<<256, 768, LDS_BYTES, stream>>>((const char*)d_in[0], (const char*)d_in[1], (const char*)d_in[2],
                                                (const char*)d_in[3], (const char*)d_in[5], (const char*)d_in[6],
                                                (const char*)d_in[7], (char*)d_out, (char*)d_ws);
}
```
